```python
import torch

def _softmax(x, axis=-1):
    return torch.softmax(x, dim=axis)

def _gelu(x):
    return torch.nn.functional.gelu(x, approximate='tanh')

def _silu(x):
    return torch.nn.functional.silu(x)

def _sigmoid(x):
    return torch.sigmoid(x)

def _layernorm(x, axis=-1, eps=1e-05):
    m = x.mean(dim=axis, keepdim=True)
    v = x.var(dim=axis, keepdim=True, unbiased=False)
    return (x - m) / torch.sqrt(v + eps)

def _scatter_add(tgt, idx, src, axis=0):
    return tgt.clone().index_add_(axis, idx, src)

def _segment_sum(src, seg, n):
    out = torch.zeros((n,) + src.shape[1:], dtype=src.dtype)
    return out.index_add_(0, seg, src)

def _pad(x, pw):
    flat = tuple((v for pair in reversed(pw) for v in pair))
    return torch.nn.functional.pad(x, flat)
kernel_name = 'recurrent_scan__memory_aug_recurrent_mem_tr__glu_gated_bilinear__prompt'

def setup_inputs():

    _w = lambda a: (lambda t: t.to(torch.bfloat16) if t.is_floating_point() else t)(torch.as_tensor(a))

    def _A_setup():
        B, S, Dm = (1, 2048, 512)
        H, Dh = (32, 128)
        M, seg = (8, 32)
        seg = min(seg, S)
        S = S // seg * seg
        return {'x': _w((lambda *s: torch.randn(*s, dtype=torch.bfloat16) * 0.1)(B, S, Dm)), 'mem0': _w((lambda *s: torch.randn(*s, dtype=torch.bfloat16) * 1.6)(B, M, Dm)), 'Wq': _w((lambda *s: torch.randn(*s, dtype=torch.bfloat16) * 0.8)(Dm, H, Dh) * Dm ** (-0.5)), 'Wk': _w((lambda *s: torch.randn(*s, dtype=torch.bfloat16) * 0.8)(Dm, H, Dh) * Dm ** (-0.5)), 'Wv': _w((lambda *s: torch.randn(*s, dtype=torch.bfloat16) * 1.6)(Dm, H, Dh) * Dm ** (-0.5)), 'Wo': _w((lambda *s: torch.randn(*s, dtype=torch.bfloat16) * 1.6)(H, Dh, Dm) * (H * Dh) ** (-0.5)), 'seg_len': seg}
    _A_in = _A_setup()

    def _B_setup():
        B, S, D, F = (1, 2048, 512, 1376)
        return {'x': _w((lambda *s: torch.randn(*s, dtype=torch.bfloat16) * 0.1)(B, S, D)), 'w_gate': _w((lambda *s: torch.randn(*s, dtype=torch.bfloat16) * 0.4)(D, F) * D ** (-0.5)), 'w_up': _w((lambda *s: torch.randn(*s, dtype=torch.bfloat16) * 0.4)(D, F) * D ** (-0.5)), 'w_down': _w((lambda *s: torch.randn(*s, dtype=torch.bfloat16) * 0.4)(F, D) * F ** (-0.5))}
    _B_in = _B_setup()
    return {'A_x': _A_in['x'], 'A_mem0': _A_in['mem0'], 'A_Wq': _A_in['Wq'], 'A_Wk': _A_in['Wk'], 'A_Wv': _A_in['Wv'], 'A_Wo': _A_in['Wo'], 'A_seg_len': _A_in['seg_len'], 'B_x': _B_in['x'], 'B_w_gate': _B_in['w_gate'], 'B_w_up': _B_in['w_up'], 'B_w_down': _B_in['w_down']}

def reference(A_x, A_mem0, A_Wq, A_Wk, A_Wv, A_Wo, A_seg_len, B_x, B_w_gate, B_w_up, B_w_down):

    def _A_ref(x, mem0, Wq, Wk, Wv, Wo, seg_len):
        B, S, Dm = x.shape
        H, Dh = (Wq.shape[1], Wq.shape[2])
        M = mem0.shape[1]
        scale = Dh ** (-0.5)
        n_seg = S // seg_len
        L = M + seg_len
        idx = torch.arange(L)
        mask = (idx[:, None] < M) | (idx[:, None] >= idx[None, :])
        mem = mem0
        outs = []
        for s in range(n_seg):
            xs = x[:, s * seg_len:(s + 1) * seg_len, :].clone()
            h = torch.concatenate([mem, xs], axis=1)
            q = torch.einsum('btd,dhe->bhte', h, Wq)
            k = torch.einsum('btd,dhe->bhte', h, Wk)
            v = torch.einsum('btd,dhe->bhte', h, Wv)
            scores = torch.einsum('bhqe,bhke->bhqk', q, k) * scale
            scores = torch.where(mask[None, None], scores, -1000000000.0)
            p = _softmax(scores, axis=-1)
            o = torch.einsum('bhqk,bhke->bhqe', p, v)
            o = torch.einsum('bhte,hed->btd', o, Wo)
            mem = o[:, :M, :].clone()
            outs.append(o[:, M:, :])
        return torch.concatenate(outs, axis=1)

    def _B_ref(x, w_gate, w_up, w_down):
        g = torch.einsum('bsd,df->bsf', x, w_gate)
        u = torch.einsum('bsd,df->bsf', x, w_up)
        h = g * u
        return torch.einsum('bsf,fd->bsd', h, w_down)
    x = A_x
    xn = _layernorm(x, axis=-1)
    s = _A_ref(xn, A_mem0, A_Wq, A_Wk, A_Wv, A_Wo, A_seg_len)
    g = _silu(_B_ref(xn, B_w_gate, B_w_up, B_w_down))
    return x + s * g
```

```cpp
#include <hip/hip_runtime.h>
#include <hip/hip_bf16.h>

typedef unsigned u32;
typedef u32 u32x2 __attribute__((ext_vector_type(2)));
typedef u32 u32x4 __attribute__((ext_vector_type(4)));
typedef u32 u32x8 __attribute__((ext_vector_type(8)));
typedef u32 u32x16 __attribute__((ext_vector_type(16)));
typedef u32 u32x32 __attribute__((ext_vector_type(32)));
typedef float f32x4 __attribute__((ext_vector_type(4)));
typedef float f32x8 __attribute__((ext_vector_type(8)));
typedef float f32x16 __attribute__((ext_vector_type(16)));
typedef short s16x4 __attribute__((ext_vector_type(4)));
typedef __bf16 bf16x8 __attribute__((ext_vector_type(8)));
#define LAS __attribute__((address_space(3)))
#define DEV __device__ __forceinline__

constexpr u32 C_EPOCH = 0, C_TICKET = 128, C_LNF = 2 * 128, C_APROG = 66 * 128, C_CPROG = 194 * 128, C_VOTE = 226 * 128, C_READY = 290 * 128, C_CLAIM = 354 * 128, C_ARRIVE = C_CLAIM + 256 * 32;
constexpr u32 C_OFLAG = 6u << 20;
constexpr u32 OFF_XN = 8u << 20, OFF_Q = 16u << 20, OFF_KV = 32u << 20, OFF_H = 64u << 20, OFF_KM = 72u << 20, OFF_O = 80u << 20, OFF_DBG = 96u << 20;
constexpr u32 WS_USED = 100u << 20, OOB = 0x80000000u;
constexpr u32 X_IN = 0, X_H2 = 3 * 64 * 4096, X_PR = X_H2 + 16 * 8192;
constexpr u32 DBGS = 64;
#define NOREP 0
#define FARSL 50
#define ATT_SL 2
#define OUT_SL 2
#define OUT_SL0 8
constexpr u32 CODE_BYTES = 76 * 1024;
constexpr u32 SPIN = 400000;
constexpr float SC2 = 0.08838834764831845f * 1.4426950408889634f;
constexpr int LDS_BYTES = 160 * 1024;

DEV u32 rfl(u32 x) { return __builtin_amdgcn_readfirstlane(x); }
DEV u32x4 mksrd(const void* p, u32 bytes) { unsigned long long a = (unsigned long long)p; return u32x4{rfl((u32)a), rfl((u32)(a >> 32)) & 0xffffu, bytes, 0x00020000u}; }
DEV __amdgpu_buffer_rsrc_t mkrs(void* p, u32 bytes) { return __builtin_amdgcn_make_buffer_rsrc(p, 0, bytes, 0x00020000); }
DEV u32x4 ld16(__amdgpu_buffer_rsrc_t r, u32 vo, u32 so) { return __builtin_amdgcn_raw_buffer_load_b128(r, vo, so, 16); }
DEV u32x2 ld8(__amdgpu_buffer_rsrc_t r, u32 vo, u32 so) { return __builtin_amdgcn_raw_buffer_load_b64(r, vo, so, 16); }
DEV u32 ld4(__amdgpu_buffer_rsrc_t r, u32 vo) { return __builtin_amdgcn_raw_buffer_load_b32(r, vo, 0, 16); }
DEV void st16(__amdgpu_buffer_rsrc_t r, u32x4 v, u32 vo, u32 so) { __builtin_amdgcn_raw_buffer_store_b128(v, r, vo + so, 0, 16); }
DEV void st8(__amdgpu_buffer_rsrc_t r, u32 v, u32 tag, u32 vo) { __builtin_amdgcn_raw_buffer_store_b64(u32x2{v, tag}, r, vo, 0, 16); }
DEV void st4(__amdgpu_buffer_rsrc_t r, u32 v, u32 vo) { __builtin_amdgcn_raw_buffer_store_b32(v, r, vo, 0, 16); }
typedef __bf16 bf16x2 __attribute__((ext_vector_type(2)));
typedef float f32x2 __attribute__((ext_vector_type(2)));
DEV u32 pk(float a, float b) { const f32x2 f = {a, b}; return __builtin_bit_cast(u32, __builtin_convertvector(f, bf16x2)); }
DEV float blo(u32 x) { return __builtin_bit_cast(float, x << 16); }
DEV float bhi(u32 x) { return __builtin_bit_cast(float, x & 0xffff0000u); }
DEV float rbf(float x) { return blo(pk(x, x)); }
#define END() __builtin_amdgcn_endpgm()
DEV int lane_id() { int l; asm volatile("v_mbcnt_lo_u32_b32 %0, -1, 0\n\tv_mbcnt_hi_u32_b32 %0, -1, %0" : "=v"(l)); return l; }
#define TS(i)
#define TW(i)
#define TO(i)
#define TU(i)
DEV void cbar() { asm volatile("" ::: "memory"); }
DEV void bar_lds() { asm volatile("s_waitcnt lgkmcnt(0)\n\ts_barrier" ::: "memory"); }
template <int N> DEV void bar_vm() { asm volatile("s_waitcnt vmcnt(%0) lgkmcnt(0)\n\ts_barrier" :: "n"(N) : "memory"); }
DEV u32x2 trd(u32 a) { return __builtin_bit_cast(u32x2, __builtin_amdgcn_ds_read_tr16_b64_v4i16((LAS s16x4*)(size_t)a)); }
DEV f32x4 mf32(u32x4 a, u32x4 b, f32x4 c) { return __builtin_amdgcn_mfma_f32_16x16x32_bf16(__builtin_bit_cast(bf16x8, a), __builtin_bit_cast(bf16x8, b), c, 0, 0, 0); }
DEV f32x4 mf16(u32x2 a, u32x2 b, f32x4 c) { return __builtin_amdgcn_mfma_f32_16x16x16bf16_1k(__builtin_bit_cast(s16x4, a), __builtin_bit_cast(s16x4, b), c, 0, 0, 0); }
#define MF0(d, a, b) asm volatile("v_mfma_f32_16x16x32_bf16 %0, %1, %2, 0" : "=&v"(d) : "v"(a), "v"(b))
#define MFA(d, a, b) asm volatile("v_mfma_f32_16x16x32_bf16 %0, %1, %2, %0" : "+v"(d) : "v"(a), "v"(b))
#define MH1(d, a, b) asm volatile("s_nop 1\n\tv_mfma_f32_16x16x16_bf16 %0, %1, %2, %0" : "+v"(d) : "v"(a), "v"(b))
DEV float x16(float v, bool mx) { u32 u = __builtin_bit_cast(u32, v); auto r = __builtin_amdgcn_permlane16_swap(u, u, false, false); u32 p = r[0], q = r[1];
  float a = __builtin_bit_cast(float, p), b = __builtin_bit_cast(float, q); return mx ? fmaxf(a, b) : a + b; }
DEV float x32(float v, bool mx) { u32 u = __builtin_bit_cast(u32, v); auto r = __builtin_amdgcn_permlane32_swap(u, u, false, false); u32 p = r[0], q = r[1];
  float a = __builtin_bit_cast(float, p), b = __builtin_bit_cast(float, q); return mx ? fmaxf(a, b) : a + b; }
DEV bool wait_ge(__amdgpu_buffer_rsrc_t r, u32 vo, bool act, u32 need, u32 tag, u32& budget, int slp) {
  for (;;) {
    cbar();
    const u32x2 x = ld8(r, vo, 0); const u32 v = x.y == tag ? x.x : 0u;
    if (__all(!act || v >= need)) { cbar(); return true; }
    if (budget == 0) return false;
    --budget;
    if (slp == 1) __builtin_amdgcn_s_sleep(2); else if (slp == 2) __builtin_amdgcn_s_sleep(8);
    else if (rfl(v) + 2 < need) __builtin_amdgcn_s_sleep(FARSL); else __builtin_amdgcn_s_sleep(ATT_SL);
  }
}
DEV bool wait_ln(__amdgpu_buffer_rsrc_t r, u32 seg, u32 tag, u32& budget) {
  for (;;) {
    cbar();
    const u32x4 x = ld16(r, C_LNF + seg * 128, 0);
    if (__all(x.y == tag && x.w == tag)) { cbar(); return true; }
    if (budget == 0) return false;
    --budget; __builtin_amdgcn_s_sleep(2);
  }
}
DEV u32 swz(u32 row) { return ((row & 1) | (((row >> 1) ^ (row >> 2)) & 1) << 1 | ((row >> 3) & 1) << 2) << 1; }
DEV void stage(u32x4 srd, const u32 (&vo)[4], u32 pitch, u32 row0, u32 lds, int w) {
  u32 keep, t;
  asm volatile("s_mov_b32 %0, m0\n\ts_mov_b32 m0, %8\n\ts_mov_b32 %1, %7\n\ts_nop 1\n\tbuffer_load_dwordx4 %2, %6, %1 offen lds\n\ts_add_u32 m0, m0, 1024\n\ts_nop 0\n\tbuffer_load_dwordx4 %3, %6, %1 offen lds\n\ts_add_u32 m0, m0, 1024\n\ts_nop 0\n\tbuffer_load_dwordx4 %4, %6, %1 offen lds\n\ts_add_u32 m0, m0, 1024\n\ts_nop 0\n\tbuffer_load_dwordx4 %5, %6, %1 offen lds\n\ts_add_u32 m0, m0, 1024\n\ts_add_u32 %1, %1, %9\n\tbuffer_load_dwordx4 %2, %6, %1 offen lds\n\ts_add_u32 m0, m0, 1024\n\ts_nop 0\n\tbuffer_load_dwordx4 %3, %6, %1 offen lds\n\ts_add_u32 m0, m0, 1024\n\ts_nop 0\n\tbuffer_load_dwordx4 %4, %6, %1 offen lds\n\ts_add_u32 m0, m0, 1024\n\ts_nop 0\n\tbuffer_load_dwordx4 %5, %6, %1 offen lds\n\ts_add_u32 m0, m0, 1024\n\ts_add_u32 %1, %1, %9\n\tbuffer_load_dwordx4 %2, %6, %1 offen lds\n\ts_add_u32 m0, m0, 1024\n\ts_nop 0\n\tbuffer_load_dwordx4 %3, %6, %1 offen lds\n\ts_add_u32 m0, m0, 1024\n\ts_nop 0\n\tbuffer_load_dwordx4 %4, %6, %1 offen lds\n\ts_add_u32 m0, m0, 1024\n\ts_nop 0\n\tbuffer_load_dwordx4 %5, %6, %1 offen lds\n\ts_add_u32 m0, m0, 1024\n\ts_add_u32 %1, %1, %9\n\tbuffer_load_dwordx4 %2, %6, %1 offen lds\n\ts_add_u32 m0, m0, 1024\n\ts_nop 0\n\tbuffer_load_dwordx4 %3, %6, %1 offen lds\n\ts_add_u32 m0, m0, 1024\n\ts_nop 0\n\tbuffer_load_dwordx4 %4, %6, %1 offen lds\n\ts_add_u32 m0, m0, 1024\n\ts_nop 0\n\tbuffer_load_dwordx4 %5, %6, %1 offen lds\n\ts_mov_b32 m0, %0"
               : "=&s"(keep), "=&s"(t) : "v"(vo[0]), "v"(vo[1]), "v"(vo[2]), "v"(vo[3]), "s"(srd), "s"(rfl((row0 + 64 * w) * pitch)), "s"(rfl(lds + w * 16384)), "s"(16 * pitch) : "memory", "scc");
}
template <int N> DEV void dma_run(u32x4 srd, u32 vo, u32 so, u32 lds) {
  u32 keep;
  asm volatile("s_mov_b32 %0, m0\n\ts_mov_b32 m0, %4\n\ts_nop 2\n\tbuffer_load_dwordx4 %1, %2, %3 offen sc1 lds\n\tbuffer_load_dwordx4 %1, %2, %3 offen offset:1024 sc1 lds\n\t"
               "buffer_load_dwordx4 %1, %2, %3 offen offset:2048 sc1 lds\n\tbuffer_load_dwordx4 %1, %2, %3 offen offset:3072 sc1 lds\n\ts_mov_b32 m0, %0"
               : "=&s"(keep) : "v"(vo), "s"(srd), "s"(so), "s"(lds) : "memory");
  if constexpr (N > 4) dma_run<N - 4>(srd, vo, so + 4096, lds + 4096);
}
DEV void stage_offsets(u32 (&vo)[4], u32 pitch, u32 col, int lane) {
#pragma unroll
  for (int i = 0; i < 4; ++i) { u32 row = 4 * i + (lane >> 4); vo[i] = row * pitch + col + (((u32)lane & 15u) ^ swz(row)) * 16; }
}
DEV u32 traddr(u32 row, u32 ch, u32 p) { return row * 256 + ((ch ^ swz(row)) << 4) + (p & 1) * 8; }

#define A_ASM3 \
  "ds_read_b128 v[40:43], v39 offset:0\n\t" \
  "ds_read_b128 v[44:47], v39 offset:16384\n\t" \
  "ds_read_b128 v[48:51], v39 offset:1024\n\t" \
  "ds_read_b128 v[52:55], v39 offset:17408\n\t" \
  "s_waitcnt lgkmcnt(2)\n\t" \
  "v_mfma_f32_16x16x32_bf16 v[64:67], a[0:3], v[40:43], 0\n\t" \
  "ds_read_b128 v[56:59], v39 offset:2048\n\t" \
  "v_mfma_f32_16x16x32_bf16 v[68:71], a[0:3], v[44:47], 0\n\t" \
  "ds_read_b128 v[60:63], v39 offset:18432\n\t" \
  "v_mfma_f32_16x16x32_bf16 v[72:75], a[64:67], v[40:43], 0\n\t" \
  "v_mfma_f32_16x16x32_bf16 v[76:79], a[64:67], v[44:47], 0\n\t" \
  "v_mfma_f32_16x16x32_bf16 v[80:83], a[128:131], v[40:43], 0\n\t" \
  "v_mfma_f32_16x16x32_bf16 v[84:87], a[128:131], v[44:47], 0\n\t" \
  "v_mfma_f32_16x16x32_bf16 v[88:91], a[192:195], v[40:43], 0\n\t" \
  "v_mfma_f32_16x16x32_bf16 v[92:95], a[192:195], v[44:47], 0\n\t" \
  "v_mfma_f32_16x16x32_bf16 v[96:99], v[40:43], v[128:131], 0\n\t" \
  "v_mfma_f32_16x16x32_bf16 v[100:103], v[44:47], v[128:131], 0\n\t" \
  "v_mfma_f32_16x16x32_bf16 v[104:107], v[40:43], v[192:195], 0\n\t" \
  "v_mfma_f32_16x16x32_bf16 v[108:111], v[44:47], v[192:195], 0\n\t" \
  "s_waitcnt lgkmcnt(2)\n\t" \
  "v_mfma_f32_16x16x32_bf16 v[64:67], a[4:7], v[48:51], v[64:67]\n\t" \
  "ds_read_b128 v[40:43], v39 offset:3072\n\t" \
  "v_mfma_f32_16x16x32_bf16 v[68:71], a[4:7], v[52:55], v[68:71]\n\t" \
  "ds_read_b128 v[44:47], v39 offset:19456\n\t" \
  "v_mfma_f32_16x16x32_bf16 v[72:75], a[68:71], v[48:51], v[72:75]\n\t" \
  "v_mfma_f32_16x16x32_bf16 v[76:79], a[68:71], v[52:55], v[76:79]\n\t" \
  "v_mfma_f32_16x16x32_bf16 v[80:83], a[132:135], v[48:51], v[80:83]\n\t" \
  "v_mfma_f32_16x16x32_bf16 v[84:87], a[132:135], v[52:55], v[84:87]\n\t" \
  "v_mfma_f32_16x16x32_bf16 v[88:91], a[196:199], v[48:51], v[88:91]\n\t" \
  "v_mfma_f32_16x16x32_bf16 v[92:95], a[196:199], v[52:55], v[92:95]\n\t" \
  "v_mfma_f32_16x16x32_bf16 v[96:99], v[48:51], v[132:135], v[96:99]\n\t" \
  "v_mfma_f32_16x16x32_bf16 v[100:103], v[52:55], v[132:135], v[100:103]\n\t" \
  "v_mfma_f32_16x16x32_bf16 v[104:107], v[48:51], v[196:199], v[104:107]\n\t" \
  "v_mfma_f32_16x16x32_bf16 v[108:111], v[52:55], v[196:199], v[108:111]\n\t" \
  "s_waitcnt lgkmcnt(2)\n\t" \
  "v_mfma_f32_16x16x32_bf16 v[64:67], a[8:11], v[56:59], v[64:67]\n\t" \
  "ds_read_b128 v[48:51], v39 offset:4096\n\t" \
  "v_mfma_f32_16x16x32_bf16 v[68:71], a[8:11], v[60:63], v[68:71]\n\t" \
  "ds_read_b128 v[52:55], v39 offset:20480\n\t" \
  "v_mfma_f32_16x16x32_bf16 v[72:75], a[72:75], v[56:59], v[72:75]\n\t" \
  "v_mfma_f32_16x16x32_bf16 v[76:79], a[72:75], v[60:63], v[76:79]\n\t" \
  "v_mfma_f32_16x16x32_bf16 v[80:83], a[136:139], v[56:59], v[80:83]\n\t" \
  "v_mfma_f32_16x16x32_bf16 v[84:87], a[136:139], v[60:63], v[84:87]\n\t" \
  "v_mfma_f32_16x16x32_bf16 v[88:91], a[200:203], v[56:59], v[88:91]\n\t" \
  "v_mfma_f32_16x16x32_bf16 v[92:95], a[200:203], v[60:63], v[92:95]\n\t" \
  "v_mfma_f32_16x16x32_bf16 v[96:99], v[56:59], v[136:139], v[96:99]\n\t" \
  "v_mfma_f32_16x16x32_bf16 v[100:103], v[60:63], v[136:139], v[100:103]\n\t" \
  "v_mfma_f32_16x16x32_bf16 v[104:107], v[56:59], v[200:203], v[104:107]\n\t" \
  "v_mfma_f32_16x16x32_bf16 v[108:111], v[60:63], v[200:203], v[108:111]\n\t" \
  "s_waitcnt lgkmcnt(2)\n\t" \
  "v_mfma_f32_16x16x32_bf16 v[64:67], a[12:15], v[40:43], v[64:67]\n\t" \
  "ds_read_b128 v[56:59], v39 offset:5120\n\t" \
  "v_mfma_f32_16x16x32_bf16 v[68:71], a[12:15], v[44:47], v[68:71]\n\t" \
  "ds_read_b128 v[60:63], v39 offset:21504\n\t" \
  "v_mfma_f32_16x16x32_bf16 v[72:75], a[76:79], v[40:43], v[72:75]\n\t" \
  "v_mfma_f32_16x16x32_bf16 v[76:79], a[76:79], v[44:47], v[76:79]\n\t" \
  "v_mfma_f32_16x16x32_bf16 v[80:83], a[140:143], v[40:43], v[80:83]\n\t" \
  "v_mfma_f32_16x16x32_bf16 v[84:87], a[140:143], v[44:47], v[84:87]\n\t" \
  "v_mfma_f32_16x16x32_bf16 v[88:91], a[204:207], v[40:43], v[88:91]\n\t" \
  "v_mfma_f32_16x16x32_bf16 v[92:95], a[204:207], v[44:47], v[92:95]\n\t" \
  "v_mfma_f32_16x16x32_bf16 v[96:99], v[40:43], v[140:143], v[96:99]\n\t" \
  "v_mfma_f32_16x16x32_bf16 v[100:103], v[44:47], v[140:143], v[100:103]\n\t" \
  "v_mfma_f32_16x16x32_bf16 v[104:107], v[40:43], v[204:207], v[104:107]\n\t" \
  "v_mfma_f32_16x16x32_bf16 v[108:111], v[44:47], v[204:207], v[108:111]\n\t" \
  "s_waitcnt lgkmcnt(2)\n\t" \
  "v_mfma_f32_16x16x32_bf16 v[64:67], a[16:19], v[48:51], v[64:67]\n\t" \
  "ds_read_b128 v[40:43], v39 offset:6144\n\t" \
  "v_mfma_f32_16x16x32_bf16 v[68:71], a[16:19], v[52:55], v[68:71]\n\t" \
  "ds_read_b128 v[44:47], v39 offset:22528\n\t" \
  "v_mfma_f32_16x16x32_bf16 v[72:75], a[80:83], v[48:51], v[72:75]\n\t" \
  "v_mfma_f32_16x16x32_bf16 v[76:79], a[80:83], v[52:55], v[76:79]\n\t" \
  "v_mfma_f32_16x16x32_bf16 v[80:83], a[144:147], v[48:51], v[80:83]\n\t" \
  "v_mfma_f32_16x16x32_bf16 v[84:87], a[144:147], v[52:55], v[84:87]\n\t" \
  "v_mfma_f32_16x16x32_bf16 v[88:91], a[208:211], v[48:51], v[88:91]\n\t" \
  "v_mfma_f32_16x16x32_bf16 v[92:95], a[208:211], v[52:55], v[92:95]\n\t" \
  "v_mfma_f32_16x16x32_bf16 v[96:99], v[48:51], v[144:147], v[96:99]\n\t" \
  "v_mfma_f32_16x16x32_bf16 v[100:103], v[52:55], v[144:147], v[100:103]\n\t" \
  "v_mfma_f32_16x16x32_bf16 v[104:107], v[48:51], v[208:211], v[104:107]\n\t" \
  "v_mfma_f32_16x16x32_bf16 v[108:111], v[52:55], v[208:211], v[108:111]\n\t" \
  "s_waitcnt lgkmcnt(2)\n\t" \
  "v_mfma_f32_16x16x32_bf16 v[64:67], a[20:23], v[56:59], v[64:67]\n\t" \
  "ds_read_b128 v[48:51], v39 offset:7168\n\t" \
  "v_mfma_f32_16x16x32_bf16 v[68:71], a[20:23], v[60:63], v[68:71]\n\t" \
  "ds_read_b128 v[52:55], v39 offset:23552\n\t" \
  "v_mfma_f32_16x16x32_bf16 v[72:75], a[84:87], v[56:59], v[72:75]\n\t" \
  "v_mfma_f32_16x16x32_bf16 v[76:79], a[84:87], v[60:63], v[76:79]\n\t" \
  "v_mfma_f32_16x16x32_bf16 v[80:83], a[148:151], v[56:59], v[80:83]\n\t" \
  "v_mfma_f32_16x16x32_bf16 v[84:87], a[148:151], v[60:63], v[84:87]\n\t" \
  "v_mfma_f32_16x16x32_bf16 v[88:91], a[212:215], v[56:59], v[88:91]\n\t" \
  "v_mfma_f32_16x16x32_bf16 v[92:95], a[212:215], v[60:63], v[92:95]\n\t" \
  "v_mfma_f32_16x16x32_bf16 v[96:99], v[56:59], v[148:151], v[96:99]\n\t" \
  "v_mfma_f32_16x16x32_bf16 v[100:103], v[60:63], v[148:151], v[100:103]\n\t" \
  "v_mfma_f32_16x16x32_bf16 v[104:107], v[56:59], v[212:215], v[104:107]\n\t" \
  "v_mfma_f32_16x16x32_bf16 v[108:111], v[60:63], v[212:215], v[108:111]\n\t" \
  "s_waitcnt lgkmcnt(2)\n\t" \
  "v_mfma_f32_16x16x32_bf16 v[64:67], a[24:27], v[40:43], v[64:67]\n\t" \
  "ds_read_b128 v[56:59], v39 offset:8192\n\t" \
  "v_mfma_f32_16x16x32_bf16 v[68:71], a[24:27], v[44:47], v[68:71]\n\t" \
  "ds_read_b128 v[60:63], v39 offset:24576\n\t" \
  "v_mfma_f32_16x16x32_bf16 v[72:75], a[88:91], v[40:43], v[72:75]\n\t" \
  "v_mfma_f32_16x16x32_bf16 v[76:79], a[88:91], v[44:47], v[76:79]\n\t" \
  "v_mfma_f32_16x16x32_bf16 v[80:83], a[152:155], v[40:43], v[80:83]\n\t" \
  "v_mfma_f32_16x16x32_bf16 v[84:87], a[152:155], v[44:47], v[84:87]\n\t" \
  "v_mfma_f32_16x16x32_bf16 v[88:91], a[216:219], v[40:43], v[88:91]\n\t" \
  "v_mfma_f32_16x16x32_bf16 v[92:95], a[216:219], v[44:47], v[92:95]\n\t" \
  "v_mfma_f32_16x16x32_bf16 v[96:99], v[40:43], v[152:155], v[96:99]\n\t" \
  "v_mfma_f32_16x16x32_bf16 v[100:103], v[44:47], v[152:155], v[100:103]\n\t" \
  "v_mfma_f32_16x16x32_bf16 v[104:107], v[40:43], v[216:219], v[104:107]\n\t" \
  "v_mfma_f32_16x16x32_bf16 v[108:111], v[44:47], v[216:219], v[108:111]\n\t" \
  "s_waitcnt lgkmcnt(2)\n\t" \
  "v_mfma_f32_16x16x32_bf16 v[64:67], a[28:31], v[48:51], v[64:67]\n\t" \
  "ds_read_b128 v[40:43], v39 offset:9216\n\t" \
  "v_mfma_f32_16x16x32_bf16 v[68:71], a[28:31], v[52:55], v[68:71]\n\t" \
  "ds_read_b128 v[44:47], v39 offset:25600\n\t" \
  "v_mfma_f32_16x16x32_bf16 v[72:75], a[92:95], v[48:51], v[72:75]\n\t" \
  "v_mfma_f32_16x16x32_bf16 v[76:79], a[92:95], v[52:55], v[76:79]\n\t" \
  "v_mfma_f32_16x16x32_bf16 v[80:83], a[156:159], v[48:51], v[80:83]\n\t" \
  "v_mfma_f32_16x16x32_bf16 v[84:87], a[156:159], v[52:55], v[84:87]\n\t" \
  "v_mfma_f32_16x16x32_bf16 v[88:91], a[220:223], v[48:51], v[88:91]\n\t" \
  "v_mfma_f32_16x16x32_bf16 v[92:95], a[220:223], v[52:55], v[92:95]\n\t" \
  "v_mfma_f32_16x16x32_bf16 v[96:99], v[48:51], v[156:159], v[96:99]\n\t" \
  "v_mfma_f32_16x16x32_bf16 v[100:103], v[52:55], v[156:159], v[100:103]\n\t" \
  "v_mfma_f32_16x16x32_bf16 v[104:107], v[48:51], v[220:223], v[104:107]\n\t" \
  "v_mfma_f32_16x16x32_bf16 v[108:111], v[52:55], v[220:223], v[108:111]\n\t" \
  "s_waitcnt lgkmcnt(2)\n\t" \
  "v_mfma_f32_16x16x32_bf16 v[64:67], a[32:35], v[56:59], v[64:67]\n\t" \
  "ds_read_b128 v[48:51], v39 offset:10240\n\t" \
  "v_mfma_f32_16x16x32_bf16 v[68:71], a[32:35], v[60:63], v[68:71]\n\t" \
  "ds_read_b128 v[52:55], v39 offset:26624\n\t" \
  "v_mfma_f32_16x16x32_bf16 v[72:75], a[96:99], v[56:59], v[72:75]\n\t" \
  "v_mfma_f32_16x16x32_bf16 v[76:79], a[96:99], v[60:63], v[76:79]\n\t" \
  "v_mfma_f32_16x16x32_bf16 v[80:83], a[160:163], v[56:59], v[80:83]\n\t" \
  "v_mfma_f32_16x16x32_bf16 v[84:87], a[160:163], v[60:63], v[84:87]\n\t" \
  "v_mfma_f32_16x16x32_bf16 v[88:91], a[224:227], v[56:59], v[88:91]\n\t" \
  "v_mfma_f32_16x16x32_bf16 v[92:95], a[224:227], v[60:63], v[92:95]\n\t" \
  "v_mfma_f32_16x16x32_bf16 v[96:99], v[56:59], v[160:163], v[96:99]\n\t" \
  "v_mfma_f32_16x16x32_bf16 v[100:103], v[60:63], v[160:163], v[100:103]\n\t" \
  "v_mfma_f32_16x16x32_bf16 v[104:107], v[56:59], v[224:227], v[104:107]\n\t" \
  "v_mfma_f32_16x16x32_bf16 v[108:111], v[60:63], v[224:227], v[108:111]\n\t" \
  "s_waitcnt lgkmcnt(2)\n\t" \
  "v_mfma_f32_16x16x32_bf16 v[64:67], a[36:39], v[40:43], v[64:67]\n\t" \
  "ds_read_b128 v[56:59], v39 offset:11264\n\t" \
  "v_mfma_f32_16x16x32_bf16 v[68:71], a[36:39], v[44:47], v[68:71]\n\t" \
  "ds_read_b128 v[60:63], v39 offset:27648\n\t" \
  "v_mfma_f32_16x16x32_bf16 v[72:75], a[100:103], v[40:43], v[72:75]\n\t" \
  "v_mfma_f32_16x16x32_bf16 v[76:79], a[100:103], v[44:47], v[76:79]\n\t" \
  "v_mfma_f32_16x16x32_bf16 v[80:83], a[164:167], v[40:43], v[80:83]\n\t" \
  "v_mfma_f32_16x16x32_bf16 v[84:87], a[164:167], v[44:47], v[84:87]\n\t" \
  "v_mfma_f32_16x16x32_bf16 v[88:91], a[228:231], v[40:43], v[88:91]\n\t" \
  "v_mfma_f32_16x16x32_bf16 v[92:95], a[228:231], v[44:47], v[92:95]\n\t" \
  "v_mfma_f32_16x16x32_bf16 v[96:99], v[40:43], v[164:167], v[96:99]\n\t" \
  "v_mfma_f32_16x16x32_bf16 v[100:103], v[44:47], v[164:167], v[100:103]\n\t" \
  "v_mfma_f32_16x16x32_bf16 v[104:107], v[40:43], v[228:231], v[104:107]\n\t" \
  "v_mfma_f32_16x16x32_bf16 v[108:111], v[44:47], v[228:231], v[108:111]\n\t" \
  "s_waitcnt lgkmcnt(2)\n\t" \
  "v_mfma_f32_16x16x32_bf16 v[64:67], a[40:43], v[48:51], v[64:67]\n\t" \
  "ds_read_b128 v[40:43], v39 offset:12288\n\t" \
  "v_mfma_f32_16x16x32_bf16 v[68:71], a[40:43], v[52:55], v[68:71]\n\t" \
  "ds_read_b128 v[44:47], v39 offset:28672\n\t" \
  "v_mfma_f32_16x16x32_bf16 v[72:75], a[104:107], v[48:51], v[72:75]\n\t" \
  "v_mfma_f32_16x16x32_bf16 v[76:79], a[104:107], v[52:55], v[76:79]\n\t" \
  "v_mfma_f32_16x16x32_bf16 v[80:83], a[168:171], v[48:51], v[80:83]\n\t" \
  "v_mfma_f32_16x16x32_bf16 v[84:87], a[168:171], v[52:55], v[84:87]\n\t" \
  "v_mfma_f32_16x16x32_bf16 v[88:91], a[232:235], v[48:51], v[88:91]\n\t" \
  "v_mfma_f32_16x16x32_bf16 v[92:95], a[232:235], v[52:55], v[92:95]\n\t" \
  "v_mfma_f32_16x16x32_bf16 v[96:99], v[48:51], v[168:171], v[96:99]\n\t" \
  "v_mfma_f32_16x16x32_bf16 v[100:103], v[52:55], v[168:171], v[100:103]\n\t" \
  "v_mfma_f32_16x16x32_bf16 v[104:107], v[48:51], v[232:235], v[104:107]\n\t" \
  "v_mfma_f32_16x16x32_bf16 v[108:111], v[52:55], v[232:235], v[108:111]\n\t" \
  "s_waitcnt lgkmcnt(2)\n\t" \
  "v_mfma_f32_16x16x32_bf16 v[64:67], a[44:47], v[56:59], v[64:67]\n\t" \
  "ds_read_b128 v[48:51], v39 offset:13312\n\t" \
  "v_mfma_f32_16x16x32_bf16 v[68:71], a[44:47], v[60:63], v[68:71]\n\t" \
  "ds_read_b128 v[52:55], v39 offset:29696\n\t" \
  "v_mfma_f32_16x16x32_bf16 v[72:75], a[108:111], v[56:59], v[72:75]\n\t" \
  "v_mfma_f32_16x16x32_bf16 v[76:79], a[108:111], v[60:63], v[76:79]\n\t" \
  "v_mfma_f32_16x16x32_bf16 v[80:83], a[172:175], v[56:59], v[80:83]\n\t" \
  "v_mfma_f32_16x16x32_bf16 v[84:87], a[172:175], v[60:63], v[84:87]\n\t" \
  "v_mfma_f32_16x16x32_bf16 v[88:91], a[236:239], v[56:59], v[88:91]\n\t" \
  "v_mfma_f32_16x16x32_bf16 v[92:95], a[236:239], v[60:63], v[92:95]\n\t" \
  "v_mfma_f32_16x16x32_bf16 v[96:99], v[56:59], v[172:175], v[96:99]\n\t" \
  "v_mfma_f32_16x16x32_bf16 v[100:103], v[60:63], v[172:175], v[100:103]\n\t" \
  "v_mfma_f32_16x16x32_bf16 v[104:107], v[56:59], v[236:239], v[104:107]\n\t" \
  "v_mfma_f32_16x16x32_bf16 v[108:111], v[60:63], v[236:239], v[108:111]\n\t" \
  "s_waitcnt lgkmcnt(2)\n\t" \
  "v_mfma_f32_16x16x32_bf16 v[64:67], a[48:51], v[40:43], v[64:67]\n\t" \
  "ds_read_b128 v[56:59], v39 offset:14336\n\t" \
  "v_mfma_f32_16x16x32_bf16 v[68:71], a[48:51], v[44:47], v[68:71]\n\t" \
  "ds_read_b128 v[60:63], v39 offset:30720\n\t" \
  "v_mfma_f32_16x16x32_bf16 v[72:75], a[112:115], v[40:43], v[72:75]\n\t" \
  "v_mfma_f32_16x16x32_bf16 v[76:79], a[112:115], v[44:47], v[76:79]\n\t" \
  "v_mfma_f32_16x16x32_bf16 v[80:83], a[176:179], v[40:43], v[80:83]\n\t" \
  "v_mfma_f32_16x16x32_bf16 v[84:87], a[176:179], v[44:47], v[84:87]\n\t" \
  "v_mfma_f32_16x16x32_bf16 v[88:91], a[240:243], v[40:43], v[88:91]\n\t" \
  "v_mfma_f32_16x16x32_bf16 v[92:95], a[240:243], v[44:47], v[92:95]\n\t" \
  "v_mfma_f32_16x16x32_bf16 v[96:99], v[40:43], v[176:179], v[96:99]\n\t" \
  "v_mfma_f32_16x16x32_bf16 v[100:103], v[44:47], v[176:179], v[100:103]\n\t" \
  "v_mfma_f32_16x16x32_bf16 v[104:107], v[40:43], v[240:243], v[104:107]\n\t" \
  "v_mfma_f32_16x16x32_bf16 v[108:111], v[44:47], v[240:243], v[108:111]\n\t" \
  "s_waitcnt lgkmcnt(2)\n\t" \
  "v_mfma_f32_16x16x32_bf16 v[64:67], a[52:55], v[48:51], v[64:67]\n\t" \
  "ds_read_b128 v[40:43], v39 offset:15360\n\t" \
  "v_mfma_f32_16x16x32_bf16 v[68:71], a[52:55], v[52:55], v[68:71]\n\t" \
  "ds_read_b128 v[44:47], v39 offset:31744\n\t" \
  "v_mfma_f32_16x16x32_bf16 v[72:75], a[116:119], v[48:51], v[72:75]\n\t" \
  "v_mfma_f32_16x16x32_bf16 v[76:79], a[116:119], v[52:55], v[76:79]\n\t" \
  "v_mfma_f32_16x16x32_bf16 v[80:83], a[180:183], v[48:51], v[80:83]\n\t" \
  "v_mfma_f32_16x16x32_bf16 v[84:87], a[180:183], v[52:55], v[84:87]\n\t" \
  "v_mfma_f32_16x16x32_bf16 v[88:91], a[244:247], v[48:51], v[88:91]\n\t" \
  "v_mfma_f32_16x16x32_bf16 v[92:95], a[244:247], v[52:55], v[92:95]\n\t" \
  "v_mfma_f32_16x16x32_bf16 v[96:99], v[48:51], v[180:183], v[96:99]\n\t" \
  "v_mfma_f32_16x16x32_bf16 v[100:103], v[52:55], v[180:183], v[100:103]\n\t" \
  "v_mfma_f32_16x16x32_bf16 v[104:107], v[48:51], v[244:247], v[104:107]\n\t" \
  "v_mfma_f32_16x16x32_bf16 v[108:111], v[52:55], v[244:247], v[108:111]\n\t" \
  "s_waitcnt lgkmcnt(2)\n\t" \
  "v_mfma_f32_16x16x32_bf16 v[64:67], a[56:59], v[56:59], v[64:67]\n\t" \
  "v_mfma_f32_16x16x32_bf16 v[68:71], a[56:59], v[60:63], v[68:71]\n\t" \
  "v_mfma_f32_16x16x32_bf16 v[72:75], a[120:123], v[56:59], v[72:75]\n\t" \
  "v_mfma_f32_16x16x32_bf16 v[76:79], a[120:123], v[60:63], v[76:79]\n\t" \
  "v_mfma_f32_16x16x32_bf16 v[80:83], a[184:187], v[56:59], v[80:83]\n\t" \
  "v_mfma_f32_16x16x32_bf16 v[84:87], a[184:187], v[60:63], v[84:87]\n\t" \
  "v_mfma_f32_16x16x32_bf16 v[88:91], a[248:251], v[56:59], v[88:91]\n\t" \
  "v_mfma_f32_16x16x32_bf16 v[92:95], a[248:251], v[60:63], v[92:95]\n\t" \
  "v_mfma_f32_16x16x32_bf16 v[96:99], v[56:59], v[184:187], v[96:99]\n\t" \
  "v_mfma_f32_16x16x32_bf16 v[100:103], v[60:63], v[184:187], v[100:103]\n\t" \
  "v_mfma_f32_16x16x32_bf16 v[104:107], v[56:59], v[248:251], v[104:107]\n\t" \
  "v_mfma_f32_16x16x32_bf16 v[108:111], v[60:63], v[248:251], v[108:111]\n\t" \
  "s_waitcnt lgkmcnt(0)\n\t" \
  "v_mfma_f32_16x16x32_bf16 v[64:67], a[60:63], v[40:43], v[64:67]\n\t" \
  "v_mfma_f32_16x16x32_bf16 v[68:71], a[60:63], v[44:47], v[68:71]\n\t" \
  "v_mfma_f32_16x16x32_bf16 v[72:75], a[124:127], v[40:43], v[72:75]\n\t" \
  "v_mfma_f32_16x16x32_bf16 v[76:79], a[124:127], v[44:47], v[76:79]\n\t" \
  "v_mfma_f32_16x16x32_bf16 v[80:83], a[188:191], v[40:43], v[80:83]\n\t" \
  "v_mfma_f32_16x16x32_bf16 v[84:87], a[188:191], v[44:47], v[84:87]\n\t" \
  "v_mfma_f32_16x16x32_bf16 v[88:91], a[252:255], v[40:43], v[88:91]\n\t" \
  "v_mfma_f32_16x16x32_bf16 v[92:95], a[252:255], v[44:47], v[92:95]\n\t" \
  "v_mfma_f32_16x16x32_bf16 v[96:99], v[40:43], v[188:191], v[96:99]\n\t" \
  "v_mfma_f32_16x16x32_bf16 v[100:103], v[44:47], v[188:191], v[100:103]\n\t" \
  "v_mfma_f32_16x16x32_bf16 v[104:107], v[40:43], v[252:255], v[104:107]\n\t" \
  "v_mfma_f32_16x16x32_bf16 v[108:111], v[44:47], v[252:255], v[108:111]\n\t" \
  "s_nop 7\n\t"
#define A_ASM2 \
  "ds_read_b128 v[40:43], v39 offset:0\n\t" \
  "ds_read_b128 v[44:47], v39 offset:16384\n\t" \
  "ds_read_b128 v[48:51], v39 offset:1024\n\t" \
  "ds_read_b128 v[52:55], v39 offset:17408\n\t" \
  "s_waitcnt lgkmcnt(2)\n\t" \
  "v_mfma_f32_16x16x32_bf16 v[64:67], a[0:3], v[40:43], 0\n\t" \
  "ds_read_b128 v[56:59], v39 offset:2048\n\t" \
  "v_mfma_f32_16x16x32_bf16 v[68:71], a[0:3], v[44:47], 0\n\t" \
  "ds_read_b128 v[60:63], v39 offset:18432\n\t" \
  "v_mfma_f32_16x16x32_bf16 v[72:75], a[64:67], v[40:43], 0\n\t" \
  "v_mfma_f32_16x16x32_bf16 v[76:79], a[64:67], v[44:47], 0\n\t" \
  "v_mfma_f32_16x16x32_bf16 v[80:83], a[128:131], v[40:43], 0\n\t" \
  "v_mfma_f32_16x16x32_bf16 v[84:87], a[128:131], v[44:47], 0\n\t" \
  "v_mfma_f32_16x16x32_bf16 v[88:91], a[192:195], v[40:43], 0\n\t" \
  "v_mfma_f32_16x16x32_bf16 v[92:95], a[192:195], v[44:47], 0\n\t" \
  "s_waitcnt lgkmcnt(2)\n\t" \
  "v_mfma_f32_16x16x32_bf16 v[64:67], a[4:7], v[48:51], v[64:67]\n\t" \
  "ds_read_b128 v[40:43], v39 offset:3072\n\t" \
  "v_mfma_f32_16x16x32_bf16 v[68:71], a[4:7], v[52:55], v[68:71]\n\t" \
  "ds_read_b128 v[44:47], v39 offset:19456\n\t" \
  "v_mfma_f32_16x16x32_bf16 v[72:75], a[68:71], v[48:51], v[72:75]\n\t" \
  "v_mfma_f32_16x16x32_bf16 v[76:79], a[68:71], v[52:55], v[76:79]\n\t" \
  "v_mfma_f32_16x16x32_bf16 v[80:83], a[132:135], v[48:51], v[80:83]\n\t" \
  "v_mfma_f32_16x16x32_bf16 v[84:87], a[132:135], v[52:55], v[84:87]\n\t" \
  "v_mfma_f32_16x16x32_bf16 v[88:91], a[196:199], v[48:51], v[88:91]\n\t" \
  "v_mfma_f32_16x16x32_bf16 v[92:95], a[196:199], v[52:55], v[92:95]\n\t" \
  "s_waitcnt lgkmcnt(2)\n\t" \
  "v_mfma_f32_16x16x32_bf16 v[64:67], a[8:11], v[56:59], v[64:67]\n\t" \
  "ds_read_b128 v[48:51], v39 offset:4096\n\t" \
  "v_mfma_f32_16x16x32_bf16 v[68:71], a[8:11], v[60:63], v[68:71]\n\t" \
  "ds_read_b128 v[52:55], v39 offset:20480\n\t" \
  "v_mfma_f32_16x16x32_bf16 v[72:75], a[72:75], v[56:59], v[72:75]\n\t" \
  "v_mfma_f32_16x16x32_bf16 v[76:79], a[72:75], v[60:63], v[76:79]\n\t" \
  "v_mfma_f32_16x16x32_bf16 v[80:83], a[136:139], v[56:59], v[80:83]\n\t" \
  "v_mfma_f32_16x16x32_bf16 v[84:87], a[136:139], v[60:63], v[84:87]\n\t" \
  "v_mfma_f32_16x16x32_bf16 v[88:91], a[200:203], v[56:59], v[88:91]\n\t" \
  "v_mfma_f32_16x16x32_bf16 v[92:95], a[200:203], v[60:63], v[92:95]\n\t" \
  "s_waitcnt lgkmcnt(2)\n\t" \
  "v_mfma_f32_16x16x32_bf16 v[64:67], a[12:15], v[40:43], v[64:67]\n\t" \
  "ds_read_b128 v[56:59], v39 offset:5120\n\t" \
  "v_mfma_f32_16x16x32_bf16 v[68:71], a[12:15], v[44:47], v[68:71]\n\t" \
  "ds_read_b128 v[60:63], v39 offset:21504\n\t" \
  "v_mfma_f32_16x16x32_bf16 v[72:75], a[76:79], v[40:43], v[72:75]\n\t" \
  "v_mfma_f32_16x16x32_bf16 v[76:79], a[76:79], v[44:47], v[76:79]\n\t" \
  "v_mfma_f32_16x16x32_bf16 v[80:83], a[140:143], v[40:43], v[80:83]\n\t" \
  "v_mfma_f32_16x16x32_bf16 v[84:87], a[140:143], v[44:47], v[84:87]\n\t" \
  "v_mfma_f32_16x16x32_bf16 v[88:91], a[204:207], v[40:43], v[88:91]\n\t" \
  "v_mfma_f32_16x16x32_bf16 v[92:95], a[204:207], v[44:47], v[92:95]\n\t" \
  "s_waitcnt lgkmcnt(2)\n\t" \
  "v_mfma_f32_16x16x32_bf16 v[64:67], a[16:19], v[48:51], v[64:67]\n\t" \
  "ds_read_b128 v[40:43], v39 offset:6144\n\t" \
  "v_mfma_f32_16x16x32_bf16 v[68:71], a[16:19], v[52:55], v[68:71]\n\t" \
  "ds_read_b128 v[44:47], v39 offset:22528\n\t" \
  "v_mfma_f32_16x16x32_bf16 v[72:75], a[80:83], v[48:51], v[72:75]\n\t" \
  "v_mfma_f32_16x16x32_bf16 v[76:79], a[80:83], v[52:55], v[76:79]\n\t" \
  "v_mfma_f32_16x16x32_bf16 v[80:83], a[144:147], v[48:51], v[80:83]\n\t" \
  "v_mfma_f32_16x16x32_bf16 v[84:87], a[144:147], v[52:55], v[84:87]\n\t" \
  "v_mfma_f32_16x16x32_bf16 v[88:91], a[208:211], v[48:51], v[88:91]\n\t" \
  "v_mfma_f32_16x16x32_bf16 v[92:95], a[208:211], v[52:55], v[92:95]\n\t" \
  "s_waitcnt lgkmcnt(2)\n\t" \
  "v_mfma_f32_16x16x32_bf16 v[64:67], a[20:23], v[56:59], v[64:67]\n\t" \
  "ds_read_b128 v[48:51], v39 offset:7168\n\t" \
  "v_mfma_f32_16x16x32_bf16 v[68:71], a[20:23], v[60:63], v[68:71]\n\t" \
  "ds_read_b128 v[52:55], v39 offset:23552\n\t" \
  "v_mfma_f32_16x16x32_bf16 v[72:75], a[84:87], v[56:59], v[72:75]\n\t" \
  "v_mfma_f32_16x16x32_bf16 v[76:79], a[84:87], v[60:63], v[76:79]\n\t" \
  "v_mfma_f32_16x16x32_bf16 v[80:83], a[148:151], v[56:59], v[80:83]\n\t" \
  "v_mfma_f32_16x16x32_bf16 v[84:87], a[148:151], v[60:63], v[84:87]\n\t" \
  "v_mfma_f32_16x16x32_bf16 v[88:91], a[212:215], v[56:59], v[88:91]\n\t" \
  "v_mfma_f32_16x16x32_bf16 v[92:95], a[212:215], v[60:63], v[92:95]\n\t" \
  "s_waitcnt lgkmcnt(2)\n\t" \
  "v_mfma_f32_16x16x32_bf16 v[64:67], a[24:27], v[40:43], v[64:67]\n\t" \
  "ds_read_b128 v[56:59], v39 offset:8192\n\t" \
  "v_mfma_f32_16x16x32_bf16 v[68:71], a[24:27], v[44:47], v[68:71]\n\t" \
  "ds_read_b128 v[60:63], v39 offset:24576\n\t" \
  "v_mfma_f32_16x16x32_bf16 v[72:75], a[88:91], v[40:43], v[72:75]\n\t" \
  "v_mfma_f32_16x16x32_bf16 v[76:79], a[88:91], v[44:47], v[76:79]\n\t" \
  "v_mfma_f32_16x16x32_bf16 v[80:83], a[152:155], v[40:43], v[80:83]\n\t" \
  "v_mfma_f32_16x16x32_bf16 v[84:87], a[152:155], v[44:47], v[84:87]\n\t" \
  "v_mfma_f32_16x16x32_bf16 v[88:91], a[216:219], v[40:43], v[88:91]\n\t" \
  "v_mfma_f32_16x16x32_bf16 v[92:95], a[216:219], v[44:47], v[92:95]\n\t" \
  "s_waitcnt lgkmcnt(2)\n\t" \
  "v_mfma_f32_16x16x32_bf16 v[64:67], a[28:31], v[48:51], v[64:67]\n\t" \
  "ds_read_b128 v[40:43], v39 offset:9216\n\t" \
  "v_mfma_f32_16x16x32_bf16 v[68:71], a[28:31], v[52:55], v[68:71]\n\t" \
  "ds_read_b128 v[44:47], v39 offset:25600\n\t" \
  "v_mfma_f32_16x16x32_bf16 v[72:75], a[92:95], v[48:51], v[72:75]\n\t" \
  "v_mfma_f32_16x16x32_bf16 v[76:79], a[92:95], v[52:55], v[76:79]\n\t" \
  "v_mfma_f32_16x16x32_bf16 v[80:83], a[156:159], v[48:51], v[80:83]\n\t" \
  "v_mfma_f32_16x16x32_bf16 v[84:87], a[156:159], v[52:55], v[84:87]\n\t" \
  "v_mfma_f32_16x16x32_bf16 v[88:91], a[220:223], v[48:51], v[88:91]\n\t" \
  "v_mfma_f32_16x16x32_bf16 v[92:95], a[220:223], v[52:55], v[92:95]\n\t" \
  "s_waitcnt lgkmcnt(2)\n\t" \
  "v_mfma_f32_16x16x32_bf16 v[64:67], a[32:35], v[56:59], v[64:67]\n\t" \
  "ds_read_b128 v[48:51], v39 offset:10240\n\t" \
  "v_mfma_f32_16x16x32_bf16 v[68:71], a[32:35], v[60:63], v[68:71]\n\t" \
  "ds_read_b128 v[52:55], v39 offset:26624\n\t" \
  "v_mfma_f32_16x16x32_bf16 v[72:75], a[96:99], v[56:59], v[72:75]\n\t" \
  "v_mfma_f32_16x16x32_bf16 v[76:79], a[96:99], v[60:63], v[76:79]\n\t" \
  "v_mfma_f32_16x16x32_bf16 v[80:83], a[160:163], v[56:59], v[80:83]\n\t" \
  "v_mfma_f32_16x16x32_bf16 v[84:87], a[160:163], v[60:63], v[84:87]\n\t" \
  "v_mfma_f32_16x16x32_bf16 v[88:91], a[224:227], v[56:59], v[88:91]\n\t" \
  "v_mfma_f32_16x16x32_bf16 v[92:95], a[224:227], v[60:63], v[92:95]\n\t" \
  "s_waitcnt lgkmcnt(2)\n\t" \
  "v_mfma_f32_16x16x32_bf16 v[64:67], a[36:39], v[40:43], v[64:67]\n\t" \
  "ds_read_b128 v[56:59], v39 offset:11264\n\t" \
  "v_mfma_f32_16x16x32_bf16 v[68:71], a[36:39], v[44:47], v[68:71]\n\t" \
  "ds_read_b128 v[60:63], v39 offset:27648\n\t" \
  "v_mfma_f32_16x16x32_bf16 v[72:75], a[100:103], v[40:43], v[72:75]\n\t" \
  "v_mfma_f32_16x16x32_bf16 v[76:79], a[100:103], v[44:47], v[76:79]\n\t" \
  "v_mfma_f32_16x16x32_bf16 v[80:83], a[164:167], v[40:43], v[80:83]\n\t" \
  "v_mfma_f32_16x16x32_bf16 v[84:87], a[164:167], v[44:47], v[84:87]\n\t" \
  "v_mfma_f32_16x16x32_bf16 v[88:91], a[228:231], v[40:43], v[88:91]\n\t" \
  "v_mfma_f32_16x16x32_bf16 v[92:95], a[228:231], v[44:47], v[92:95]\n\t" \
  "s_waitcnt lgkmcnt(2)\n\t" \
  "v_mfma_f32_16x16x32_bf16 v[64:67], a[40:43], v[48:51], v[64:67]\n\t" \
  "ds_read_b128 v[40:43], v39 offset:12288\n\t" \
  "v_mfma_f32_16x16x32_bf16 v[68:71], a[40:43], v[52:55], v[68:71]\n\t" \
  "ds_read_b128 v[44:47], v39 offset:28672\n\t" \
  "v_mfma_f32_16x16x32_bf16 v[72:75], a[104:107], v[48:51], v[72:75]\n\t" \
  "v_mfma_f32_16x16x32_bf16 v[76:79], a[104:107], v[52:55], v[76:79]\n\t" \
  "v_mfma_f32_16x16x32_bf16 v[80:83], a[168:171], v[48:51], v[80:83]\n\t" \
  "v_mfma_f32_16x16x32_bf16 v[84:87], a[168:171], v[52:55], v[84:87]\n\t" \
  "v_mfma_f32_16x16x32_bf16 v[88:91], a[232:235], v[48:51], v[88:91]\n\t" \
  "v_mfma_f32_16x16x32_bf16 v[92:95], a[232:235], v[52:55], v[92:95]\n\t" \
  "s_waitcnt lgkmcnt(2)\n\t" \
  "v_mfma_f32_16x16x32_bf16 v[64:67], a[44:47], v[56:59], v[64:67]\n\t" \
  "ds_read_b128 v[48:51], v39 offset:13312\n\t" \
  "v_mfma_f32_16x16x32_bf16 v[68:71], a[44:47], v[60:63], v[68:71]\n\t" \
  "ds_read_b128 v[52:55], v39 offset:29696\n\t" \
  "v_mfma_f32_16x16x32_bf16 v[72:75], a[108:111], v[56:59], v[72:75]\n\t" \
  "v_mfma_f32_16x16x32_bf16 v[76:79], a[108:111], v[60:63], v[76:79]\n\t" \
  "v_mfma_f32_16x16x32_bf16 v[80:83], a[172:175], v[56:59], v[80:83]\n\t" \
  "v_mfma_f32_16x16x32_bf16 v[84:87], a[172:175], v[60:63], v[84:87]\n\t" \
  "v_mfma_f32_16x16x32_bf16 v[88:91], a[236:239], v[56:59], v[88:91]\n\t" \
  "v_mfma_f32_16x16x32_bf16 v[92:95], a[236:239], v[60:63], v[92:95]\n\t" \
  "s_waitcnt lgkmcnt(2)\n\t" \
  "v_mfma_f32_16x16x32_bf16 v[64:67], a[48:51], v[40:43], v[64:67]\n\t" \
  "ds_read_b128 v[56:59], v39 offset:14336\n\t" \
  "v_mfma_f32_16x16x32_bf16 v[68:71], a[48:51], v[44:47], v[68:71]\n\t" \
  "ds_read_b128 v[60:63], v39 offset:30720\n\t" \
  "v_mfma_f32_16x16x32_bf16 v[72:75], a[112:115], v[40:43], v[72:75]\n\t" \
  "v_mfma_f32_16x16x32_bf16 v[76:79], a[112:115], v[44:47], v[76:79]\n\t" \
  "v_mfma_f32_16x16x32_bf16 v[80:83], a[176:179], v[40:43], v[80:83]\n\t" \
  "v_mfma_f32_16x16x32_bf16 v[84:87], a[176:179], v[44:47], v[84:87]\n\t" \
  "v_mfma_f32_16x16x32_bf16 v[88:91], a[240:243], v[40:43], v[88:91]\n\t" \
  "v_mfma_f32_16x16x32_bf16 v[92:95], a[240:243], v[44:47], v[92:95]\n\t" \
  "s_waitcnt lgkmcnt(2)\n\t" \
  "v_mfma_f32_16x16x32_bf16 v[64:67], a[52:55], v[48:51], v[64:67]\n\t" \
  "ds_read_b128 v[40:43], v39 offset:15360\n\t" \
  "v_mfma_f32_16x16x32_bf16 v[68:71], a[52:55], v[52:55], v[68:71]\n\t" \
  "ds_read_b128 v[44:47], v39 offset:31744\n\t" \
  "v_mfma_f32_16x16x32_bf16 v[72:75], a[116:119], v[48:51], v[72:75]\n\t" \
  "v_mfma_f32_16x16x32_bf16 v[76:79], a[116:119], v[52:55], v[76:79]\n\t" \
  "v_mfma_f32_16x16x32_bf16 v[80:83], a[180:183], v[48:51], v[80:83]\n\t" \
  "v_mfma_f32_16x16x32_bf16 v[84:87], a[180:183], v[52:55], v[84:87]\n\t" \
  "v_mfma_f32_16x16x32_bf16 v[88:91], a[244:247], v[48:51], v[88:91]\n\t" \
  "v_mfma_f32_16x16x32_bf16 v[92:95], a[244:247], v[52:55], v[92:95]\n\t" \
  "s_waitcnt lgkmcnt(2)\n\t" \
  "v_mfma_f32_16x16x32_bf16 v[64:67], a[56:59], v[56:59], v[64:67]\n\t" \
  "v_mfma_f32_16x16x32_bf16 v[68:71], a[56:59], v[60:63], v[68:71]\n\t" \
  "v_mfma_f32_16x16x32_bf16 v[72:75], a[120:123], v[56:59], v[72:75]\n\t" \
  "v_mfma_f32_16x16x32_bf16 v[76:79], a[120:123], v[60:63], v[76:79]\n\t" \
  "v_mfma_f32_16x16x32_bf16 v[80:83], a[184:187], v[56:59], v[80:83]\n\t" \
  "v_mfma_f32_16x16x32_bf16 v[84:87], a[184:187], v[60:63], v[84:87]\n\t" \
  "v_mfma_f32_16x16x32_bf16 v[88:91], a[248:251], v[56:59], v[88:91]\n\t" \
  "v_mfma_f32_16x16x32_bf16 v[92:95], a[248:251], v[60:63], v[92:95]\n\t" \
  "s_waitcnt lgkmcnt(0)\n\t" \
  "v_mfma_f32_16x16x32_bf16 v[64:67], a[60:63], v[40:43], v[64:67]\n\t" \
  "v_mfma_f32_16x16x32_bf16 v[68:71], a[60:63], v[44:47], v[68:71]\n\t" \
  "v_mfma_f32_16x16x32_bf16 v[72:75], a[124:127], v[40:43], v[72:75]\n\t" \
  "v_mfma_f32_16x16x32_bf16 v[76:79], a[124:127], v[44:47], v[76:79]\n\t" \
  "v_mfma_f32_16x16x32_bf16 v[80:83], a[188:191], v[40:43], v[80:83]\n\t" \
  "v_mfma_f32_16x16x32_bf16 v[84:87], a[188:191], v[44:47], v[84:87]\n\t" \
  "v_mfma_f32_16x16x32_bf16 v[88:91], a[252:255], v[40:43], v[88:91]\n\t" \
  "v_mfma_f32_16x16x32_bf16 v[92:95], a[252:255], v[44:47], v[92:95]\n\t" \
  "s_nop 7\n\t"
#define A_ASMKV \
  "ds_read_b128 v[40:43], v39 offset:0\n\t" \
  "ds_read_b128 v[44:47], v39 offset:16384\n\t" \
  "ds_read_b128 v[48:51], v39 offset:1024\n\t" \
  "ds_read_b128 v[52:55], v39 offset:17408\n\t" \
  "s_waitcnt lgkmcnt(2)\n\t" \
  "v_mfma_f32_16x16x32_bf16 v[80:83], a[128:131], v[40:43], 0\n\t" \
  "ds_read_b128 v[56:59], v39 offset:2048\n\t" \
  "v_mfma_f32_16x16x32_bf16 v[84:87], a[128:131], v[44:47], 0\n\t" \
  "ds_read_b128 v[60:63], v39 offset:18432\n\t" \
  "v_mfma_f32_16x16x32_bf16 v[88:91], a[192:195], v[40:43], 0\n\t" \
  "v_mfma_f32_16x16x32_bf16 v[92:95], a[192:195], v[44:47], 0\n\t" \
  "v_mfma_f32_16x16x32_bf16 v[96:99], v[40:43], v[128:131], 0\n\t" \
  "v_mfma_f32_16x16x32_bf16 v[100:103], v[44:47], v[128:131], 0\n\t" \
  "v_mfma_f32_16x16x32_bf16 v[104:107], v[40:43], v[192:195], 0\n\t" \
  "v_mfma_f32_16x16x32_bf16 v[108:111], v[44:47], v[192:195], 0\n\t" \
  "s_waitcnt lgkmcnt(2)\n\t" \
  "v_mfma_f32_16x16x32_bf16 v[80:83], a[132:135], v[48:51], v[80:83]\n\t" \
  "ds_read_b128 v[40:43], v39 offset:3072\n\t" \
  "v_mfma_f32_16x16x32_bf16 v[84:87], a[132:135], v[52:55], v[84:87]\n\t" \
  "ds_read_b128 v[44:47], v39 offset:19456\n\t" \
  "v_mfma_f32_16x16x32_bf16 v[88:91], a[196:199], v[48:51], v[88:91]\n\t" \
  "v_mfma_f32_16x16x32_bf16 v[92:95], a[196:199], v[52:55], v[92:95]\n\t" \
  "v_mfma_f32_16x16x32_bf16 v[96:99], v[48:51], v[132:135], v[96:99]\n\t" \
  "v_mfma_f32_16x16x32_bf16 v[100:103], v[52:55], v[132:135], v[100:103]\n\t" \
  "v_mfma_f32_16x16x32_bf16 v[104:107], v[48:51], v[196:199], v[104:107]\n\t" \
  "v_mfma_f32_16x16x32_bf16 v[108:111], v[52:55], v[196:199], v[108:111]\n\t" \
  "s_waitcnt lgkmcnt(2)\n\t" \
  "v_mfma_f32_16x16x32_bf16 v[80:83], a[136:139], v[56:59], v[80:83]\n\t" \
  "ds_read_b128 v[48:51], v39 offset:4096\n\t" \
  "v_mfma_f32_16x16x32_bf16 v[84:87], a[136:139], v[60:63], v[84:87]\n\t" \
  "ds_read_b128 v[52:55], v39 offset:20480\n\t" \
  "v_mfma_f32_16x16x32_bf16 v[88:91], a[200:203], v[56:59], v[88:91]\n\t" \
  "v_mfma_f32_16x16x32_bf16 v[92:95], a[200:203], v[60:63], v[92:95]\n\t" \
  "v_mfma_f32_16x16x32_bf16 v[96:99], v[56:59], v[136:139], v[96:99]\n\t" \
  "v_mfma_f32_16x16x32_bf16 v[100:103], v[60:63], v[136:139], v[100:103]\n\t" \
  "v_mfma_f32_16x16x32_bf16 v[104:107], v[56:59], v[200:203], v[104:107]\n\t" \
  "v_mfma_f32_16x16x32_bf16 v[108:111], v[60:63], v[200:203], v[108:111]\n\t" \
  "s_waitcnt lgkmcnt(2)\n\t" \
  "v_mfma_f32_16x16x32_bf16 v[80:83], a[140:143], v[40:43], v[80:83]\n\t" \
  "ds_read_b128 v[56:59], v39 offset:5120\n\t" \
  "v_mfma_f32_16x16x32_bf16 v[84:87], a[140:143], v[44:47], v[84:87]\n\t" \
  "ds_read_b128 v[60:63], v39 offset:21504\n\t" \
  "v_mfma_f32_16x16x32_bf16 v[88:91], a[204:207], v[40:43], v[88:91]\n\t" \
  "v_mfma_f32_16x16x32_bf16 v[92:95], a[204:207], v[44:47], v[92:95]\n\t" \
  "v_mfma_f32_16x16x32_bf16 v[96:99], v[40:43], v[140:143], v[96:99]\n\t" \
  "v_mfma_f32_16x16x32_bf16 v[100:103], v[44:47], v[140:143], v[100:103]\n\t" \
  "v_mfma_f32_16x16x32_bf16 v[104:107], v[40:43], v[204:207], v[104:107]\n\t" \
  "v_mfma_f32_16x16x32_bf16 v[108:111], v[44:47], v[204:207], v[108:111]\n\t" \
  "s_waitcnt lgkmcnt(2)\n\t" \
  "v_mfma_f32_16x16x32_bf16 v[80:83], a[144:147], v[48:51], v[80:83]\n\t" \
  "ds_read_b128 v[40:43], v39 offset:6144\n\t" \
  "v_mfma_f32_16x16x32_bf16 v[84:87], a[144:147], v[52:55], v[84:87]\n\t" \
  "ds_read_b128 v[44:47], v39 offset:22528\n\t" \
  "v_mfma_f32_16x16x32_bf16 v[88:91], a[208:211], v[48:51], v[88:91]\n\t" \
  "v_mfma_f32_16x16x32_bf16 v[92:95], a[208:211], v[52:55], v[92:95]\n\t" \
  "v_mfma_f32_16x16x32_bf16 v[96:99], v[48:51], v[144:147], v[96:99]\n\t" \
  "v_mfma_f32_16x16x32_bf16 v[100:103], v[52:55], v[144:147], v[100:103]\n\t" \
  "v_mfma_f32_16x16x32_bf16 v[104:107], v[48:51], v[208:211], v[104:107]\n\t" \
  "v_mfma_f32_16x16x32_bf16 v[108:111], v[52:55], v[208:211], v[108:111]\n\t" \
  "s_waitcnt lgkmcnt(2)\n\t" \
  "v_mfma_f32_16x16x32_bf16 v[80:83], a[148:151], v[56:59], v[80:83]\n\t" \
  "ds_read_b128 v[48:51], v39 offset:7168\n\t" \
  "v_mfma_f32_16x16x32_bf16 v[84:87], a[148:151], v[60:63], v[84:87]\n\t" \
  "ds_read_b128 v[52:55], v39 offset:23552\n\t" \
  "v_mfma_f32_16x16x32_bf16 v[88:91], a[212:215], v[56:59], v[88:91]\n\t" \
  "v_mfma_f32_16x16x32_bf16 v[92:95], a[212:215], v[60:63], v[92:95]\n\t" \
  "v_mfma_f32_16x16x32_bf16 v[96:99], v[56:59], v[148:151], v[96:99]\n\t" \
  "v_mfma_f32_16x16x32_bf16 v[100:103], v[60:63], v[148:151], v[100:103]\n\t" \
  "v_mfma_f32_16x16x32_bf16 v[104:107], v[56:59], v[212:215], v[104:107]\n\t" \
  "v_mfma_f32_16x16x32_bf16 v[108:111], v[60:63], v[212:215], v[108:111]\n\t" \
  "s_waitcnt lgkmcnt(2)\n\t" \
  "v_mfma_f32_16x16x32_bf16 v[80:83], a[152:155], v[40:43], v[80:83]\n\t" \
  "ds_read_b128 v[56:59], v39 offset:8192\n\t" \
  "v_mfma_f32_16x16x32_bf16 v[84:87], a[152:155], v[44:47], v[84:87]\n\t" \
  "ds_read_b128 v[60:63], v39 offset:24576\n\t" \
  "v_mfma_f32_16x16x32_bf16 v[88:91], a[216:219], v[40:43], v[88:91]\n\t" \
  "v_mfma_f32_16x16x32_bf16 v[92:95], a[216:219], v[44:47], v[92:95]\n\t" \
  "v_mfma_f32_16x16x32_bf16 v[96:99], v[40:43], v[152:155], v[96:99]\n\t" \
  "v_mfma_f32_16x16x32_bf16 v[100:103], v[44:47], v[152:155], v[100:103]\n\t" \
  "v_mfma_f32_16x16x32_bf16 v[104:107], v[40:43], v[216:219], v[104:107]\n\t" \
  "v_mfma_f32_16x16x32_bf16 v[108:111], v[44:47], v[216:219], v[108:111]\n\t" \
  "s_waitcnt lgkmcnt(2)\n\t" \
  "v_mfma_f32_16x16x32_bf16 v[80:83], a[156:159], v[48:51], v[80:83]\n\t" \
  "ds_read_b128 v[40:43], v39 offset:9216\n\t" \
  "v_mfma_f32_16x16x32_bf16 v[84:87], a[156:159], v[52:55], v[84:87]\n\t" \
  "ds_read_b128 v[44:47], v39 offset:25600\n\t" \
  "v_mfma_f32_16x16x32_bf16 v[88:91], a[220:223], v[48:51], v[88:91]\n\t" \
  "v_mfma_f32_16x16x32_bf16 v[92:95], a[220:223], v[52:55], v[92:95]\n\t" \
  "v_mfma_f32_16x16x32_bf16 v[96:99], v[48:51], v[156:159], v[96:99]\n\t" \
  "v_mfma_f32_16x16x32_bf16 v[100:103], v[52:55], v[156:159], v[100:103]\n\t" \
  "v_mfma_f32_16x16x32_bf16 v[104:107], v[48:51], v[220:223], v[104:107]\n\t" \
  "v_mfma_f32_16x16x32_bf16 v[108:111], v[52:55], v[220:223], v[108:111]\n\t" \
  "s_waitcnt lgkmcnt(2)\n\t" \
  "v_mfma_f32_16x16x32_bf16 v[80:83], a[160:163], v[56:59], v[80:83]\n\t" \
  "ds_read_b128 v[48:51], v39 offset:10240\n\t" \
  "v_mfma_f32_16x16x32_bf16 v[84:87], a[160:163], v[60:63], v[84:87]\n\t" \
  "ds_read_b128 v[52:55], v39 offset:26624\n\t" \
  "v_mfma_f32_16x16x32_bf16 v[88:91], a[224:227], v[56:59], v[88:91]\n\t" \
  "v_mfma_f32_16x16x32_bf16 v[92:95], a[224:227], v[60:63], v[92:95]\n\t" \
  "v_mfma_f32_16x16x32_bf16 v[96:99], v[56:59], v[160:163], v[96:99]\n\t" \
  "v_mfma_f32_16x16x32_bf16 v[100:103], v[60:63], v[160:163], v[100:103]\n\t" \
  "v_mfma_f32_16x16x32_bf16 v[104:107], v[56:59], v[224:227], v[104:107]\n\t" \
  "v_mfma_f32_16x16x32_bf16 v[108:111], v[60:63], v[224:227], v[108:111]\n\t" \
  "s_waitcnt lgkmcnt(2)\n\t" \
  "v_mfma_f32_16x16x32_bf16 v[80:83], a[164:167], v[40:43], v[80:83]\n\t" \
  "ds_read_b128 v[56:59], v39 offset:11264\n\t" \
  "v_mfma_f32_16x16x32_bf16 v[84:87], a[164:167], v[44:47], v[84:87]\n\t" \
  "ds_read_b128 v[60:63], v39 offset:27648\n\t" \
  "v_mfma_f32_16x16x32_bf16 v[88:91], a[228:231], v[40:43], v[88:91]\n\t" \
  "v_mfma_f32_16x16x32_bf16 v[92:95], a[228:231], v[44:47], v[92:95]\n\t" \
  "v_mfma_f32_16x16x32_bf16 v[96:99], v[40:43], v[164:167], v[96:99]\n\t" \
  "v_mfma_f32_16x16x32_bf16 v[100:103], v[44:47], v[164:167], v[100:103]\n\t" \
  "v_mfma_f32_16x16x32_bf16 v[104:107], v[40:43], v[228:231], v[104:107]\n\t" \
  "v_mfma_f32_16x16x32_bf16 v[108:111], v[44:47], v[228:231], v[108:111]\n\t" \
  "s_waitcnt lgkmcnt(2)\n\t" \
  "v_mfma_f32_16x16x32_bf16 v[80:83], a[168:171], v[48:51], v[80:83]\n\t" \
  "ds_read_b128 v[40:43], v39 offset:12288\n\t" \
  "v_mfma_f32_16x16x32_bf16 v[84:87], a[168:171], v[52:55], v[84:87]\n\t" \
  "ds_read_b128 v[44:47], v39 offset:28672\n\t" \
  "v_mfma_f32_16x16x32_bf16 v[88:91], a[232:235], v[48:51], v[88:91]\n\t" \
  "v_mfma_f32_16x16x32_bf16 v[92:95], a[232:235], v[52:55], v[92:95]\n\t" \
  "v_mfma_f32_16x16x32_bf16 v[96:99], v[48:51], v[168:171], v[96:99]\n\t" \
  "v_mfma_f32_16x16x32_bf16 v[100:103], v[52:55], v[168:171], v[100:103]\n\t" \
  "v_mfma_f32_16x16x32_bf16 v[104:107], v[48:51], v[232:235], v[104:107]\n\t" \
  "v_mfma_f32_16x16x32_bf16 v[108:111], v[52:55], v[232:235], v[108:111]\n\t" \
  "s_waitcnt lgkmcnt(2)\n\t" \
  "v_mfma_f32_16x16x32_bf16 v[80:83], a[172:175], v[56:59], v[80:83]\n\t" \
  "ds_read_b128 v[48:51], v39 offset:13312\n\t" \
  "v_mfma_f32_16x16x32_bf16 v[84:87], a[172:175], v[60:63], v[84:87]\n\t" \
  "ds_read_b128 v[52:55], v39 offset:29696\n\t" \
  "v_mfma_f32_16x16x32_bf16 v[88:91], a[236:239], v[56:59], v[88:91]\n\t" \
  "v_mfma_f32_16x16x32_bf16 v[92:95], a[236:239], v[60:63], v[92:95]\n\t" \
  "v_mfma_f32_16x16x32_bf16 v[96:99], v[56:59], v[172:175], v[96:99]\n\t" \
  "v_mfma_f32_16x16x32_bf16 v[100:103], v[60:63], v[172:175], v[100:103]\n\t" \
  "v_mfma_f32_16x16x32_bf16 v[104:107], v[56:59], v[236:239], v[104:107]\n\t" \
  "v_mfma_f32_16x16x32_bf16 v[108:111], v[60:63], v[236:239], v[108:111]\n\t" \
  "s_waitcnt lgkmcnt(2)\n\t" \
  "v_mfma_f32_16x16x32_bf16 v[80:83], a[176:179], v[40:43], v[80:83]\n\t" \
  "ds_read_b128 v[56:59], v39 offset:14336\n\t" \
  "v_mfma_f32_16x16x32_bf16 v[84:87], a[176:179], v[44:47], v[84:87]\n\t" \
  "ds_read_b128 v[60:63], v39 offset:30720\n\t" \
  "v_mfma_f32_16x16x32_bf16 v[88:91], a[240:243], v[40:43], v[88:91]\n\t" \
  "v_mfma_f32_16x16x32_bf16 v[92:95], a[240:243], v[44:47], v[92:95]\n\t" \
  "v_mfma_f32_16x16x32_bf16 v[96:99], v[40:43], v[176:179], v[96:99]\n\t" \
  "v_mfma_f32_16x16x32_bf16 v[100:103], v[44:47], v[176:179], v[100:103]\n\t" \
  "v_mfma_f32_16x16x32_bf16 v[104:107], v[40:43], v[240:243], v[104:107]\n\t" \
  "v_mfma_f32_16x16x32_bf16 v[108:111], v[44:47], v[240:243], v[108:111]\n\t" \
  "s_waitcnt lgkmcnt(2)\n\t" \
  "v_mfma_f32_16x16x32_bf16 v[80:83], a[180:183], v[48:51], v[80:83]\n\t" \
  "ds_read_b128 v[40:43], v39 offset:15360\n\t" \
  "v_mfma_f32_16x16x32_bf16 v[84:87], a[180:183], v[52:55], v[84:87]\n\t" \
  "ds_read_b128 v[44:47], v39 offset:31744\n\t" \
  "v_mfma_f32_16x16x32_bf16 v[88:91], a[244:247], v[48:51], v[88:91]\n\t" \
  "v_mfma_f32_16x16x32_bf16 v[92:95], a[244:247], v[52:55], v[92:95]\n\t" \
  "v_mfma_f32_16x16x32_bf16 v[96:99], v[48:51], v[180:183], v[96:99]\n\t" \
  "v_mfma_f32_16x16x32_bf16 v[100:103], v[52:55], v[180:183], v[100:103]\n\t" \
  "v_mfma_f32_16x16x32_bf16 v[104:107], v[48:51], v[244:247], v[104:107]\n\t" \
  "v_mfma_f32_16x16x32_bf16 v[108:111], v[52:55], v[244:247], v[108:111]\n\t" \
  "s_waitcnt lgkmcnt(2)\n\t" \
  "v_mfma_f32_16x16x32_bf16 v[80:83], a[184:187], v[56:59], v[80:83]\n\t" \
  "v_mfma_f32_16x16x32_bf16 v[84:87], a[184:187], v[60:63], v[84:87]\n\t" \
  "v_mfma_f32_16x16x32_bf16 v[88:91], a[248:251], v[56:59], v[88:91]\n\t" \
  "v_mfma_f32_16x16x32_bf16 v[92:95], a[248:251], v[60:63], v[92:95]\n\t" \
  "v_mfma_f32_16x16x32_bf16 v[96:99], v[56:59], v[184:187], v[96:99]\n\t" \
  "v_mfma_f32_16x16x32_bf16 v[100:103], v[60:63], v[184:187], v[100:103]\n\t" \
  "v_mfma_f32_16x16x32_bf16 v[104:107], v[56:59], v[248:251], v[104:107]\n\t" \
  "v_mfma_f32_16x16x32_bf16 v[108:111], v[60:63], v[248:251], v[108:111]\n\t" \
  "s_waitcnt lgkmcnt(0)\n\t" \
  "v_mfma_f32_16x16x32_bf16 v[80:83], a[188:191], v[40:43], v[80:83]\n\t" \
  "v_mfma_f32_16x16x32_bf16 v[84:87], a[188:191], v[44:47], v[84:87]\n\t" \
  "v_mfma_f32_16x16x32_bf16 v[88:91], a[252:255], v[40:43], v[88:91]\n\t" \
  "v_mfma_f32_16x16x32_bf16 v[92:95], a[252:255], v[44:47], v[92:95]\n\t" \
  "v_mfma_f32_16x16x32_bf16 v[96:99], v[40:43], v[188:191], v[96:99]\n\t" \
  "v_mfma_f32_16x16x32_bf16 v[100:103], v[44:47], v[188:191], v[100:103]\n\t" \
  "v_mfma_f32_16x16x32_bf16 v[104:107], v[40:43], v[252:255], v[104:107]\n\t" \
  "v_mfma_f32_16x16x32_bf16 v[108:111], v[44:47], v[252:255], v[108:111]\n\t" \
  "s_nop 7\n\t"
#define A_ASMQ \
  "ds_read_b128 v[40:43], v39 offset:0\n\t" \
  "ds_read_b128 v[44:47], v39 offset:16384\n\t" \
  "ds_read_b128 v[48:51], v39 offset:1024\n\t" \
  "ds_read_b128 v[52:55], v39 offset:17408\n\t" \
  "s_waitcnt lgkmcnt(2)\n\t" \
  "v_mfma_f32_16x16x32_bf16 v[64:67], a[0:3], v[40:43], 0\n\t" \
  "ds_read_b128 v[56:59], v39 offset:2048\n\t" \
  "v_mfma_f32_16x16x32_bf16 v[68:71], a[0:3], v[44:47], 0\n\t" \
  "ds_read_b128 v[60:63], v39 offset:18432\n\t" \
  "v_mfma_f32_16x16x32_bf16 v[72:75], a[64:67], v[40:43], 0\n\t" \
  "v_mfma_f32_16x16x32_bf16 v[76:79], a[64:67], v[44:47], 0\n\t" \
  "s_waitcnt lgkmcnt(2)\n\t" \
  "v_mfma_f32_16x16x32_bf16 v[64:67], a[4:7], v[48:51], v[64:67]\n\t" \
  "ds_read_b128 v[40:43], v39 offset:3072\n\t" \
  "v_mfma_f32_16x16x32_bf16 v[68:71], a[4:7], v[52:55], v[68:71]\n\t" \
  "ds_read_b128 v[44:47], v39 offset:19456\n\t" \
  "v_mfma_f32_16x16x32_bf16 v[72:75], a[68:71], v[48:51], v[72:75]\n\t" \
  "v_mfma_f32_16x16x32_bf16 v[76:79], a[68:71], v[52:55], v[76:79]\n\t" \
  "s_waitcnt lgkmcnt(2)\n\t" \
  "v_mfma_f32_16x16x32_bf16 v[64:67], a[8:11], v[56:59], v[64:67]\n\t" \
  "ds_read_b128 v[48:51], v39 offset:4096\n\t" \
  "v_mfma_f32_16x16x32_bf16 v[68:71], a[8:11], v[60:63], v[68:71]\n\t" \
  "ds_read_b128 v[52:55], v39 offset:20480\n\t" \
  "v_mfma_f32_16x16x32_bf16 v[72:75], a[72:75], v[56:59], v[72:75]\n\t" \
  "v_mfma_f32_16x16x32_bf16 v[76:79], a[72:75], v[60:63], v[76:79]\n\t" \
  "s_waitcnt lgkmcnt(2)\n\t" \
  "v_mfma_f32_16x16x32_bf16 v[64:67], a[12:15], v[40:43], v[64:67]\n\t" \
  "ds_read_b128 v[56:59], v39 offset:5120\n\t" \
  "v_mfma_f32_16x16x32_bf16 v[68:71], a[12:15], v[44:47], v[68:71]\n\t" \
  "ds_read_b128 v[60:63], v39 offset:21504\n\t" \
  "v_mfma_f32_16x16x32_bf16 v[72:75], a[76:79], v[40:43], v[72:75]\n\t" \
  "v_mfma_f32_16x16x32_bf16 v[76:79], a[76:79], v[44:47], v[76:79]\n\t" \
  "s_waitcnt lgkmcnt(2)\n\t" \
  "v_mfma_f32_16x16x32_bf16 v[64:67], a[16:19], v[48:51], v[64:67]\n\t" \
  "ds_read_b128 v[40:43], v39 offset:6144\n\t" \
  "v_mfma_f32_16x16x32_bf16 v[68:71], a[16:19], v[52:55], v[68:71]\n\t" \
  "ds_read_b128 v[44:47], v39 offset:22528\n\t" \
  "v_mfma_f32_16x16x32_bf16 v[72:75], a[80:83], v[48:51], v[72:75]\n\t" \
  "v_mfma_f32_16x16x32_bf16 v[76:79], a[80:83], v[52:55], v[76:79]\n\t" \
  "s_waitcnt lgkmcnt(2)\n\t" \
  "v_mfma_f32_16x16x32_bf16 v[64:67], a[20:23], v[56:59], v[64:67]\n\t" \
  "ds_read_b128 v[48:51], v39 offset:7168\n\t" \
  "v_mfma_f32_16x16x32_bf16 v[68:71], a[20:23], v[60:63], v[68:71]\n\t" \
  "ds_read_b128 v[52:55], v39 offset:23552\n\t" \
  "v_mfma_f32_16x16x32_bf16 v[72:75], a[84:87], v[56:59], v[72:75]\n\t" \
  "v_mfma_f32_16x16x32_bf16 v[76:79], a[84:87], v[60:63], v[76:79]\n\t" \
  "s_waitcnt lgkmcnt(2)\n\t" \
  "v_mfma_f32_16x16x32_bf16 v[64:67], a[24:27], v[40:43], v[64:67]\n\t" \
  "ds_read_b128 v[56:59], v39 offset:8192\n\t" \
  "v_mfma_f32_16x16x32_bf16 v[68:71], a[24:27], v[44:47], v[68:71]\n\t" \
  "ds_read_b128 v[60:63], v39 offset:24576\n\t" \
  "v_mfma_f32_16x16x32_bf16 v[72:75], a[88:91], v[40:43], v[72:75]\n\t" \
  "v_mfma_f32_16x16x32_bf16 v[76:79], a[88:91], v[44:47], v[76:79]\n\t" \
  "s_waitcnt lgkmcnt(2)\n\t" \
  "v_mfma_f32_16x16x32_bf16 v[64:67], a[28:31], v[48:51], v[64:67]\n\t" \
  "ds_read_b128 v[40:43], v39 offset:9216\n\t" \
  "v_mfma_f32_16x16x32_bf16 v[68:71], a[28:31], v[52:55], v[68:71]\n\t" \
  "ds_read_b128 v[44:47], v39 offset:25600\n\t" \
  "v_mfma_f32_16x16x32_bf16 v[72:75], a[92:95], v[48:51], v[72:75]\n\t" \
  "v_mfma_f32_16x16x32_bf16 v[76:79], a[92:95], v[52:55], v[76:79]\n\t" \
  "s_waitcnt lgkmcnt(2)\n\t" \
  "v_mfma_f32_16x16x32_bf16 v[64:67], a[32:35], v[56:59], v[64:67]\n\t" \
  "ds_read_b128 v[48:51], v39 offset:10240\n\t" \
  "v_mfma_f32_16x16x32_bf16 v[68:71], a[32:35], v[60:63], v[68:71]\n\t" \
  "ds_read_b128 v[52:55], v39 offset:26624\n\t" \
  "v_mfma_f32_16x16x32_bf16 v[72:75], a[96:99], v[56:59], v[72:75]\n\t" \
  "v_mfma_f32_16x16x32_bf16 v[76:79], a[96:99], v[60:63], v[76:79]\n\t" \
  "s_waitcnt lgkmcnt(2)\n\t" \
  "v_mfma_f32_16x16x32_bf16 v[64:67], a[36:39], v[40:43], v[64:67]\n\t" \
  "ds_read_b128 v[56:59], v39 offset:11264\n\t" \
  "v_mfma_f32_16x16x32_bf16 v[68:71], a[36:39], v[44:47], v[68:71]\n\t" \
  "ds_read_b128 v[60:63], v39 offset:27648\n\t" \
  "v_mfma_f32_16x16x32_bf16 v[72:75], a[100:103], v[40:43], v[72:75]\n\t" \
  "v_mfma_f32_16x16x32_bf16 v[76:79], a[100:103], v[44:47], v[76:79]\n\t" \
  "s_waitcnt lgkmcnt(2)\n\t" \
  "v_mfma_f32_16x16x32_bf16 v[64:67], a[40:43], v[48:51], v[64:67]\n\t" \
  "ds_read_b128 v[40:43], v39 offset:12288\n\t" \
  "v_mfma_f32_16x16x32_bf16 v[68:71], a[40:43], v[52:55], v[68:71]\n\t" \
  "ds_read_b128 v[44:47], v39 offset:28672\n\t" \
  "v_mfma_f32_16x16x32_bf16 v[72:75], a[104:107], v[48:51], v[72:75]\n\t" \
  "v_mfma_f32_16x16x32_bf16 v[76:79], a[104:107], v[52:55], v[76:79]\n\t" \
  "s_waitcnt lgkmcnt(2)\n\t" \
  "v_mfma_f32_16x16x32_bf16 v[64:67], a[44:47], v[56:59], v[64:67]\n\t" \
  "ds_read_b128 v[48:51], v39 offset:13312\n\t" \
  "v_mfma_f32_16x16x32_bf16 v[68:71], a[44:47], v[60:63], v[68:71]\n\t" \
  "ds_read_b128 v[52:55], v39 offset:29696\n\t" \
  "v_mfma_f32_16x16x32_bf16 v[72:75], a[108:111], v[56:59], v[72:75]\n\t" \
  "v_mfma_f32_16x16x32_bf16 v[76:79], a[108:111], v[60:63], v[76:79]\n\t" \
  "s_waitcnt lgkmcnt(2)\n\t" \
  "v_mfma_f32_16x16x32_bf16 v[64:67], a[48:51], v[40:43], v[64:67]\n\t" \
  "ds_read_b128 v[56:59], v39 offset:14336\n\t" \
  "v_mfma_f32_16x16x32_bf16 v[68:71], a[48:51], v[44:47], v[68:71]\n\t" \
  "ds_read_b128 v[60:63], v39 offset:30720\n\t" \
  "v_mfma_f32_16x16x32_bf16 v[72:75], a[112:115], v[40:43], v[72:75]\n\t" \
  "v_mfma_f32_16x16x32_bf16 v[76:79], a[112:115], v[44:47], v[76:79]\n\t" \
  "s_waitcnt lgkmcnt(2)\n\t" \
  "v_mfma_f32_16x16x32_bf16 v[64:67], a[52:55], v[48:51], v[64:67]\n\t" \
  "ds_read_b128 v[40:43], v39 offset:15360\n\t" \
  "v_mfma_f32_16x16x32_bf16 v[68:71], a[52:55], v[52:55], v[68:71]\n\t" \
  "ds_read_b128 v[44:47], v39 offset:31744\n\t" \
  "v_mfma_f32_16x16x32_bf16 v[72:75], a[116:119], v[48:51], v[72:75]\n\t" \
  "v_mfma_f32_16x16x32_bf16 v[76:79], a[116:119], v[52:55], v[76:79]\n\t" \
  "s_waitcnt lgkmcnt(2)\n\t" \
  "v_mfma_f32_16x16x32_bf16 v[64:67], a[56:59], v[56:59], v[64:67]\n\t" \
  "v_mfma_f32_16x16x32_bf16 v[68:71], a[56:59], v[60:63], v[68:71]\n\t" \
  "v_mfma_f32_16x16x32_bf16 v[72:75], a[120:123], v[56:59], v[72:75]\n\t" \
  "v_mfma_f32_16x16x32_bf16 v[76:79], a[120:123], v[60:63], v[76:79]\n\t" \
  "s_waitcnt lgkmcnt(0)\n\t" \
  "v_mfma_f32_16x16x32_bf16 v[64:67], a[60:63], v[40:43], v[64:67]\n\t" \
  "v_mfma_f32_16x16x32_bf16 v[68:71], a[60:63], v[44:47], v[68:71]\n\t" \
  "v_mfma_f32_16x16x32_bf16 v[72:75], a[124:127], v[40:43], v[72:75]\n\t" \
  "v_mfma_f32_16x16x32_bf16 v[76:79], a[124:127], v[44:47], v[76:79]\n\t" \
  "s_nop 7\n\t"
#define DRY2 \
  "s_mov_b32 s73, m0\n\t" \
  "s_mov_b32 s0, 0\n\t" \
  "s_mov_b32 s3, 0\n\t" \
  "s_mov_b32 s4, 0\n\t" \
  "s_mov_b32 s7, 0\n\t" \
  "s_mov_b32 s23, 0\n\t" \
  "s_mov_b32 s24, 0\n\t" \
  "s_mov_b32 s25, 0\n\t" \
  "s_mov_b32 s28, 0\n\t" \
  "s_mov_b32 s30, 0\n\t" \
  "s_mov_b32 s31, 0\n\t" \
  "s_mov_b32 s37, 0\n\t" \
  "s_mov_b32 s45, 0\n\t" \
  "s_mov_b32 s46, 0\n\t" \
  "s_mov_b32 s47, 0\n\t" \
  "s_mov_b32 s48, 0\n\t" \
  "s_mov_b32 s51, 0\n\t" \
  "s_mov_b32 s52, 0\n\t" \
  "s_mov_b32 s53, 0\n\t" \
  "s_mov_b32 s54, 0\n\t" \
  "s_mov_b32 s55, 0\n\t" \
  "s_mov_b32 s64, 0\n\t" \
  "s_mov_b32 s65, 0\n\t" \
  "s_mov_b32 s70, 0\n\t" \
  "s_mov_b32 s94, 0\n\t" \
  "s_mov_b32 s95, 0\n\t" \
  "s_mov_b32 s96, 0\n\t" \
  "s_mov_b32 s97, 0\n\t" \
  "s_mov_b32 s44, 1\n\t" \
  "s_mov_b32 s98, 0\n\t" \
  "s_mov_b32 s58, 1000\n\t" \
  "s_mov_b64 exec, 0\n\t" \
  "s_getpc_b64 s[74:75]\n\t" \
  "s_add_u32 s74, s74, 12\n\t" \
  "s_addc_u32 s75, s75, 0\n\t" \
  "s_branch rmt_chain_dry2\n\t" \
  "s_mov_b64 exec, -1\n\t" \
  "s_mov_b32 m0, s73\n\t" \
  "s_nop 7\n\t"
#define CLOB_DRY2 "memory", "scc", "vcc", "s0", "s3", "s4", "s5", "s6", "s7", "s8", "s9", "s10", "s11", "s12", "s13", "s14", "s15", "s16", "s17", "s18", "s19", "s20", "s21", "s23", "s24", "s25", "s26", "s27", "s28", "s30", "s31", "s34", "s35", "s36", "s37", "s38", "s39", "s44", "s45", "s46", "s47", "s48", "s51", "s52", "s53", "s54", "s55", "s58", "s63", "s64", "s65", "s66", "s67", "s70", "s73", "s74", "s75", "s78", "s79", "s94", "s95", "s96", "s97", "s98", "s99", "v28", "v29", "v30", "v31"
#define DRY \
  "s_mov_b32 s25, m0\n\t" \
  "s_mov_b32 s49, 7\n\t" \
  "s_mov_b32 s44, 0\n\t" \
  "s_mov_b32 s45, 0\n\t" \
  "s_mov_b32 s51, 0\n\t" \
  "s_mov_b32 s70, 0\n\t" \
  "s_mov_b64 exec, 0\n\t" \
  "s_getpc_b64 s[76:77]\n\t" \
  "s_add_u32 s76, s76, 12\n\t" \
  "s_addc_u32 s77, s77, 0\n\t" \
  "s_branch rmt_chain_dry\n\t" \
  "s_mov_b64 exec, -1\n\t" \
  "s_mov_b32 m0, s25\n\t" \
  "s_nop 7\n\t"
#define CLOB_DRY "memory", "scc", "vcc", "s25", "s44", "s45", "s49", "s51", "s70", "s76", "s77", "v56", "v57", "v58", "v59", "v60", "v61", "v62", "v63", "v64", "v65", "v66", "v67", "v68", "v69", "v70", "v71", "v72", "v73", "v74", "v75", "v76", "v77", "v78", "v79", "v80", "v81", "v82", "v83", "v84", "v85", "v86", "v87", "v88", "v89", "v90", "v91", "v92", "v93", "v94", "v95", "v96", "v97", "v98", "v99", "v100", "v101", "v102", "v103", "v104", "v105", "v106", "v107", "v108", "v109", "v110", "v111", "v112", "v113", "v114", "v115", "v116", "v117", "v118", "v119", "v120", "v121", "v122", "v123", "v124", "v125", "v126", "v127"
#define CHAIN_LOAD \
  "s_mov_b32 s25, m0\n\t" \
  "v_add_u32 v24, 65536, v8\n\t" \
  "v_add_u32 v25, 65536, v9\n\t" \
  "v_add_u32 v26, 65536, v10\n\t" \
  "v_add_u32 v27, 65536, v11\n\t" \
  "v_add_u32 v28, 65536, v12\n\t" \
  "v_add_u32 v29, 65536, v13\n\t" \
  "v_add_u32 v30, 65536, v14\n\t" \
  "v_add_u32 v31, 65536, v15\n\t" \
  "v_add_u32 v32, 65536, v16\n\t" \
  "v_add_u32 v33, 65536, v17\n\t" \
  "v_add_u32 v34, 65536, v18\n\t" \
  "v_add_u32 v35, 65536, v19\n\t" \
  "v_add_u32 v36, 65536, v20\n\t" \
  "v_add_u32 v37, 65536, v21\n\t" \
  "v_add_u32 v38, 65536, v22\n\t" \
  "v_add_u32 v39, 65536, v23\n\t" \
  "s_cmp_lg_u32 s21, 0\n\t" \
  "s_cbranch_scc1 L_side1_%=\n\t" \
  "s_waitcnt vmcnt(16)\n\t" \
  "s_barrier\n\t" \
  "ds_read_b64_tr_b16 v[56:57], v8 offset:0\n\t" \
  "ds_read_b64_tr_b16 v[60:61], v8 offset:8192\n\t" \
  "ds_read_b64_tr_b16 a[96:97], v9 offset:0\n\t" \
  "ds_read_b64_tr_b16 a[100:101], v9 offset:8192\n\t" \
  "ds_read_b64_tr_b16 a[112:113], v10 offset:0\n\t" \
  "ds_read_b64_tr_b16 a[116:117], v10 offset:8192\n\t" \
  "ds_read_b64_tr_b16 a[128:129], v11 offset:0\n\t" \
  "ds_read_b64_tr_b16 a[132:133], v11 offset:8192\n\t" \
  "ds_read_b64_tr_b16 v[58:59], v12 offset:0\n\t" \
  "ds_read_b64_tr_b16 v[62:63], v12 offset:8192\n\t" \
  "ds_read_b64_tr_b16 a[98:99], v13 offset:0\n\t" \
  "ds_read_b64_tr_b16 a[102:103], v13 offset:8192\n\t" \
  "ds_read_b64_tr_b16 a[114:115], v14 offset:0\n\t" \
  "ds_read_b64_tr_b16 a[118:119], v14 offset:8192\n\t" \
  "ds_read_b64_tr_b16 a[130:131], v15 offset:0\n\t" \
  "ds_read_b64_tr_b16 a[134:135], v15 offset:8192\n\t" \
  "ds_read_b64_tr_b16 v[64:65], v16 offset:0\n\t" \
  "ds_read_b64_tr_b16 v[68:69], v16 offset:8192\n\t" \
  "ds_read_b64_tr_b16 a[144:145], v17 offset:0\n\t" \
  "ds_read_b64_tr_b16 a[148:149], v17 offset:8192\n\t" \
  "ds_read_b64_tr_b16 a[160:161], v18 offset:0\n\t" \
  "ds_read_b64_tr_b16 a[164:165], v18 offset:8192\n\t" \
  "ds_read_b64_tr_b16 a[176:177], v19 offset:0\n\t" \
  "ds_read_b64_tr_b16 a[180:181], v19 offset:8192\n\t" \
  "ds_read_b64_tr_b16 v[66:67], v20 offset:0\n\t" \
  "ds_read_b64_tr_b16 v[70:71], v20 offset:8192\n\t" \
  "ds_read_b64_tr_b16 a[146:147], v21 offset:0\n\t" \
  "ds_read_b64_tr_b16 a[150:151], v21 offset:8192\n\t" \
  "ds_read_b64_tr_b16 a[162:163], v22 offset:0\n\t" \
  "ds_read_b64_tr_b16 a[166:167], v22 offset:8192\n\t" \
  "ds_read_b64_tr_b16 a[178:179], v23 offset:0\n\t" \
  "ds_read_b64_tr_b16 a[182:183], v23 offset:8192\n\t" \
  "s_waitcnt lgkmcnt(0)\n\t" \
  "s_barrier\n\t" \
  "s_lshl_b32 s22, s20, 19\n\t" \
  "s_add_u32 s22, s22, 0\n\t" \
  "s_lshl_b32 s23, s20, 14\n\t" \
  "s_add_u32 m0, s23, 32768\n\t" \
  "s_nop 0\n\t" \
  "buffer_load_dwordx4 v0, s[12:15], s22 offen lds\n\t" \
  "s_add_u32 m0, m0, 1024\n\t" \
  "s_nop 0\n\t" \
  "buffer_load_dwordx4 v1, s[12:15], s22 offen lds\n\t" \
  "s_add_u32 m0, m0, 1024\n\t" \
  "s_nop 0\n\t" \
  "buffer_load_dwordx4 v2, s[12:15], s22 offen lds\n\t" \
  "s_add_u32 m0, m0, 1024\n\t" \
  "s_nop 0\n\t" \
  "buffer_load_dwordx4 v3, s[12:15], s22 offen lds\n\t" \
  "s_add_u32 m0, m0, 1024\n\t" \
  "s_add_u32 s22, s22, 131072\n\t" \
  "buffer_load_dwordx4 v0, s[12:15], s22 offen lds\n\t" \
  "s_add_u32 m0, m0, 1024\n\t" \
  "s_nop 0\n\t" \
  "buffer_load_dwordx4 v1, s[12:15], s22 offen lds\n\t" \
  "s_add_u32 m0, m0, 1024\n\t" \
  "s_nop 0\n\t" \
  "buffer_load_dwordx4 v2, s[12:15], s22 offen lds\n\t" \
  "s_add_u32 m0, m0, 1024\n\t" \
  "s_nop 0\n\t" \
  "buffer_load_dwordx4 v3, s[12:15], s22 offen lds\n\t" \
  "s_add_u32 m0, m0, 1024\n\t" \
  "s_add_u32 s22, s22, 131072\n\t" \
  "buffer_load_dwordx4 v0, s[12:15], s22 offen lds\n\t" \
  "s_add_u32 m0, m0, 1024\n\t" \
  "s_nop 0\n\t" \
  "buffer_load_dwordx4 v1, s[12:15], s22 offen lds\n\t" \
  "s_add_u32 m0, m0, 1024\n\t" \
  "s_nop 0\n\t" \
  "buffer_load_dwordx4 v2, s[12:15], s22 offen lds\n\t" \
  "s_add_u32 m0, m0, 1024\n\t" \
  "s_nop 0\n\t" \
  "buffer_load_dwordx4 v3, s[12:15], s22 offen lds\n\t" \
  "s_add_u32 m0, m0, 1024\n\t" \
  "s_add_u32 s22, s22, 131072\n\t" \
  "buffer_load_dwordx4 v0, s[12:15], s22 offen lds\n\t" \
  "s_add_u32 m0, m0, 1024\n\t" \
  "s_nop 0\n\t" \
  "buffer_load_dwordx4 v1, s[12:15], s22 offen lds\n\t" \
  "s_add_u32 m0, m0, 1024\n\t" \
  "s_nop 0\n\t" \
  "buffer_load_dwordx4 v2, s[12:15], s22 offen lds\n\t" \
  "s_add_u32 m0, m0, 1024\n\t" \
  "s_nop 0\n\t" \
  "buffer_load_dwordx4 v3, s[12:15], s22 offen lds\n\t" \
  "s_waitcnt vmcnt(16)\n\t" \
  "s_barrier\n\t" \
  "ds_read_b64_tr_b16 v[88:89], v24 offset:0\n\t" \
  "ds_read_b64_tr_b16 v[92:93], v24 offset:8192\n\t" \
  "ds_read_b64_tr_b16 a[104:105], v25 offset:0\n\t" \
  "ds_read_b64_tr_b16 a[108:109], v25 offset:8192\n\t" \
  "ds_read_b64_tr_b16 a[120:121], v26 offset:0\n\t" \
  "ds_read_b64_tr_b16 a[124:125], v26 offset:8192\n\t" \
  "ds_read_b64_tr_b16 a[136:137], v27 offset:0\n\t" \
  "ds_read_b64_tr_b16 a[140:141], v27 offset:8192\n\t" \
  "ds_read_b64_tr_b16 v[90:91], v28 offset:0\n\t" \
  "ds_read_b64_tr_b16 v[94:95], v28 offset:8192\n\t" \
  "ds_read_b64_tr_b16 a[106:107], v29 offset:0\n\t" \
  "ds_read_b64_tr_b16 a[110:111], v29 offset:8192\n\t" \
  "ds_read_b64_tr_b16 a[122:123], v30 offset:0\n\t" \
  "ds_read_b64_tr_b16 a[126:127], v30 offset:8192\n\t" \
  "ds_read_b64_tr_b16 a[138:139], v31 offset:0\n\t" \
  "ds_read_b64_tr_b16 a[142:143], v31 offset:8192\n\t" \
  "ds_read_b64_tr_b16 v[96:97], v32 offset:0\n\t" \
  "ds_read_b64_tr_b16 v[100:101], v32 offset:8192\n\t" \
  "ds_read_b64_tr_b16 a[152:153], v33 offset:0\n\t" \
  "ds_read_b64_tr_b16 a[156:157], v33 offset:8192\n\t" \
  "ds_read_b64_tr_b16 a[168:169], v34 offset:0\n\t" \
  "ds_read_b64_tr_b16 a[172:173], v34 offset:8192\n\t" \
  "ds_read_b64_tr_b16 a[184:185], v35 offset:0\n\t" \
  "ds_read_b64_tr_b16 a[188:189], v35 offset:8192\n\t" \
  "ds_read_b64_tr_b16 v[98:99], v36 offset:0\n\t" \
  "ds_read_b64_tr_b16 v[102:103], v36 offset:8192\n\t" \
  "ds_read_b64_tr_b16 a[154:155], v37 offset:0\n\t" \
  "ds_read_b64_tr_b16 a[158:159], v37 offset:8192\n\t" \
  "ds_read_b64_tr_b16 a[170:171], v38 offset:0\n\t" \
  "ds_read_b64_tr_b16 a[174:175], v38 offset:8192\n\t" \
  "ds_read_b64_tr_b16 a[186:187], v39 offset:0\n\t" \
  "ds_read_b64_tr_b16 a[190:191], v39 offset:8192\n\t" \
  "s_waitcnt lgkmcnt(0)\n\t" \
  "s_barrier\n\t" \
  "s_branch L_join_%=\n\t" \
  "L_side1_%=:\n\t" \
  "s_waitcnt vmcnt(16)\n\t" \
  "s_barrier\n\t" \
  "ds_read_b64_tr_b16 v[88:89], v8 offset:0\n\t" \
  "ds_read_b64_tr_b16 v[92:93], v8 offset:8192\n\t" \
  "ds_read_b64_tr_b16 a[104:105], v9 offset:0\n\t" \
  "ds_read_b64_tr_b16 a[108:109], v9 offset:8192\n\t" \
  "ds_read_b64_tr_b16 a[120:121], v10 offset:0\n\t" \
  "ds_read_b64_tr_b16 a[124:125], v10 offset:8192\n\t" \
  "ds_read_b64_tr_b16 a[136:137], v11 offset:0\n\t" \
  "ds_read_b64_tr_b16 a[140:141], v11 offset:8192\n\t" \
  "ds_read_b64_tr_b16 v[90:91], v12 offset:0\n\t" \
  "ds_read_b64_tr_b16 v[94:95], v12 offset:8192\n\t" \
  "ds_read_b64_tr_b16 a[106:107], v13 offset:0\n\t" \
  "ds_read_b64_tr_b16 a[110:111], v13 offset:8192\n\t" \
  "ds_read_b64_tr_b16 a[122:123], v14 offset:0\n\t" \
  "ds_read_b64_tr_b16 a[126:127], v14 offset:8192\n\t" \
  "ds_read_b64_tr_b16 a[138:139], v15 offset:0\n\t" \
  "ds_read_b64_tr_b16 a[142:143], v15 offset:8192\n\t" \
  "ds_read_b64_tr_b16 v[96:97], v16 offset:0\n\t" \
  "ds_read_b64_tr_b16 v[100:101], v16 offset:8192\n\t" \
  "ds_read_b64_tr_b16 a[152:153], v17 offset:0\n\t" \
  "ds_read_b64_tr_b16 a[156:157], v17 offset:8192\n\t" \
  "ds_read_b64_tr_b16 a[168:169], v18 offset:0\n\t" \
  "ds_read_b64_tr_b16 a[172:173], v18 offset:8192\n\t" \
  "ds_read_b64_tr_b16 a[184:185], v19 offset:0\n\t" \
  "ds_read_b64_tr_b16 a[188:189], v19 offset:8192\n\t" \
  "ds_read_b64_tr_b16 v[98:99], v20 offset:0\n\t" \
  "ds_read_b64_tr_b16 v[102:103], v20 offset:8192\n\t" \
  "ds_read_b64_tr_b16 a[154:155], v21 offset:0\n\t" \
  "ds_read_b64_tr_b16 a[158:159], v21 offset:8192\n\t" \
  "ds_read_b64_tr_b16 a[170:171], v22 offset:0\n\t" \
  "ds_read_b64_tr_b16 a[174:175], v22 offset:8192\n\t" \
  "ds_read_b64_tr_b16 a[186:187], v23 offset:0\n\t" \
  "ds_read_b64_tr_b16 a[190:191], v23 offset:8192\n\t" \
  "s_waitcnt lgkmcnt(0)\n\t" \
  "s_barrier\n\t" \
  "s_lshl_b32 s22, s20, 19\n\t" \
  "s_add_u32 s22, s22, 0\n\t" \
  "s_lshl_b32 s23, s20, 14\n\t" \
  "s_add_u32 m0, s23, 32768\n\t" \
  "s_nop 0\n\t" \
  "buffer_load_dwordx4 v0, s[12:15], s22 offen lds\n\t" \
  "s_add_u32 m0, m0, 1024\n\t" \
  "s_nop 0\n\t" \
  "buffer_load_dwordx4 v1, s[12:15], s22 offen lds\n\t" \
  "s_add_u32 m0, m0, 1024\n\t" \
  "s_nop 0\n\t" \
  "buffer_load_dwordx4 v2, s[12:15], s22 offen lds\n\t" \
  "s_add_u32 m0, m0, 1024\n\t" \
  "s_nop 0\n\t" \
  "buffer_load_dwordx4 v3, s[12:15], s22 offen lds\n\t" \
  "s_add_u32 m0, m0, 1024\n\t" \
  "s_add_u32 s22, s22, 131072\n\t" \
  "buffer_load_dwordx4 v0, s[12:15], s22 offen lds\n\t" \
  "s_add_u32 m0, m0, 1024\n\t" \
  "s_nop 0\n\t" \
  "buffer_load_dwordx4 v1, s[12:15], s22 offen lds\n\t" \
  "s_add_u32 m0, m0, 1024\n\t" \
  "s_nop 0\n\t" \
  "buffer_load_dwordx4 v2, s[12:15], s22 offen lds\n\t" \
  "s_add_u32 m0, m0, 1024\n\t" \
  "s_nop 0\n\t" \
  "buffer_load_dwordx4 v3, s[12:15], s22 offen lds\n\t" \
  "s_add_u32 m0, m0, 1024\n\t" \
  "s_add_u32 s22, s22, 131072\n\t" \
  "buffer_load_dwordx4 v0, s[12:15], s22 offen lds\n\t" \
  "s_add_u32 m0, m0, 1024\n\t" \
  "s_nop 0\n\t" \
  "buffer_load_dwordx4 v1, s[12:15], s22 offen lds\n\t" \
  "s_add_u32 m0, m0, 1024\n\t" \
  "s_nop 0\n\t" \
  "buffer_load_dwordx4 v2, s[12:15], s22 offen lds\n\t" \
  "s_add_u32 m0, m0, 1024\n\t" \
  "s_nop 0\n\t" \
  "buffer_load_dwordx4 v3, s[12:15], s22 offen lds\n\t" \
  "s_add_u32 m0, m0, 1024\n\t" \
  "s_add_u32 s22, s22, 131072\n\t" \
  "buffer_load_dwordx4 v0, s[12:15], s22 offen lds\n\t" \
  "s_add_u32 m0, m0, 1024\n\t" \
  "s_nop 0\n\t" \
  "buffer_load_dwordx4 v1, s[12:15], s22 offen lds\n\t" \
  "s_add_u32 m0, m0, 1024\n\t" \
  "s_nop 0\n\t" \
  "buffer_load_dwordx4 v2, s[12:15], s22 offen lds\n\t" \
  "s_add_u32 m0, m0, 1024\n\t" \
  "s_nop 0\n\t" \
  "buffer_load_dwordx4 v3, s[12:15], s22 offen lds\n\t" \
  "s_waitcnt vmcnt(16)\n\t" \
  "s_barrier\n\t" \
  "ds_read_b64_tr_b16 v[56:57], v24 offset:0\n\t" \
  "ds_read_b64_tr_b16 v[60:61], v24 offset:8192\n\t" \
  "ds_read_b64_tr_b16 a[96:97], v25 offset:0\n\t" \
  "ds_read_b64_tr_b16 a[100:101], v25 offset:8192\n\t" \
  "ds_read_b64_tr_b16 a[112:113], v26 offset:0\n\t" \
  "ds_read_b64_tr_b16 a[116:117], v26 offset:8192\n\t" \
  "ds_read_b64_tr_b16 a[128:129], v27 offset:0\n\t" \
  "ds_read_b64_tr_b16 a[132:133], v27 offset:8192\n\t" \
  "ds_read_b64_tr_b16 v[58:59], v28 offset:0\n\t" \
  "ds_read_b64_tr_b16 v[62:63], v28 offset:8192\n\t" \
  "ds_read_b64_tr_b16 a[98:99], v29 offset:0\n\t" \
  "ds_read_b64_tr_b16 a[102:103], v29 offset:8192\n\t" \
  "ds_read_b64_tr_b16 a[114:115], v30 offset:0\n\t" \
  "ds_read_b64_tr_b16 a[118:119], v30 offset:8192\n\t" \
  "ds_read_b64_tr_b16 a[130:131], v31 offset:0\n\t" \
  "ds_read_b64_tr_b16 a[134:135], v31 offset:8192\n\t" \
  "ds_read_b64_tr_b16 v[64:65], v32 offset:0\n\t" \
  "ds_read_b64_tr_b16 v[68:69], v32 offset:8192\n\t" \
  "ds_read_b64_tr_b16 a[144:145], v33 offset:0\n\t" \
  "ds_read_b64_tr_b16 a[148:149], v33 offset:8192\n\t" \
  "ds_read_b64_tr_b16 a[160:161], v34 offset:0\n\t" \
  "ds_read_b64_tr_b16 a[164:165], v34 offset:8192\n\t" \
  "ds_read_b64_tr_b16 a[176:177], v35 offset:0\n\t" \
  "ds_read_b64_tr_b16 a[180:181], v35 offset:8192\n\t" \
  "ds_read_b64_tr_b16 v[66:67], v36 offset:0\n\t" \
  "ds_read_b64_tr_b16 v[70:71], v36 offset:8192\n\t" \
  "ds_read_b64_tr_b16 a[146:147], v37 offset:0\n\t" \
  "ds_read_b64_tr_b16 a[150:151], v37 offset:8192\n\t" \
  "ds_read_b64_tr_b16 a[162:163], v38 offset:0\n\t" \
  "ds_read_b64_tr_b16 a[166:167], v38 offset:8192\n\t" \
  "ds_read_b64_tr_b16 a[178:179], v39 offset:0\n\t" \
  "ds_read_b64_tr_b16 a[182:183], v39 offset:8192\n\t" \
  "s_waitcnt lgkmcnt(0)\n\t" \
  "s_barrier\n\t" \
  "L_join_%=:\n\t" \
  "s_lshl_b32 s22, s20, 19\n\t" \
  "s_add_u32 s22, s22, 2097152\n\t" \
  "s_lshl_b32 s23, s20, 14\n\t" \
  "s_add_u32 m0, s23, 98304\n\t" \
  "s_nop 0\n\t" \
  "buffer_load_dwordx4 v0, s[12:15], s22 offen lds\n\t" \
  "s_add_u32 m0, m0, 1024\n\t" \
  "s_nop 0\n\t" \
  "buffer_load_dwordx4 v1, s[12:15], s22 offen lds\n\t" \
  "s_add_u32 m0, m0, 1024\n\t" \
  "s_nop 0\n\t" \
  "buffer_load_dwordx4 v2, s[12:15], s22 offen lds\n\t" \
  "s_add_u32 m0, m0, 1024\n\t" \
  "s_nop 0\n\t" \
  "buffer_load_dwordx4 v3, s[12:15], s22 offen lds\n\t" \
  "s_add_u32 m0, m0, 1024\n\t" \
  "s_add_u32 s22, s22, 131072\n\t" \
  "buffer_load_dwordx4 v0, s[12:15], s22 offen lds\n\t" \
  "s_add_u32 m0, m0, 1024\n\t" \
  "s_nop 0\n\t" \
  "buffer_load_dwordx4 v1, s[12:15], s22 offen lds\n\t" \
  "s_add_u32 m0, m0, 1024\n\t" \
  "s_nop 0\n\t" \
  "buffer_load_dwordx4 v2, s[12:15], s22 offen lds\n\t" \
  "s_add_u32 m0, m0, 1024\n\t" \
  "s_nop 0\n\t" \
  "buffer_load_dwordx4 v3, s[12:15], s22 offen lds\n\t" \
  "s_add_u32 m0, m0, 1024\n\t" \
  "s_add_u32 s22, s22, 131072\n\t" \
  "buffer_load_dwordx4 v0, s[12:15], s22 offen lds\n\t" \
  "s_add_u32 m0, m0, 1024\n\t" \
  "s_nop 0\n\t" \
  "buffer_load_dwordx4 v1, s[12:15], s22 offen lds\n\t" \
  "s_add_u32 m0, m0, 1024\n\t" \
  "s_nop 0\n\t" \
  "buffer_load_dwordx4 v2, s[12:15], s22 offen lds\n\t" \
  "s_add_u32 m0, m0, 1024\n\t" \
  "s_nop 0\n\t" \
  "buffer_load_dwordx4 v3, s[12:15], s22 offen lds\n\t" \
  "s_add_u32 m0, m0, 1024\n\t" \
  "s_add_u32 s22, s22, 131072\n\t" \
  "buffer_load_dwordx4 v0, s[12:15], s22 offen lds\n\t" \
  "s_add_u32 m0, m0, 1024\n\t" \
  "s_nop 0\n\t" \
  "buffer_load_dwordx4 v1, s[12:15], s22 offen lds\n\t" \
  "s_add_u32 m0, m0, 1024\n\t" \
  "s_nop 0\n\t" \
  "buffer_load_dwordx4 v2, s[12:15], s22 offen lds\n\t" \
  "s_add_u32 m0, m0, 1024\n\t" \
  "s_nop 0\n\t" \
  "buffer_load_dwordx4 v3, s[12:15], s22 offen lds\n\t" \
  "s_waitcnt vmcnt(16)\n\t" \
  "s_barrier\n\t" \
  "ds_read_b64_tr_b16 v[128:129], v8 offset:0\n\t" \
  "ds_read_b64_tr_b16 v[132:133], v8 offset:8192\n\t" \
  "ds_read_b64_tr_b16 v[144:145], v9 offset:0\n\t" \
  "ds_read_b64_tr_b16 v[148:149], v9 offset:8192\n\t" \
  "ds_read_b64_tr_b16 v[160:161], v10 offset:0\n\t" \
  "ds_read_b64_tr_b16 v[164:165], v10 offset:8192\n\t" \
  "ds_read_b64_tr_b16 v[176:177], v11 offset:0\n\t" \
  "ds_read_b64_tr_b16 v[180:181], v11 offset:8192\n\t" \
  "ds_read_b64_tr_b16 v[130:131], v12 offset:0\n\t" \
  "ds_read_b64_tr_b16 v[134:135], v12 offset:8192\n\t" \
  "ds_read_b64_tr_b16 v[146:147], v13 offset:0\n\t" \
  "ds_read_b64_tr_b16 v[150:151], v13 offset:8192\n\t" \
  "ds_read_b64_tr_b16 v[162:163], v14 offset:0\n\t" \
  "ds_read_b64_tr_b16 v[166:167], v14 offset:8192\n\t" \
  "ds_read_b64_tr_b16 v[178:179], v15 offset:0\n\t" \
  "ds_read_b64_tr_b16 v[182:183], v15 offset:8192\n\t" \
  "ds_read_b64_tr_b16 v[192:193], v16 offset:0\n\t" \
  "ds_read_b64_tr_b16 v[196:197], v16 offset:8192\n\t" \
  "ds_read_b64_tr_b16 v[208:209], v17 offset:0\n\t" \
  "ds_read_b64_tr_b16 v[212:213], v17 offset:8192\n\t" \
  "ds_read_b64_tr_b16 v[224:225], v18 offset:0\n\t" \
  "ds_read_b64_tr_b16 v[228:229], v18 offset:8192\n\t" \
  "ds_read_b64_tr_b16 v[240:241], v19 offset:0\n\t" \
  "ds_read_b64_tr_b16 v[244:245], v19 offset:8192\n\t" \
  "ds_read_b64_tr_b16 v[194:195], v20 offset:0\n\t" \
  "ds_read_b64_tr_b16 v[198:199], v20 offset:8192\n\t" \
  "ds_read_b64_tr_b16 v[210:211], v21 offset:0\n\t" \
  "ds_read_b64_tr_b16 v[214:215], v21 offset:8192\n\t" \
  "ds_read_b64_tr_b16 v[226:227], v22 offset:0\n\t" \
  "ds_read_b64_tr_b16 v[230:231], v22 offset:8192\n\t" \
  "ds_read_b64_tr_b16 v[242:243], v23 offset:0\n\t" \
  "ds_read_b64_tr_b16 v[246:247], v23 offset:8192\n\t" \
  "s_waitcnt lgkmcnt(0)\n\t" \
  "s_barrier\n\t" \
  "s_lshl_b32 s22, s20, 19\n\t" \
  "s_add_u32 s22, s22, 0\n\t" \
  "s_lshl_b32 s23, s20, 14\n\t" \
  "s_add_u32 m0, s23, 32768\n\t" \
  "s_nop 0\n\t" \
  "buffer_load_dwordx4 v0, s[4:7], s22 offen lds\n\t" \
  "s_add_u32 m0, m0, 1024\n\t" \
  "s_nop 0\n\t" \
  "buffer_load_dwordx4 v1, s[4:7], s22 offen lds\n\t" \
  "s_add_u32 m0, m0, 1024\n\t" \
  "s_nop 0\n\t" \
  "buffer_load_dwordx4 v2, s[4:7], s22 offen lds\n\t" \
  "s_add_u32 m0, m0, 1024\n\t" \
  "s_nop 0\n\t" \
  "buffer_load_dwordx4 v3, s[4:7], s22 offen lds\n\t" \
  "s_add_u32 m0, m0, 1024\n\t" \
  "s_add_u32 s22, s22, 131072\n\t" \
  "buffer_load_dwordx4 v0, s[4:7], s22 offen lds\n\t" \
  "s_add_u32 m0, m0, 1024\n\t" \
  "s_nop 0\n\t" \
  "buffer_load_dwordx4 v1, s[4:7], s22 offen lds\n\t" \
  "s_add_u32 m0, m0, 1024\n\t" \
  "s_nop 0\n\t" \
  "buffer_load_dwordx4 v2, s[4:7], s22 offen lds\n\t" \
  "s_add_u32 m0, m0, 1024\n\t" \
  "s_nop 0\n\t" \
  "buffer_load_dwordx4 v3, s[4:7], s22 offen lds\n\t" \
  "s_add_u32 m0, m0, 1024\n\t" \
  "s_add_u32 s22, s22, 131072\n\t" \
  "buffer_load_dwordx4 v0, s[4:7], s22 offen lds\n\t" \
  "s_add_u32 m0, m0, 1024\n\t" \
  "s_nop 0\n\t" \
  "buffer_load_dwordx4 v1, s[4:7], s22 offen lds\n\t" \
  "s_add_u32 m0, m0, 1024\n\t" \
  "s_nop 0\n\t" \
  "buffer_load_dwordx4 v2, s[4:7], s22 offen lds\n\t" \
  "s_add_u32 m0, m0, 1024\n\t" \
  "s_nop 0\n\t" \
  "buffer_load_dwordx4 v3, s[4:7], s22 offen lds\n\t" \
  "s_add_u32 m0, m0, 1024\n\t" \
  "s_add_u32 s22, s22, 131072\n\t" \
  "buffer_load_dwordx4 v0, s[4:7], s22 offen lds\n\t" \
  "s_add_u32 m0, m0, 1024\n\t" \
  "s_nop 0\n\t" \
  "buffer_load_dwordx4 v1, s[4:7], s22 offen lds\n\t" \
  "s_add_u32 m0, m0, 1024\n\t" \
  "s_nop 0\n\t" \
  "buffer_load_dwordx4 v2, s[4:7], s22 offen lds\n\t" \
  "s_add_u32 m0, m0, 1024\n\t" \
  "s_nop 0\n\t" \
  "buffer_load_dwordx4 v3, s[4:7], s22 offen lds\n\t" \
  "s_waitcnt vmcnt(16)\n\t" \
  "s_barrier\n\t" \
  "ds_read_b64_tr_b16 v[136:137], v24 offset:0\n\t" \
  "ds_read_b64_tr_b16 v[140:141], v24 offset:8192\n\t" \
  "ds_read_b64_tr_b16 v[152:153], v25 offset:0\n\t" \
  "ds_read_b64_tr_b16 v[156:157], v25 offset:8192\n\t" \
  "ds_read_b64_tr_b16 v[168:169], v26 offset:0\n\t" \
  "ds_read_b64_tr_b16 v[172:173], v26 offset:8192\n\t" \
  "ds_read_b64_tr_b16 v[184:185], v27 offset:0\n\t" \
  "ds_read_b64_tr_b16 v[188:189], v27 offset:8192\n\t" \
  "ds_read_b64_tr_b16 v[138:139], v28 offset:0\n\t" \
  "ds_read_b64_tr_b16 v[142:143], v28 offset:8192\n\t" \
  "ds_read_b64_tr_b16 v[154:155], v29 offset:0\n\t" \
  "ds_read_b64_tr_b16 v[158:159], v29 offset:8192\n\t" \
  "ds_read_b64_tr_b16 v[170:171], v30 offset:0\n\t" \
  "ds_read_b64_tr_b16 v[174:175], v30 offset:8192\n\t" \
  "ds_read_b64_tr_b16 v[186:187], v31 offset:0\n\t" \
  "ds_read_b64_tr_b16 v[190:191], v31 offset:8192\n\t" \
  "ds_read_b64_tr_b16 v[200:201], v32 offset:0\n\t" \
  "ds_read_b64_tr_b16 v[204:205], v32 offset:8192\n\t" \
  "ds_read_b64_tr_b16 v[216:217], v33 offset:0\n\t" \
  "ds_read_b64_tr_b16 v[220:221], v33 offset:8192\n\t" \
  "ds_read_b64_tr_b16 v[232:233], v34 offset:0\n\t" \
  "ds_read_b64_tr_b16 v[236:237], v34 offset:8192\n\t" \
  "ds_read_b64_tr_b16 v[248:249], v35 offset:0\n\t" \
  "ds_read_b64_tr_b16 v[252:253], v35 offset:8192\n\t" \
  "ds_read_b64_tr_b16 v[202:203], v36 offset:0\n\t" \
  "ds_read_b64_tr_b16 v[206:207], v36 offset:8192\n\t" \
  "ds_read_b64_tr_b16 v[218:219], v37 offset:0\n\t" \
  "ds_read_b64_tr_b16 v[222:223], v37 offset:8192\n\t" \
  "ds_read_b64_tr_b16 v[234:235], v38 offset:0\n\t" \
  "ds_read_b64_tr_b16 v[238:239], v38 offset:8192\n\t" \
  "ds_read_b64_tr_b16 v[250:251], v39 offset:0\n\t" \
  "ds_read_b64_tr_b16 v[254:255], v39 offset:8192\n\t" \
  "s_waitcnt lgkmcnt(0)\n\t" \
  "s_barrier\n\t" \
  "s_lshl_b32 s22, s20, 19\n\t" \
  "s_add_u32 s22, s22, 2097152\n\t" \
  "s_lshl_b32 s23, s20, 14\n\t" \
  "s_add_u32 m0, s23, 98304\n\t" \
  "s_nop 0\n\t" \
  "buffer_load_dwordx4 v0, s[4:7], s22 offen lds\n\t" \
  "s_add_u32 m0, m0, 1024\n\t" \
  "s_nop 0\n\t" \
  "buffer_load_dwordx4 v1, s[4:7], s22 offen lds\n\t" \
  "s_add_u32 m0, m0, 1024\n\t" \
  "s_nop 0\n\t" \
  "buffer_load_dwordx4 v2, s[4:7], s22 offen lds\n\t" \
  "s_add_u32 m0, m0, 1024\n\t" \
  "s_nop 0\n\t" \
  "buffer_load_dwordx4 v3, s[4:7], s22 offen lds\n\t" \
  "s_add_u32 m0, m0, 1024\n\t" \
  "s_add_u32 s22, s22, 131072\n\t" \
  "buffer_load_dwordx4 v0, s[4:7], s22 offen lds\n\t" \
  "s_add_u32 m0, m0, 1024\n\t" \
  "s_nop 0\n\t" \
  "buffer_load_dwordx4 v1, s[4:7], s22 offen lds\n\t" \
  "s_add_u32 m0, m0, 1024\n\t" \
  "s_nop 0\n\t" \
  "buffer_load_dwordx4 v2, s[4:7], s22 offen lds\n\t" \
  "s_add_u32 m0, m0, 1024\n\t" \
  "s_nop 0\n\t" \
  "buffer_load_dwordx4 v3, s[4:7], s22 offen lds\n\t" \
  "s_add_u32 m0, m0, 1024\n\t" \
  "s_add_u32 s22, s22, 131072\n\t" \
  "buffer_load_dwordx4 v0, s[4:7], s22 offen lds\n\t" \
  "s_add_u32 m0, m0, 1024\n\t" \
  "s_nop 0\n\t" \
  "buffer_load_dwordx4 v1, s[4:7], s22 offen lds\n\t" \
  "s_add_u32 m0, m0, 1024\n\t" \
  "s_nop 0\n\t" \
  "buffer_load_dwordx4 v2, s[4:7], s22 offen lds\n\t" \
  "s_add_u32 m0, m0, 1024\n\t" \
  "s_nop 0\n\t" \
  "buffer_load_dwordx4 v3, s[4:7], s22 offen lds\n\t" \
  "s_add_u32 m0, m0, 1024\n\t" \
  "s_add_u32 s22, s22, 131072\n\t" \
  "buffer_load_dwordx4 v0, s[4:7], s22 offen lds\n\t" \
  "s_add_u32 m0, m0, 1024\n\t" \
  "s_nop 0\n\t" \
  "buffer_load_dwordx4 v1, s[4:7], s22 offen lds\n\t" \
  "s_add_u32 m0, m0, 1024\n\t" \
  "s_nop 0\n\t" \
  "buffer_load_dwordx4 v2, s[4:7], s22 offen lds\n\t" \
  "s_add_u32 m0, m0, 1024\n\t" \
  "s_nop 0\n\t" \
  "buffer_load_dwordx4 v3, s[4:7], s22 offen lds\n\t" \
  "s_waitcnt vmcnt(16)\n\t" \
  "s_barrier\n\t" \
  "ds_read_b64_tr_b16 v[40:41], v8 offset:0\n\t" \
  "ds_read_b64_tr_b16 v[44:45], v8 offset:8192\n\t" \
  "ds_read_b64_tr_b16 a[0:1], v9 offset:0\n\t" \
  "ds_read_b64_tr_b16 a[4:5], v9 offset:8192\n\t" \
  "ds_read_b64_tr_b16 a[16:17], v10 offset:0\n\t" \
  "ds_read_b64_tr_b16 a[20:21], v10 offset:8192\n\t" \
  "ds_read_b64_tr_b16 a[32:33], v11 offset:0\n\t" \
  "ds_read_b64_tr_b16 a[36:37], v11 offset:8192\n\t" \
  "ds_read_b64_tr_b16 v[42:43], v12 offset:0\n\t" \
  "ds_read_b64_tr_b16 v[46:47], v12 offset:8192\n\t" \
  "ds_read_b64_tr_b16 a[2:3], v13 offset:0\n\t" \
  "ds_read_b64_tr_b16 a[6:7], v13 offset:8192\n\t" \
  "ds_read_b64_tr_b16 a[18:19], v14 offset:0\n\t" \
  "ds_read_b64_tr_b16 a[22:23], v14 offset:8192\n\t" \
  "ds_read_b64_tr_b16 a[34:35], v15 offset:0\n\t" \
  "ds_read_b64_tr_b16 a[38:39], v15 offset:8192\n\t" \
  "ds_read_b64_tr_b16 v[48:49], v16 offset:0\n\t" \
  "ds_read_b64_tr_b16 v[52:53], v16 offset:8192\n\t" \
  "ds_read_b64_tr_b16 a[48:49], v17 offset:0\n\t" \
  "ds_read_b64_tr_b16 a[52:53], v17 offset:8192\n\t" \
  "ds_read_b64_tr_b16 a[64:65], v18 offset:0\n\t" \
  "ds_read_b64_tr_b16 a[68:69], v18 offset:8192\n\t" \
  "ds_read_b64_tr_b16 a[80:81], v19 offset:0\n\t" \
  "ds_read_b64_tr_b16 a[84:85], v19 offset:8192\n\t" \
  "ds_read_b64_tr_b16 v[50:51], v20 offset:0\n\t" \
  "ds_read_b64_tr_b16 v[54:55], v20 offset:8192\n\t" \
  "ds_read_b64_tr_b16 a[50:51], v21 offset:0\n\t" \
  "ds_read_b64_tr_b16 a[54:55], v21 offset:8192\n\t" \
  "ds_read_b64_tr_b16 a[66:67], v22 offset:0\n\t" \
  "ds_read_b64_tr_b16 a[70:71], v22 offset:8192\n\t" \
  "ds_read_b64_tr_b16 a[82:83], v23 offset:0\n\t" \
  "ds_read_b64_tr_b16 a[86:87], v23 offset:8192\n\t" \
  "s_waitcnt lgkmcnt(0)\n\t" \
  "s_barrier\n\t" \
  "s_lshl_b32 s23, s20, 14\n\t" \
  "s_add_u32 m0, s23, 32768\n\t" \
  "s_mov_b32 s22, 0\n\t" \
  "buffer_load_dwordx4 v4, s[16:19], s22 offen lds\n\t" \
  "s_add_u32 m0, m0, 1024\n\t" \
  "s_add_u32 s22, s22, 8192\n\t" \
  "buffer_load_dwordx4 v4, s[16:19], s22 offen lds\n\t" \
  "s_add_u32 m0, m0, 1024\n\t" \
  "s_add_u32 s22, s22, 8192\n\t" \
  "buffer_load_dwordx4 v4, s[16:19], s22 offen lds\n\t" \
  "s_add_u32 m0, m0, 1024\n\t" \
  "s_add_u32 s22, s22, 8192\n\t" \
  "buffer_load_dwordx4 v4, s[16:19], s22 offen lds\n\t" \
  "s_add_u32 m0, m0, 1024\n\t" \
  "s_add_u32 s22, s22, 8192\n\t" \
  "buffer_load_dwordx4 v4, s[16:19], s22 offen lds\n\t" \
  "s_add_u32 m0, m0, 1024\n\t" \
  "s_add_u32 s22, s22, 8192\n\t" \
  "buffer_load_dwordx4 v4, s[16:19], s22 offen lds\n\t" \
  "s_add_u32 m0, m0, 1024\n\t" \
  "s_add_u32 s22, s22, 8192\n\t" \
  "buffer_load_dwordx4 v4, s[16:19], s22 offen lds\n\t" \
  "s_add_u32 m0, m0, 1024\n\t" \
  "s_add_u32 s22, s22, 8192\n\t" \
  "buffer_load_dwordx4 v4, s[16:19], s22 offen lds\n\t" \
  "s_add_u32 m0, m0, 1024\n\t" \
  "s_add_u32 s22, s22, 8192\n\t" \
  "buffer_load_dwordx4 v4, s[16:19], s22 offen lds\n\t" \
  "s_add_u32 m0, m0, 1024\n\t" \
  "s_add_u32 s22, s22, 8192\n\t" \
  "buffer_load_dwordx4 v4, s[16:19], s22 offen lds\n\t" \
  "s_add_u32 m0, m0, 1024\n\t" \
  "s_add_u32 s22, s22, 8192\n\t" \
  "buffer_load_dwordx4 v4, s[16:19], s22 offen lds\n\t" \
  "s_add_u32 m0, m0, 1024\n\t" \
  "s_add_u32 s22, s22, 8192\n\t" \
  "buffer_load_dwordx4 v4, s[16:19], s22 offen lds\n\t" \
  "s_add_u32 m0, m0, 1024\n\t" \
  "s_add_u32 s22, s22, 8192\n\t" \
  "buffer_load_dwordx4 v4, s[16:19], s22 offen lds\n\t" \
  "s_add_u32 m0, m0, 1024\n\t" \
  "s_add_u32 s22, s22, 8192\n\t" \
  "buffer_load_dwordx4 v4, s[16:19], s22 offen lds\n\t" \
  "s_add_u32 m0, m0, 1024\n\t" \
  "s_add_u32 s22, s22, 8192\n\t" \
  "buffer_load_dwordx4 v4, s[16:19], s22 offen lds\n\t" \
  "s_add_u32 m0, m0, 1024\n\t" \
  "s_add_u32 s22, s22, 8192\n\t" \
  "buffer_load_dwordx4 v4, s[16:19], s22 offen lds\n\t" \
  "s_waitcnt vmcnt(16)\n\t" \
  "s_barrier\n\t" \
  "ds_read_b64_tr_b16 v[72:73], v24 offset:0\n\t" \
  "ds_read_b64_tr_b16 v[76:77], v24 offset:8192\n\t" \
  "ds_read_b64_tr_b16 a[8:9], v25 offset:0\n\t" \
  "ds_read_b64_tr_b16 a[12:13], v25 offset:8192\n\t" \
  "ds_read_b64_tr_b16 a[24:25], v26 offset:0\n\t" \
  "ds_read_b64_tr_b16 a[28:29], v26 offset:8192\n\t" \
  "ds_read_b64_tr_b16 a[40:41], v27 offset:0\n\t" \
  "ds_read_b64_tr_b16 a[44:45], v27 offset:8192\n\t" \
  "ds_read_b64_tr_b16 v[74:75], v28 offset:0\n\t" \
  "ds_read_b64_tr_b16 v[78:79], v28 offset:8192\n\t" \
  "ds_read_b64_tr_b16 a[10:11], v29 offset:0\n\t" \
  "ds_read_b64_tr_b16 a[14:15], v29 offset:8192\n\t" \
  "ds_read_b64_tr_b16 a[26:27], v30 offset:0\n\t" \
  "ds_read_b64_tr_b16 a[30:31], v30 offset:8192\n\t" \
  "ds_read_b64_tr_b16 a[42:43], v31 offset:0\n\t" \
  "ds_read_b64_tr_b16 a[46:47], v31 offset:8192\n\t" \
  "ds_read_b64_tr_b16 v[80:81], v32 offset:0\n\t" \
  "ds_read_b64_tr_b16 v[84:85], v32 offset:8192\n\t" \
  "ds_read_b64_tr_b16 a[56:57], v33 offset:0\n\t" \
  "ds_read_b64_tr_b16 a[60:61], v33 offset:8192\n\t" \
  "ds_read_b64_tr_b16 a[72:73], v34 offset:0\n\t" \
  "ds_read_b64_tr_b16 a[76:77], v34 offset:8192\n\t" \
  "ds_read_b64_tr_b16 a[88:89], v35 offset:0\n\t" \
  "ds_read_b64_tr_b16 a[92:93], v35 offset:8192\n\t" \
  "ds_read_b64_tr_b16 v[82:83], v36 offset:0\n\t" \
  "ds_read_b64_tr_b16 v[86:87], v36 offset:8192\n\t" \
  "ds_read_b64_tr_b16 a[58:59], v37 offset:0\n\t" \
  "ds_read_b64_tr_b16 a[62:63], v37 offset:8192\n\t" \
  "ds_read_b64_tr_b16 a[74:75], v38 offset:0\n\t" \
  "ds_read_b64_tr_b16 a[78:79], v38 offset:8192\n\t" \
  "ds_read_b64_tr_b16 a[90:91], v39 offset:0\n\t" \
  "ds_read_b64_tr_b16 a[94:95], v39 offset:8192\n\t" \
  "s_mov_b32 s22, 0x05040100\n\t" \
  "s_mov_b32 s23, 0x07060302\n\t" \
  "s_waitcnt vmcnt(0) lgkmcnt(0)\n\t" \
  "ds_read_b64 v[8:9], v6 offset:0\n\t" \
  "ds_read_b64 v[10:11], v6 offset:2048\n\t" \
  "ds_read_b64 v[12:13], v6 offset:128\n\t" \
  "ds_read_b64 v[14:15], v6 offset:2176\n\t" \
  "ds_read_b64 v[16:17], v6 offset:1024\n\t" \
  "ds_read_b64 v[18:19], v6 offset:3072\n\t" \
  "ds_read_b64 v[20:21], v6 offset:1152\n\t" \
  "ds_read_b64 v[22:23], v6 offset:3200\n\t" \
  "ds_read_b64 v[24:25], v6 offset:4096\n\t" \
  "ds_read_b64 v[26:27], v6 offset:6144\n\t" \
  "ds_read_b64 v[28:29], v6 offset:4224\n\t" \
  "ds_read_b64 v[30:31], v6 offset:6272\n\t" \
  "ds_read_b64 v[32:33], v6 offset:5120\n\t" \
  "ds_read_b64 v[34:35], v6 offset:7168\n\t" \
  "ds_read_b64 v[36:37], v6 offset:5248\n\t" \
  "ds_read_b64 v[38:39], v6 offset:7296\n\t" \
  "s_waitcnt lgkmcnt(8)\n\t" \
  "v_perm_b32 v104, v10, v8, s22\n\t" \
  "v_perm_b32 v105, v10, v8, s23\n\t" \
  "v_perm_b32 v106, v14, v12, s22\n\t" \
  "v_perm_b32 v107, v14, v12, s23\n\t" \
  "v_perm_b32 v108, v18, v16, s22\n\t" \
  "v_perm_b32 v109, v18, v16, s23\n\t" \
  "v_perm_b32 v110, v22, v20, s22\n\t" \
  "v_perm_b32 v111, v22, v20, s23\n\t" \
  "v_perm_b32 v112, v11, v9, s22\n\t" \
  "v_perm_b32 v113, v11, v9, s23\n\t" \
  "v_perm_b32 v114, v15, v13, s22\n\t" \
  "v_perm_b32 v115, v15, v13, s23\n\t" \
  "v_perm_b32 v116, v19, v17, s22\n\t" \
  "v_perm_b32 v117, v19, v17, s23\n\t" \
  "v_perm_b32 v118, v23, v21, s22\n\t" \
  "v_perm_b32 v119, v23, v21, s23\n\t" \
  "v_accvgpr_write_b32 a192, v104\n\t" \
  "v_accvgpr_write_b32 a193, v105\n\t" \
  "v_accvgpr_write_b32 a194, v106\n\t" \
  "v_accvgpr_write_b32 a195, v107\n\t" \
  "v_accvgpr_write_b32 a196, v108\n\t" \
  "v_accvgpr_write_b32 a197, v109\n\t" \
  "v_accvgpr_write_b32 a198, v110\n\t" \
  "v_accvgpr_write_b32 a199, v111\n\t" \
  "v_accvgpr_write_b32 a200, v112\n\t" \
  "v_accvgpr_write_b32 a201, v113\n\t" \
  "v_accvgpr_write_b32 a202, v114\n\t" \
  "v_accvgpr_write_b32 a203, v115\n\t" \
  "v_accvgpr_write_b32 a204, v116\n\t" \
  "v_accvgpr_write_b32 a205, v117\n\t" \
  "v_accvgpr_write_b32 a206, v118\n\t" \
  "v_accvgpr_write_b32 a207, v119\n\t" \
  "s_waitcnt lgkmcnt(0)\n\t" \
  "v_perm_b32 v104, v26, v24, s22\n\t" \
  "v_perm_b32 v105, v26, v24, s23\n\t" \
  "v_perm_b32 v106, v30, v28, s22\n\t" \
  "v_perm_b32 v107, v30, v28, s23\n\t" \
  "v_perm_b32 v108, v34, v32, s22\n\t" \
  "v_perm_b32 v109, v34, v32, s23\n\t" \
  "v_perm_b32 v110, v38, v36, s22\n\t" \
  "v_perm_b32 v111, v38, v36, s23\n\t" \
  "v_perm_b32 v112, v27, v25, s22\n\t" \
  "v_perm_b32 v113, v27, v25, s23\n\t" \
  "v_perm_b32 v114, v31, v29, s22\n\t" \
  "v_perm_b32 v115, v31, v29, s23\n\t" \
  "v_perm_b32 v116, v35, v33, s22\n\t" \
  "v_perm_b32 v117, v35, v33, s23\n\t" \
  "v_perm_b32 v118, v39, v37, s22\n\t" \
  "v_perm_b32 v119, v39, v37, s23\n\t" \
  "v_accvgpr_write_b32 a208, v104\n\t" \
  "v_accvgpr_write_b32 a209, v105\n\t" \
  "v_accvgpr_write_b32 a210, v106\n\t" \
  "v_accvgpr_write_b32 a211, v107\n\t" \
  "v_accvgpr_write_b32 a212, v108\n\t" \
  "v_accvgpr_write_b32 a213, v109\n\t" \
  "v_accvgpr_write_b32 a214, v110\n\t" \
  "v_accvgpr_write_b32 a215, v111\n\t" \
  "v_accvgpr_write_b32 a216, v112\n\t" \
  "v_accvgpr_write_b32 a217, v113\n\t" \
  "v_accvgpr_write_b32 a218, v114\n\t" \
  "v_accvgpr_write_b32 a219, v115\n\t" \
  "v_accvgpr_write_b32 a220, v116\n\t" \
  "v_accvgpr_write_b32 a221, v117\n\t" \
  "v_accvgpr_write_b32 a222, v118\n\t" \
  "v_accvgpr_write_b32 a223, v119\n\t" \
  "ds_read_b64 v[8:9], v6 offset:8192\n\t" \
  "ds_read_b64 v[10:11], v6 offset:10240\n\t" \
  "ds_read_b64 v[12:13], v6 offset:8320\n\t" \
  "ds_read_b64 v[14:15], v6 offset:10368\n\t" \
  "ds_read_b64 v[16:17], v6 offset:9216\n\t" \
  "ds_read_b64 v[18:19], v6 offset:11264\n\t" \
  "ds_read_b64 v[20:21], v6 offset:9344\n\t" \
  "ds_read_b64 v[22:23], v6 offset:11392\n\t" \
  "ds_read_b64 v[24:25], v6 offset:12288\n\t" \
  "ds_read_b64 v[26:27], v6 offset:14336\n\t" \
  "ds_read_b64 v[28:29], v6 offset:12416\n\t" \
  "ds_read_b64 v[30:31], v6 offset:14464\n\t" \
  "ds_read_b64 v[32:33], v6 offset:13312\n\t" \
  "ds_read_b64 v[34:35], v6 offset:15360\n\t" \
  "ds_read_b64 v[36:37], v6 offset:13440\n\t" \
  "ds_read_b64 v[38:39], v6 offset:15488\n\t" \
  "s_waitcnt lgkmcnt(8)\n\t" \
  "v_perm_b32 v104, v10, v8, s22\n\t" \
  "v_perm_b32 v105, v10, v8, s23\n\t" \
  "v_perm_b32 v106, v14, v12, s22\n\t" \
  "v_perm_b32 v107, v14, v12, s23\n\t" \
  "v_perm_b32 v108, v18, v16, s22\n\t" \
  "v_perm_b32 v109, v18, v16, s23\n\t" \
  "v_perm_b32 v110, v22, v20, s22\n\t" \
  "v_perm_b32 v111, v22, v20, s23\n\t" \
  "v_perm_b32 v112, v11, v9, s22\n\t" \
  "v_perm_b32 v113, v11, v9, s23\n\t" \
  "v_perm_b32 v114, v15, v13, s22\n\t" \
  "v_perm_b32 v115, v15, v13, s23\n\t" \
  "v_perm_b32 v116, v19, v17, s22\n\t" \
  "v_perm_b32 v117, v19, v17, s23\n\t" \
  "v_perm_b32 v118, v23, v21, s22\n\t" \
  "v_perm_b32 v119, v23, v21, s23\n\t" \
  "v_accvgpr_write_b32 a224, v104\n\t" \
  "v_accvgpr_write_b32 a225, v105\n\t" \
  "v_accvgpr_write_b32 a226, v106\n\t" \
  "v_accvgpr_write_b32 a227, v107\n\t" \
  "v_accvgpr_write_b32 a228, v108\n\t" \
  "v_accvgpr_write_b32 a229, v109\n\t" \
  "v_accvgpr_write_b32 a230, v110\n\t" \
  "v_accvgpr_write_b32 a231, v111\n\t" \
  "v_accvgpr_write_b32 a232, v112\n\t" \
  "v_accvgpr_write_b32 a233, v113\n\t" \
  "v_accvgpr_write_b32 a234, v114\n\t" \
  "v_accvgpr_write_b32 a235, v115\n\t" \
  "v_accvgpr_write_b32 a236, v116\n\t" \
  "v_accvgpr_write_b32 a237, v117\n\t" \
  "v_accvgpr_write_b32 a238, v118\n\t" \
  "v_accvgpr_write_b32 a239, v119\n\t" \
  "s_waitcnt lgkmcnt(0)\n\t" \
  "v_perm_b32 v104, v26, v24, s22\n\t" \
  "v_perm_b32 v105, v26, v24, s23\n\t" \
  "v_perm_b32 v106, v30, v28, s22\n\t" \
  "v_perm_b32 v107, v30, v28, s23\n\t" \
  "v_perm_b32 v108, v34, v32, s22\n\t" \
  "v_perm_b32 v109, v34, v32, s23\n\t" \
  "v_perm_b32 v110, v38, v36, s22\n\t" \
  "v_perm_b32 v111, v38, v36, s23\n\t" \
  "v_perm_b32 v112, v27, v25, s22\n\t" \
  "v_perm_b32 v113, v27, v25, s23\n\t" \
  "v_perm_b32 v114, v31, v29, s22\n\t" \
  "v_perm_b32 v115, v31, v29, s23\n\t" \
  "v_perm_b32 v116, v35, v33, s22\n\t" \
  "v_perm_b32 v117, v35, v33, s23\n\t" \
  "v_perm_b32 v118, v39, v37, s22\n\t" \
  "v_perm_b32 v119, v39, v37, s23\n\t" \
  "v_accvgpr_write_b32 a240, v104\n\t" \
  "v_accvgpr_write_b32 a241, v105\n\t" \
  "v_accvgpr_write_b32 a242, v106\n\t" \
  "v_accvgpr_write_b32 a243, v107\n\t" \
  "v_accvgpr_write_b32 a244, v108\n\t" \
  "v_accvgpr_write_b32 a245, v109\n\t" \
  "v_accvgpr_write_b32 a246, v110\n\t" \
  "v_accvgpr_write_b32 a247, v111\n\t" \
  "v_accvgpr_write_b32 a248, v112\n\t" \
  "v_accvgpr_write_b32 a249, v113\n\t" \
  "v_accvgpr_write_b32 a250, v114\n\t" \
  "v_accvgpr_write_b32 a251, v115\n\t" \
  "v_accvgpr_write_b32 a252, v116\n\t" \
  "v_accvgpr_write_b32 a253, v117\n\t" \
  "v_accvgpr_write_b32 a254, v118\n\t" \
  "v_accvgpr_write_b32 a255, v119\n\t" \
  "ds_write_b128 v5, v[40:43] offset:0\n\t" \
  "ds_write_b128 v5, v[44:47] offset:1024\n\t" \
  "ds_write_b128 v5, v[48:51] offset:2048\n\t" \
  "ds_write_b128 v5, v[52:55] offset:3072\n\t" \
  "ds_write_b128 v5, v[56:59] offset:4096\n\t" \
  "ds_write_b128 v5, v[60:63] offset:5120\n\t" \
  "ds_write_b128 v5, v[64:67] offset:6144\n\t" \
  "ds_write_b128 v5, v[68:71] offset:7168\n\t" \
  "ds_write_b128 v5, v[72:75] offset:8192\n\t" \
  "ds_write_b128 v5, v[76:79] offset:9216\n\t" \
  "ds_write_b128 v5, v[80:83] offset:10240\n\t" \
  "ds_write_b128 v5, v[84:87] offset:11264\n\t" \
  "ds_write_b128 v5, v[88:91] offset:12288\n\t" \
  "ds_write_b128 v5, v[92:95] offset:13312\n\t" \
  "ds_write_b128 v5, v[96:99] offset:14336\n\t" \
  "ds_write_b128 v5, v[100:103] offset:15360\n\t" \
  "s_waitcnt lgkmcnt(0)\n\t" \
  "s_barrier\n\t" \
  "s_mov_b32 m0, s25\n\t" \
  "s_waitcnt lgkmcnt(0)\n\t"
#define CLOB_LOAD "memory", "scc", "s22", "s23", "s25", "v24", "v25", "v26", "v27", "v28", "v29", "v30", "v31", "v32", "v33", "v34", "v35", "v36", "v37", "v38", "v39", "v40", "v41", "v42", "v43", "v44", "v45", "v46", "v47", "v48", "v49", "v50", "v51", "v52", "v53", "v54", "v55", "v56", "v57", "v58", "v59", "v60", "v61", "v62", "v63", "v64", "v65", "v66", "v67", "v68", "v69", "v70", "v71", "v72", "v73", "v74", "v75", "v76", "v77", "v78", "v79", "v80", "v81", "v82", "v83", "v84", "v85", "v86", "v87", "v88", "v89", "v90", "v91", "v92", "v93", "v94", "v95", "v96", "v97", "v98", "v99", "v100", "v101", "v102", "v103", "v104", "v105", "v106", "v107", "v108", "v109", "v110", "v111", "v112", "v113", "v114", "v115", "v116", "v117", "v118", "v119", "v120", "v121", "v122", "v123", "v124", "v125", "v126", "v127"
#define OUT_LOAD \
  "s_nop 4\n\t" \
  "s_mov_b32 s23, 0x05040100\n\t" \
  "s_mov_b32 s24, 0x07060302\n\t" \
  "s_add_u32 s22, s12, 0\n\t" \
  "s_and_b32 s22, s22, 0xfffff\n\t" \
  "buffer_load_dword a0, v0, s[4:7], s22 offen offset:0\n\t" \
  "buffer_load_dword a1, v0, s[4:7], s22 offen offset:1024\n\t" \
  "buffer_load_dword a2, v0, s[4:7], s22 offen offset:2048\n\t" \
  "buffer_load_dword a3, v0, s[4:7], s22 offen offset:3072\n\t" \
  "buffer_load_dword a4, v1, s[4:7], s22 offen offset:0\n\t" \
  "buffer_load_dword a5, v1, s[4:7], s22 offen offset:1024\n\t" \
  "buffer_load_dword a6, v1, s[4:7], s22 offen offset:2048\n\t" \
  "buffer_load_dword a7, v1, s[4:7], s22 offen offset:3072\n\t" \
  "s_add_u32 s22, s12, 32768\n\t" \
  "s_and_b32 s22, s22, 0xfffff\n\t" \
  "buffer_load_dword a8, v0, s[4:7], s22 offen offset:0\n\t" \
  "buffer_load_dword a9, v0, s[4:7], s22 offen offset:1024\n\t" \
  "buffer_load_dword a10, v0, s[4:7], s22 offen offset:2048\n\t" \
  "buffer_load_dword a11, v0, s[4:7], s22 offen offset:3072\n\t" \
  "buffer_load_dword a12, v1, s[4:7], s22 offen offset:0\n\t" \
  "buffer_load_dword a13, v1, s[4:7], s22 offen offset:1024\n\t" \
  "buffer_load_dword a14, v1, s[4:7], s22 offen offset:2048\n\t" \
  "buffer_load_dword a15, v1, s[4:7], s22 offen offset:3072\n\t" \
  "s_add_u32 s22, s12, 65536\n\t" \
  "s_and_b32 s22, s22, 0xfffff\n\t" \
  "buffer_load_dword a16, v0, s[4:7], s22 offen offset:0\n\t" \
  "buffer_load_dword a17, v0, s[4:7], s22 offen offset:1024\n\t" \
  "buffer_load_dword a18, v0, s[4:7], s22 offen offset:2048\n\t" \
  "buffer_load_dword a19, v0, s[4:7], s22 offen offset:3072\n\t" \
  "buffer_load_dword a20, v1, s[4:7], s22 offen offset:0\n\t" \
  "buffer_load_dword a21, v1, s[4:7], s22 offen offset:1024\n\t" \
  "buffer_load_dword a22, v1, s[4:7], s22 offen offset:2048\n\t" \
  "buffer_load_dword a23, v1, s[4:7], s22 offen offset:3072\n\t" \
  "s_add_u32 s22, s12, 98304\n\t" \
  "s_and_b32 s22, s22, 0xfffff\n\t" \
  "buffer_load_dword a24, v0, s[4:7], s22 offen offset:0\n\t" \
  "buffer_load_dword a25, v0, s[4:7], s22 offen offset:1024\n\t" \
  "buffer_load_dword a26, v0, s[4:7], s22 offen offset:2048\n\t" \
  "buffer_load_dword a27, v0, s[4:7], s22 offen offset:3072\n\t" \
  "buffer_load_dword a28, v1, s[4:7], s22 offen offset:0\n\t" \
  "buffer_load_dword a29, v1, s[4:7], s22 offen offset:1024\n\t" \
  "buffer_load_dword a30, v1, s[4:7], s22 offen offset:2048\n\t" \
  "buffer_load_dword a31, v1, s[4:7], s22 offen offset:3072\n\t" \
  "s_add_u32 s22, s12, 131072\n\t" \
  "s_and_b32 s22, s22, 0xfffff\n\t" \
  "buffer_load_dword a32, v0, s[4:7], s22 offen offset:0\n\t" \
  "buffer_load_dword a33, v0, s[4:7], s22 offen offset:1024\n\t" \
  "buffer_load_dword a34, v0, s[4:7], s22 offen offset:2048\n\t" \
  "buffer_load_dword a35, v0, s[4:7], s22 offen offset:3072\n\t" \
  "buffer_load_dword a36, v1, s[4:7], s22 offen offset:0\n\t" \
  "buffer_load_dword a37, v1, s[4:7], s22 offen offset:1024\n\t" \
  "buffer_load_dword a38, v1, s[4:7], s22 offen offset:2048\n\t" \
  "buffer_load_dword a39, v1, s[4:7], s22 offen offset:3072\n\t" \
  "s_add_u32 s22, s12, 163840\n\t" \
  "s_and_b32 s22, s22, 0xfffff\n\t" \
  "buffer_load_dword a40, v0, s[4:7], s22 offen offset:0\n\t" \
  "buffer_load_dword a41, v0, s[4:7], s22 offen offset:1024\n\t" \
  "buffer_load_dword a42, v0, s[4:7], s22 offen offset:2048\n\t" \
  "buffer_load_dword a43, v0, s[4:7], s22 offen offset:3072\n\t" \
  "buffer_load_dword a44, v1, s[4:7], s22 offen offset:0\n\t" \
  "buffer_load_dword a45, v1, s[4:7], s22 offen offset:1024\n\t" \
  "buffer_load_dword a46, v1, s[4:7], s22 offen offset:2048\n\t" \
  "buffer_load_dword a47, v1, s[4:7], s22 offen offset:3072\n\t" \
  "s_add_u32 s22, s12, 196608\n\t" \
  "s_and_b32 s22, s22, 0xfffff\n\t" \
  "buffer_load_dword a48, v0, s[4:7], s22 offen offset:0\n\t" \
  "buffer_load_dword a49, v0, s[4:7], s22 offen offset:1024\n\t" \
  "buffer_load_dword a50, v0, s[4:7], s22 offen offset:2048\n\t" \
  "buffer_load_dword a51, v0, s[4:7], s22 offen offset:3072\n\t" \
  "buffer_load_dword a52, v1, s[4:7], s22 offen offset:0\n\t" \
  "buffer_load_dword a53, v1, s[4:7], s22 offen offset:1024\n\t" \
  "buffer_load_dword a54, v1, s[4:7], s22 offen offset:2048\n\t" \
  "buffer_load_dword a55, v1, s[4:7], s22 offen offset:3072\n\t" \
  "s_waitcnt vmcnt(48)\n\t" \
  "v_accvgpr_read_b32 v8, a0\n\t" \
  "v_accvgpr_read_b32 v9, a1\n\t" \
  "v_accvgpr_read_b32 v10, a2\n\t" \
  "v_accvgpr_read_b32 v11, a3\n\t" \
  "v_accvgpr_read_b32 v12, a4\n\t" \
  "v_accvgpr_read_b32 v13, a5\n\t" \
  "v_accvgpr_read_b32 v14, a6\n\t" \
  "v_accvgpr_read_b32 v15, a7\n\t" \
  "v_perm_b32 v16, v12, v8, s23\n\t" \
  "v_perm_b32 v20, v12, v8, s24\n\t" \
  "v_perm_b32 v17, v13, v9, s23\n\t" \
  "v_perm_b32 v21, v13, v9, s24\n\t" \
  "v_perm_b32 v18, v14, v10, s23\n\t" \
  "v_perm_b32 v22, v14, v10, s24\n\t" \
  "v_perm_b32 v19, v15, v11, s23\n\t" \
  "v_perm_b32 v23, v15, v11, s24\n\t" \
  "v_accvgpr_write_b32 a0, v16\n\t" \
  "v_accvgpr_write_b32 a1, v17\n\t" \
  "v_accvgpr_write_b32 a2, v18\n\t" \
  "v_accvgpr_write_b32 a3, v19\n\t" \
  "v_accvgpr_write_b32 a4, v20\n\t" \
  "v_accvgpr_write_b32 a5, v21\n\t" \
  "v_accvgpr_write_b32 a6, v22\n\t" \
  "v_accvgpr_write_b32 a7, v23\n\t" \
  "s_add_u32 s22, s12, 229376\n\t" \
  "s_and_b32 s22, s22, 0xfffff\n\t" \
  "buffer_load_dword a56, v0, s[4:7], s22 offen offset:0\n\t" \
  "buffer_load_dword a57, v0, s[4:7], s22 offen offset:1024\n\t" \
  "buffer_load_dword a58, v0, s[4:7], s22 offen offset:2048\n\t" \
  "buffer_load_dword a59, v0, s[4:7], s22 offen offset:3072\n\t" \
  "buffer_load_dword a60, v1, s[4:7], s22 offen offset:0\n\t" \
  "buffer_load_dword a61, v1, s[4:7], s22 offen offset:1024\n\t" \
  "buffer_load_dword a62, v1, s[4:7], s22 offen offset:2048\n\t" \
  "buffer_load_dword a63, v1, s[4:7], s22 offen offset:3072\n\t" \
  "s_waitcnt vmcnt(48)\n\t" \
  "v_accvgpr_read_b32 v8, a8\n\t" \
  "v_accvgpr_read_b32 v9, a9\n\t" \
  "v_accvgpr_read_b32 v10, a10\n\t" \
  "v_accvgpr_read_b32 v11, a11\n\t" \
  "v_accvgpr_read_b32 v12, a12\n\t" \
  "v_accvgpr_read_b32 v13, a13\n\t" \
  "v_accvgpr_read_b32 v14, a14\n\t" \
  "v_accvgpr_read_b32 v15, a15\n\t" \
  "v_perm_b32 v16, v12, v8, s23\n\t" \
  "v_perm_b32 v20, v12, v8, s24\n\t" \
  "v_perm_b32 v17, v13, v9, s23\n\t" \
  "v_perm_b32 v21, v13, v9, s24\n\t" \
  "v_perm_b32 v18, v14, v10, s23\n\t" \
  "v_perm_b32 v22, v14, v10, s24\n\t" \
  "v_perm_b32 v19, v15, v11, s23\n\t" \
  "v_perm_b32 v23, v15, v11, s24\n\t" \
  "v_accvgpr_write_b32 a8, v16\n\t" \
  "v_accvgpr_write_b32 a9, v17\n\t" \
  "v_accvgpr_write_b32 a10, v18\n\t" \
  "v_accvgpr_write_b32 a11, v19\n\t" \
  "v_accvgpr_write_b32 a12, v20\n\t" \
  "v_accvgpr_write_b32 a13, v21\n\t" \
  "v_accvgpr_write_b32 a14, v22\n\t" \
  "v_accvgpr_write_b32 a15, v23\n\t" \
  "s_add_u32 s22, s12, 262144\n\t" \
  "s_and_b32 s22, s22, 0xfffff\n\t" \
  "buffer_load_dword a64, v0, s[4:7], s22 offen offset:0\n\t" \
  "buffer_load_dword a65, v0, s[4:7], s22 offen offset:1024\n\t" \
  "buffer_load_dword a66, v0, s[4:7], s22 offen offset:2048\n\t" \
  "buffer_load_dword a67, v0, s[4:7], s22 offen offset:3072\n\t" \
  "buffer_load_dword a68, v1, s[4:7], s22 offen offset:0\n\t" \
  "buffer_load_dword a69, v1, s[4:7], s22 offen offset:1024\n\t" \
  "buffer_load_dword a70, v1, s[4:7], s22 offen offset:2048\n\t" \
  "buffer_load_dword a71, v1, s[4:7], s22 offen offset:3072\n\t" \
  "s_waitcnt vmcnt(48)\n\t" \
  "v_accvgpr_read_b32 v8, a16\n\t" \
  "v_accvgpr_read_b32 v9, a17\n\t" \
  "v_accvgpr_read_b32 v10, a18\n\t" \
  "v_accvgpr_read_b32 v11, a19\n\t" \
  "v_accvgpr_read_b32 v12, a20\n\t" \
  "v_accvgpr_read_b32 v13, a21\n\t" \
  "v_accvgpr_read_b32 v14, a22\n\t" \
  "v_accvgpr_read_b32 v15, a23\n\t" \
  "v_perm_b32 v16, v12, v8, s23\n\t" \
  "v_perm_b32 v20, v12, v8, s24\n\t" \
  "v_perm_b32 v17, v13, v9, s23\n\t" \
  "v_perm_b32 v21, v13, v9, s24\n\t" \
  "v_perm_b32 v18, v14, v10, s23\n\t" \
  "v_perm_b32 v22, v14, v10, s24\n\t" \
  "v_perm_b32 v19, v15, v11, s23\n\t" \
  "v_perm_b32 v23, v15, v11, s24\n\t" \
  "v_accvgpr_write_b32 a16, v16\n\t" \
  "v_accvgpr_write_b32 a17, v17\n\t" \
  "v_accvgpr_write_b32 a18, v18\n\t" \
  "v_accvgpr_write_b32 a19, v19\n\t" \
  "v_accvgpr_write_b32 a20, v20\n\t" \
  "v_accvgpr_write_b32 a21, v21\n\t" \
  "v_accvgpr_write_b32 a22, v22\n\t" \
  "v_accvgpr_write_b32 a23, v23\n\t" \
  "s_add_u32 s22, s12, 294912\n\t" \
  "s_and_b32 s22, s22, 0xfffff\n\t" \
  "buffer_load_dword a72, v0, s[4:7], s22 offen offset:0\n\t" \
  "buffer_load_dword a73, v0, s[4:7], s22 offen offset:1024\n\t" \
  "buffer_load_dword a74, v0, s[4:7], s22 offen offset:2048\n\t" \
  "buffer_load_dword a75, v0, s[4:7], s22 offen offset:3072\n\t" \
  "buffer_load_dword a76, v1, s[4:7], s22 offen offset:0\n\t" \
  "buffer_load_dword a77, v1, s[4:7], s22 offen offset:1024\n\t" \
  "buffer_load_dword a78, v1, s[4:7], s22 offen offset:2048\n\t" \
  "buffer_load_dword a79, v1, s[4:7], s22 offen offset:3072\n\t" \
  "s_waitcnt vmcnt(48)\n\t" \
  "v_accvgpr_read_b32 v8, a24\n\t" \
  "v_accvgpr_read_b32 v9, a25\n\t" \
  "v_accvgpr_read_b32 v10, a26\n\t" \
  "v_accvgpr_read_b32 v11, a27\n\t" \
  "v_accvgpr_read_b32 v12, a28\n\t" \
  "v_accvgpr_read_b32 v13, a29\n\t" \
  "v_accvgpr_read_b32 v14, a30\n\t" \
  "v_accvgpr_read_b32 v15, a31\n\t" \
  "v_perm_b32 v16, v12, v8, s23\n\t" \
  "v_perm_b32 v20, v12, v8, s24\n\t" \
  "v_perm_b32 v17, v13, v9, s23\n\t" \
  "v_perm_b32 v21, v13, v9, s24\n\t" \
  "v_perm_b32 v18, v14, v10, s23\n\t" \
  "v_perm_b32 v22, v14, v10, s24\n\t" \
  "v_perm_b32 v19, v15, v11, s23\n\t" \
  "v_perm_b32 v23, v15, v11, s24\n\t" \
  "v_accvgpr_write_b32 a24, v16\n\t" \
  "v_accvgpr_write_b32 a25, v17\n\t" \
  "v_accvgpr_write_b32 a26, v18\n\t" \
  "v_accvgpr_write_b32 a27, v19\n\t" \
  "v_accvgpr_write_b32 a28, v20\n\t" \
  "v_accvgpr_write_b32 a29, v21\n\t" \
  "v_accvgpr_write_b32 a30, v22\n\t" \
  "v_accvgpr_write_b32 a31, v23\n\t" \
  "s_add_u32 s22, s12, 327680\n\t" \
  "s_and_b32 s22, s22, 0xfffff\n\t" \
  "buffer_load_dword a80, v0, s[4:7], s22 offen offset:0\n\t" \
  "buffer_load_dword a81, v0, s[4:7], s22 offen offset:1024\n\t" \
  "buffer_load_dword a82, v0, s[4:7], s22 offen offset:2048\n\t" \
  "buffer_load_dword a83, v0, s[4:7], s22 offen offset:3072\n\t" \
  "buffer_load_dword a84, v1, s[4:7], s22 offen offset:0\n\t" \
  "buffer_load_dword a85, v1, s[4:7], s22 offen offset:1024\n\t" \
  "buffer_load_dword a86, v1, s[4:7], s22 offen offset:2048\n\t" \
  "buffer_load_dword a87, v1, s[4:7], s22 offen offset:3072\n\t" \
  "s_waitcnt vmcnt(48)\n\t" \
  "v_accvgpr_read_b32 v8, a32\n\t" \
  "v_accvgpr_read_b32 v9, a33\n\t" \
  "v_accvgpr_read_b32 v10, a34\n\t" \
  "v_accvgpr_read_b32 v11, a35\n\t" \
  "v_accvgpr_read_b32 v12, a36\n\t" \
  "v_accvgpr_read_b32 v13, a37\n\t" \
  "v_accvgpr_read_b32 v14, a38\n\t" \
  "v_accvgpr_read_b32 v15, a39\n\t" \
  "v_perm_b32 v16, v12, v8, s23\n\t" \
  "v_perm_b32 v20, v12, v8, s24\n\t" \
  "v_perm_b32 v17, v13, v9, s23\n\t" \
  "v_perm_b32 v21, v13, v9, s24\n\t" \
  "v_perm_b32 v18, v14, v10, s23\n\t" \
  "v_perm_b32 v22, v14, v10, s24\n\t" \
  "v_perm_b32 v19, v15, v11, s23\n\t" \
  "v_perm_b32 v23, v15, v11, s24\n\t" \
  "v_accvgpr_write_b32 a32, v16\n\t" \
  "v_accvgpr_write_b32 a33, v17\n\t" \
  "v_accvgpr_write_b32 a34, v18\n\t" \
  "v_accvgpr_write_b32 a35, v19\n\t" \
  "v_accvgpr_write_b32 a36, v20\n\t" \
  "v_accvgpr_write_b32 a37, v21\n\t" \
  "v_accvgpr_write_b32 a38, v22\n\t" \
  "v_accvgpr_write_b32 a39, v23\n\t" \
  "s_add_u32 s22, s12, 360448\n\t" \
  "s_and_b32 s22, s22, 0xfffff\n\t" \
  "buffer_load_dword a88, v0, s[4:7], s22 offen offset:0\n\t" \
  "buffer_load_dword a89, v0, s[4:7], s22 offen offset:1024\n\t" \
  "buffer_load_dword a90, v0, s[4:7], s22 offen offset:2048\n\t" \
  "buffer_load_dword a91, v0, s[4:7], s22 offen offset:3072\n\t" \
  "buffer_load_dword a92, v1, s[4:7], s22 offen offset:0\n\t" \
  "buffer_load_dword a93, v1, s[4:7], s22 offen offset:1024\n\t" \
  "buffer_load_dword a94, v1, s[4:7], s22 offen offset:2048\n\t" \
  "buffer_load_dword a95, v1, s[4:7], s22 offen offset:3072\n\t" \
  "s_waitcnt vmcnt(48)\n\t" \
  "v_accvgpr_read_b32 v8, a40\n\t" \
  "v_accvgpr_read_b32 v9, a41\n\t" \
  "v_accvgpr_read_b32 v10, a42\n\t" \
  "v_accvgpr_read_b32 v11, a43\n\t" \
  "v_accvgpr_read_b32 v12, a44\n\t" \
  "v_accvgpr_read_b32 v13, a45\n\t" \
  "v_accvgpr_read_b32 v14, a46\n\t" \
  "v_accvgpr_read_b32 v15, a47\n\t" \
  "v_perm_b32 v16, v12, v8, s23\n\t" \
  "v_perm_b32 v20, v12, v8, s24\n\t" \
  "v_perm_b32 v17, v13, v9, s23\n\t" \
  "v_perm_b32 v21, v13, v9, s24\n\t" \
  "v_perm_b32 v18, v14, v10, s23\n\t" \
  "v_perm_b32 v22, v14, v10, s24\n\t" \
  "v_perm_b32 v19, v15, v11, s23\n\t" \
  "v_perm_b32 v23, v15, v11, s24\n\t" \
  "v_accvgpr_write_b32 a40, v16\n\t" \
  "v_accvgpr_write_b32 a41, v17\n\t" \
  "v_accvgpr_write_b32 a42, v18\n\t" \
  "v_accvgpr_write_b32 a43, v19\n\t" \
  "v_accvgpr_write_b32 a44, v20\n\t" \
  "v_accvgpr_write_b32 a45, v21\n\t" \
  "v_accvgpr_write_b32 a46, v22\n\t" \
  "v_accvgpr_write_b32 a47, v23\n\t" \
  "s_add_u32 s22, s12, 393216\n\t" \
  "s_and_b32 s22, s22, 0xfffff\n\t" \
  "buffer_load_dword a96, v0, s[4:7], s22 offen offset:0\n\t" \
  "buffer_load_dword a97, v0, s[4:7], s22 offen offset:1024\n\t" \
  "buffer_load_dword a98, v0, s[4:7], s22 offen offset:2048\n\t" \
  "buffer_load_dword a99, v0, s[4:7], s22 offen offset:3072\n\t" \
  "buffer_load_dword a100, v1, s[4:7], s22 offen offset:0\n\t" \
  "buffer_load_dword a101, v1, s[4:7], s22 offen offset:1024\n\t" \
  "buffer_load_dword a102, v1, s[4:7], s22 offen offset:2048\n\t" \
  "buffer_load_dword a103, v1, s[4:7], s22 offen offset:3072\n\t" \
  "s_waitcnt vmcnt(48)\n\t" \
  "v_accvgpr_read_b32 v8, a48\n\t" \
  "v_accvgpr_read_b32 v9, a49\n\t" \
  "v_accvgpr_read_b32 v10, a50\n\t" \
  "v_accvgpr_read_b32 v11, a51\n\t" \
  "v_accvgpr_read_b32 v12, a52\n\t" \
  "v_accvgpr_read_b32 v13, a53\n\t" \
  "v_accvgpr_read_b32 v14, a54\n\t" \
  "v_accvgpr_read_b32 v15, a55\n\t" \
  "v_perm_b32 v16, v12, v8, s23\n\t" \
  "v_perm_b32 v20, v12, v8, s24\n\t" \
  "v_perm_b32 v17, v13, v9, s23\n\t" \
  "v_perm_b32 v21, v13, v9, s24\n\t" \
  "v_perm_b32 v18, v14, v10, s23\n\t" \
  "v_perm_b32 v22, v14, v10, s24\n\t" \
  "v_perm_b32 v19, v15, v11, s23\n\t" \
  "v_perm_b32 v23, v15, v11, s24\n\t" \
  "v_accvgpr_write_b32 a48, v16\n\t" \
  "v_accvgpr_write_b32 a49, v17\n\t" \
  "v_accvgpr_write_b32 a50, v18\n\t" \
  "v_accvgpr_write_b32 a51, v19\n\t" \
  "v_accvgpr_write_b32 a52, v20\n\t" \
  "v_accvgpr_write_b32 a53, v21\n\t" \
  "v_accvgpr_write_b32 a54, v22\n\t" \
  "v_accvgpr_write_b32 a55, v23\n\t" \
  "s_add_u32 s22, s12, 425984\n\t" \
  "s_and_b32 s22, s22, 0xfffff\n\t" \
  "buffer_load_dword a104, v0, s[4:7], s22 offen offset:0\n\t" \
  "buffer_load_dword a105, v0, s[4:7], s22 offen offset:1024\n\t" \
  "buffer_load_dword a106, v0, s[4:7], s22 offen offset:2048\n\t" \
  "buffer_load_dword a107, v0, s[4:7], s22 offen offset:3072\n\t" \
  "buffer_load_dword a108, v1, s[4:7], s22 offen offset:0\n\t" \
  "buffer_load_dword a109, v1, s[4:7], s22 offen offset:1024\n\t" \
  "buffer_load_dword a110, v1, s[4:7], s22 offen offset:2048\n\t" \
  "buffer_load_dword a111, v1, s[4:7], s22 offen offset:3072\n\t" \
  "s_waitcnt vmcnt(48)\n\t" \
  "v_accvgpr_read_b32 v8, a56\n\t" \
  "v_accvgpr_read_b32 v9, a57\n\t" \
  "v_accvgpr_read_b32 v10, a58\n\t" \
  "v_accvgpr_read_b32 v11, a59\n\t" \
  "v_accvgpr_read_b32 v12, a60\n\t" \
  "v_accvgpr_read_b32 v13, a61\n\t" \
  "v_accvgpr_read_b32 v14, a62\n\t" \
  "v_accvgpr_read_b32 v15, a63\n\t" \
  "v_perm_b32 v16, v12, v8, s23\n\t" \
  "v_perm_b32 v20, v12, v8, s24\n\t" \
  "v_perm_b32 v17, v13, v9, s23\n\t" \
  "v_perm_b32 v21, v13, v9, s24\n\t" \
  "v_perm_b32 v18, v14, v10, s23\n\t" \
  "v_perm_b32 v22, v14, v10, s24\n\t" \
  "v_perm_b32 v19, v15, v11, s23\n\t" \
  "v_perm_b32 v23, v15, v11, s24\n\t" \
  "v_accvgpr_write_b32 a56, v16\n\t" \
  "v_accvgpr_write_b32 a57, v17\n\t" \
  "v_accvgpr_write_b32 a58, v18\n\t" \
  "v_accvgpr_write_b32 a59, v19\n\t" \
  "v_accvgpr_write_b32 a60, v20\n\t" \
  "v_accvgpr_write_b32 a61, v21\n\t" \
  "v_accvgpr_write_b32 a62, v22\n\t" \
  "v_accvgpr_write_b32 a63, v23\n\t" \
  "s_add_u32 s22, s12, 458752\n\t" \
  "s_and_b32 s22, s22, 0xfffff\n\t" \
  "buffer_load_dword a112, v0, s[4:7], s22 offen offset:0\n\t" \
  "buffer_load_dword a113, v0, s[4:7], s22 offen offset:1024\n\t" \
  "buffer_load_dword a114, v0, s[4:7], s22 offen offset:2048\n\t" \
  "buffer_load_dword a115, v0, s[4:7], s22 offen offset:3072\n\t" \
  "buffer_load_dword a116, v1, s[4:7], s22 offen offset:0\n\t" \
  "buffer_load_dword a117, v1, s[4:7], s22 offen offset:1024\n\t" \
  "buffer_load_dword a118, v1, s[4:7], s22 offen offset:2048\n\t" \
  "buffer_load_dword a119, v1, s[4:7], s22 offen offset:3072\n\t" \
  "s_waitcnt vmcnt(48)\n\t" \
  "v_accvgpr_read_b32 v8, a64\n\t" \
  "v_accvgpr_read_b32 v9, a65\n\t" \
  "v_accvgpr_read_b32 v10, a66\n\t" \
  "v_accvgpr_read_b32 v11, a67\n\t" \
  "v_accvgpr_read_b32 v12, a68\n\t" \
  "v_accvgpr_read_b32 v13, a69\n\t" \
  "v_accvgpr_read_b32 v14, a70\n\t" \
  "v_accvgpr_read_b32 v15, a71\n\t" \
  "v_perm_b32 v16, v12, v8, s23\n\t" \
  "v_perm_b32 v20, v12, v8, s24\n\t" \
  "v_perm_b32 v17, v13, v9, s23\n\t" \
  "v_perm_b32 v21, v13, v9, s24\n\t" \
  "v_perm_b32 v18, v14, v10, s23\n\t" \
  "v_perm_b32 v22, v14, v10, s24\n\t" \
  "v_perm_b32 v19, v15, v11, s23\n\t" \
  "v_perm_b32 v23, v15, v11, s24\n\t" \
  "v_accvgpr_write_b32 a64, v16\n\t" \
  "v_accvgpr_write_b32 a65, v17\n\t" \
  "v_accvgpr_write_b32 a66, v18\n\t" \
  "v_accvgpr_write_b32 a67, v19\n\t" \
  "v_accvgpr_write_b32 a68, v20\n\t" \
  "v_accvgpr_write_b32 a69, v21\n\t" \
  "v_accvgpr_write_b32 a70, v22\n\t" \
  "v_accvgpr_write_b32 a71, v23\n\t" \
  "s_add_u32 s22, s12, 491520\n\t" \
  "s_and_b32 s22, s22, 0xfffff\n\t" \
  "buffer_load_dword a120, v0, s[4:7], s22 offen offset:0\n\t" \
  "buffer_load_dword a121, v0, s[4:7], s22 offen offset:1024\n\t" \
  "buffer_load_dword a122, v0, s[4:7], s22 offen offset:2048\n\t" \
  "buffer_load_dword a123, v0, s[4:7], s22 offen offset:3072\n\t" \
  "buffer_load_dword a124, v1, s[4:7], s22 offen offset:0\n\t" \
  "buffer_load_dword a125, v1, s[4:7], s22 offen offset:1024\n\t" \
  "buffer_load_dword a126, v1, s[4:7], s22 offen offset:2048\n\t" \
  "buffer_load_dword a127, v1, s[4:7], s22 offen offset:3072\n\t" \
  "s_waitcnt vmcnt(48)\n\t" \
  "v_accvgpr_read_b32 v8, a72\n\t" \
  "v_accvgpr_read_b32 v9, a73\n\t" \
  "v_accvgpr_read_b32 v10, a74\n\t" \
  "v_accvgpr_read_b32 v11, a75\n\t" \
  "v_accvgpr_read_b32 v12, a76\n\t" \
  "v_accvgpr_read_b32 v13, a77\n\t" \
  "v_accvgpr_read_b32 v14, a78\n\t" \
  "v_accvgpr_read_b32 v15, a79\n\t" \
  "v_perm_b32 v16, v12, v8, s23\n\t" \
  "v_perm_b32 v20, v12, v8, s24\n\t" \
  "v_perm_b32 v17, v13, v9, s23\n\t" \
  "v_perm_b32 v21, v13, v9, s24\n\t" \
  "v_perm_b32 v18, v14, v10, s23\n\t" \
  "v_perm_b32 v22, v14, v10, s24\n\t" \
  "v_perm_b32 v19, v15, v11, s23\n\t" \
  "v_perm_b32 v23, v15, v11, s24\n\t" \
  "v_accvgpr_write_b32 a72, v16\n\t" \
  "v_accvgpr_write_b32 a73, v17\n\t" \
  "v_accvgpr_write_b32 a74, v18\n\t" \
  "v_accvgpr_write_b32 a75, v19\n\t" \
  "v_accvgpr_write_b32 a76, v20\n\t" \
  "v_accvgpr_write_b32 a77, v21\n\t" \
  "v_accvgpr_write_b32 a78, v22\n\t" \
  "v_accvgpr_write_b32 a79, v23\n\t" \
  "s_add_u32 s22, s12, 524288\n\t" \
  "s_and_b32 s22, s22, 0xfffff\n\t" \
  "buffer_load_dword a128, v0, s[4:7], s22 offen offset:0\n\t" \
  "buffer_load_dword a129, v0, s[4:7], s22 offen offset:1024\n\t" \
  "buffer_load_dword a130, v0, s[4:7], s22 offen offset:2048\n\t" \
  "buffer_load_dword a131, v0, s[4:7], s22 offen offset:3072\n\t" \
  "buffer_load_dword a132, v1, s[4:7], s22 offen offset:0\n\t" \
  "buffer_load_dword a133, v1, s[4:7], s22 offen offset:1024\n\t" \
  "buffer_load_dword a134, v1, s[4:7], s22 offen offset:2048\n\t" \
  "buffer_load_dword a135, v1, s[4:7], s22 offen offset:3072\n\t" \
  "s_waitcnt vmcnt(48)\n\t" \
  "v_accvgpr_read_b32 v8, a80\n\t" \
  "v_accvgpr_read_b32 v9, a81\n\t" \
  "v_accvgpr_read_b32 v10, a82\n\t" \
  "v_accvgpr_read_b32 v11, a83\n\t" \
  "v_accvgpr_read_b32 v12, a84\n\t" \
  "v_accvgpr_read_b32 v13, a85\n\t" \
  "v_accvgpr_read_b32 v14, a86\n\t" \
  "v_accvgpr_read_b32 v15, a87\n\t" \
  "v_perm_b32 v16, v12, v8, s23\n\t" \
  "v_perm_b32 v20, v12, v8, s24\n\t" \
  "v_perm_b32 v17, v13, v9, s23\n\t" \
  "v_perm_b32 v21, v13, v9, s24\n\t" \
  "v_perm_b32 v18, v14, v10, s23\n\t" \
  "v_perm_b32 v22, v14, v10, s24\n\t" \
  "v_perm_b32 v19, v15, v11, s23\n\t" \
  "v_perm_b32 v23, v15, v11, s24\n\t" \
  "v_accvgpr_write_b32 a80, v16\n\t" \
  "v_accvgpr_write_b32 a81, v17\n\t" \
  "v_accvgpr_write_b32 a82, v18\n\t" \
  "v_accvgpr_write_b32 a83, v19\n\t" \
  "v_accvgpr_write_b32 a84, v20\n\t" \
  "v_accvgpr_write_b32 a85, v21\n\t" \
  "v_accvgpr_write_b32 a86, v22\n\t" \
  "v_accvgpr_write_b32 a87, v23\n\t" \
  "s_add_u32 s22, s12, 557056\n\t" \
  "s_and_b32 s22, s22, 0xfffff\n\t" \
  "buffer_load_dword a136, v0, s[4:7], s22 offen offset:0\n\t" \
  "buffer_load_dword a137, v0, s[4:7], s22 offen offset:1024\n\t" \
  "buffer_load_dword a138, v0, s[4:7], s22 offen offset:2048\n\t" \
  "buffer_load_dword a139, v0, s[4:7], s22 offen offset:3072\n\t" \
  "buffer_load_dword a140, v1, s[4:7], s22 offen offset:0\n\t" \
  "buffer_load_dword a141, v1, s[4:7], s22 offen offset:1024\n\t" \
  "buffer_load_dword a142, v1, s[4:7], s22 offen offset:2048\n\t" \
  "buffer_load_dword a143, v1, s[4:7], s22 offen offset:3072\n\t" \
  "s_waitcnt vmcnt(48)\n\t" \
  "v_accvgpr_read_b32 v8, a88\n\t" \
  "v_accvgpr_read_b32 v9, a89\n\t" \
  "v_accvgpr_read_b32 v10, a90\n\t" \
  "v_accvgpr_read_b32 v11, a91\n\t" \
  "v_accvgpr_read_b32 v12, a92\n\t" \
  "v_accvgpr_read_b32 v13, a93\n\t" \
  "v_accvgpr_read_b32 v14, a94\n\t" \
  "v_accvgpr_read_b32 v15, a95\n\t" \
  "v_perm_b32 v16, v12, v8, s23\n\t" \
  "v_perm_b32 v20, v12, v8, s24\n\t" \
  "v_perm_b32 v17, v13, v9, s23\n\t" \
  "v_perm_b32 v21, v13, v9, s24\n\t" \
  "v_perm_b32 v18, v14, v10, s23\n\t" \
  "v_perm_b32 v22, v14, v10, s24\n\t" \
  "v_perm_b32 v19, v15, v11, s23\n\t" \
  "v_perm_b32 v23, v15, v11, s24\n\t" \
  "v_accvgpr_write_b32 a88, v16\n\t" \
  "v_accvgpr_write_b32 a89, v17\n\t" \
  "v_accvgpr_write_b32 a90, v18\n\t" \
  "v_accvgpr_write_b32 a91, v19\n\t" \
  "v_accvgpr_write_b32 a92, v20\n\t" \
  "v_accvgpr_write_b32 a93, v21\n\t" \
  "v_accvgpr_write_b32 a94, v22\n\t" \
  "v_accvgpr_write_b32 a95, v23\n\t" \
  "s_add_u32 s22, s12, 589824\n\t" \
  "s_and_b32 s22, s22, 0xfffff\n\t" \
  "buffer_load_dword a144, v0, s[4:7], s22 offen offset:0\n\t" \
  "buffer_load_dword a145, v0, s[4:7], s22 offen offset:1024\n\t" \
  "buffer_load_dword a146, v0, s[4:7], s22 offen offset:2048\n\t" \
  "buffer_load_dword a147, v0, s[4:7], s22 offen offset:3072\n\t" \
  "buffer_load_dword a148, v1, s[4:7], s22 offen offset:0\n\t" \
  "buffer_load_dword a149, v1, s[4:7], s22 offen offset:1024\n\t" \
  "buffer_load_dword a150, v1, s[4:7], s22 offen offset:2048\n\t" \
  "buffer_load_dword a151, v1, s[4:7], s22 offen offset:3072\n\t" \
  "s_waitcnt vmcnt(48)\n\t" \
  "v_accvgpr_read_b32 v8, a96\n\t" \
  "v_accvgpr_read_b32 v9, a97\n\t" \
  "v_accvgpr_read_b32 v10, a98\n\t" \
  "v_accvgpr_read_b32 v11, a99\n\t" \
  "v_accvgpr_read_b32 v12, a100\n\t" \
  "v_accvgpr_read_b32 v13, a101\n\t" \
  "v_accvgpr_read_b32 v14, a102\n\t" \
  "v_accvgpr_read_b32 v15, a103\n\t" \
  "v_perm_b32 v16, v12, v8, s23\n\t" \
  "v_perm_b32 v20, v12, v8, s24\n\t" \
  "v_perm_b32 v17, v13, v9, s23\n\t" \
  "v_perm_b32 v21, v13, v9, s24\n\t" \
  "v_perm_b32 v18, v14, v10, s23\n\t" \
  "v_perm_b32 v22, v14, v10, s24\n\t" \
  "v_perm_b32 v19, v15, v11, s23\n\t" \
  "v_perm_b32 v23, v15, v11, s24\n\t" \
  "v_accvgpr_write_b32 a96, v16\n\t" \
  "v_accvgpr_write_b32 a97, v17\n\t" \
  "v_accvgpr_write_b32 a98, v18\n\t" \
  "v_accvgpr_write_b32 a99, v19\n\t" \
  "v_accvgpr_write_b32 a100, v20\n\t" \
  "v_accvgpr_write_b32 a101, v21\n\t" \
  "v_accvgpr_write_b32 a102, v22\n\t" \
  "v_accvgpr_write_b32 a103, v23\n\t" \
  "s_add_u32 s22, s12, 622592\n\t" \
  "s_and_b32 s22, s22, 0xfffff\n\t" \
  "buffer_load_dword a152, v0, s[4:7], s22 offen offset:0\n\t" \
  "buffer_load_dword a153, v0, s[4:7], s22 offen offset:1024\n\t" \
  "buffer_load_dword a154, v0, s[4:7], s22 offen offset:2048\n\t" \
  "buffer_load_dword a155, v0, s[4:7], s22 offen offset:3072\n\t" \
  "buffer_load_dword a156, v1, s[4:7], s22 offen offset:0\n\t" \
  "buffer_load_dword a157, v1, s[4:7], s22 offen offset:1024\n\t" \
  "buffer_load_dword a158, v1, s[4:7], s22 offen offset:2048\n\t" \
  "buffer_load_dword a159, v1, s[4:7], s22 offen offset:3072\n\t" \
  "s_waitcnt vmcnt(48)\n\t" \
  "v_accvgpr_read_b32 v8, a104\n\t" \
  "v_accvgpr_read_b32 v9, a105\n\t" \
  "v_accvgpr_read_b32 v10, a106\n\t" \
  "v_accvgpr_read_b32 v11, a107\n\t" \
  "v_accvgpr_read_b32 v12, a108\n\t" \
  "v_accvgpr_read_b32 v13, a109\n\t" \
  "v_accvgpr_read_b32 v14, a110\n\t" \
  "v_accvgpr_read_b32 v15, a111\n\t" \
  "v_perm_b32 v16, v12, v8, s23\n\t" \
  "v_perm_b32 v20, v12, v8, s24\n\t" \
  "v_perm_b32 v17, v13, v9, s23\n\t" \
  "v_perm_b32 v21, v13, v9, s24\n\t" \
  "v_perm_b32 v18, v14, v10, s23\n\t" \
  "v_perm_b32 v22, v14, v10, s24\n\t" \
  "v_perm_b32 v19, v15, v11, s23\n\t" \
  "v_perm_b32 v23, v15, v11, s24\n\t" \
  "v_accvgpr_write_b32 a104, v16\n\t" \
  "v_accvgpr_write_b32 a105, v17\n\t" \
  "v_accvgpr_write_b32 a106, v18\n\t" \
  "v_accvgpr_write_b32 a107, v19\n\t" \
  "v_accvgpr_write_b32 a108, v20\n\t" \
  "v_accvgpr_write_b32 a109, v21\n\t" \
  "v_accvgpr_write_b32 a110, v22\n\t" \
  "v_accvgpr_write_b32 a111, v23\n\t" \
  "s_add_u32 s22, s12, 655360\n\t" \
  "s_and_b32 s22, s22, 0xfffff\n\t" \
  "buffer_load_dword a160, v0, s[4:7], s22 offen offset:0\n\t" \
  "buffer_load_dword a161, v0, s[4:7], s22 offen offset:1024\n\t" \
  "buffer_load_dword a162, v0, s[4:7], s22 offen offset:2048\n\t" \
  "buffer_load_dword a163, v0, s[4:7], s22 offen offset:3072\n\t" \
  "buffer_load_dword a164, v1, s[4:7], s22 offen offset:0\n\t" \
  "buffer_load_dword a165, v1, s[4:7], s22 offen offset:1024\n\t" \
  "buffer_load_dword a166, v1, s[4:7], s22 offen offset:2048\n\t" \
  "buffer_load_dword a167, v1, s[4:7], s22 offen offset:3072\n\t" \
  "s_waitcnt vmcnt(48)\n\t" \
  "v_accvgpr_read_b32 v8, a112\n\t" \
  "v_accvgpr_read_b32 v9, a113\n\t" \
  "v_accvgpr_read_b32 v10, a114\n\t" \
  "v_accvgpr_read_b32 v11, a115\n\t" \
  "v_accvgpr_read_b32 v12, a116\n\t" \
  "v_accvgpr_read_b32 v13, a117\n\t" \
  "v_accvgpr_read_b32 v14, a118\n\t" \
  "v_accvgpr_read_b32 v15, a119\n\t" \
  "v_perm_b32 v16, v12, v8, s23\n\t" \
  "v_perm_b32 v20, v12, v8, s24\n\t" \
  "v_perm_b32 v17, v13, v9, s23\n\t" \
  "v_perm_b32 v21, v13, v9, s24\n\t" \
  "v_perm_b32 v18, v14, v10, s23\n\t" \
  "v_perm_b32 v22, v14, v10, s24\n\t" \
  "v_perm_b32 v19, v15, v11, s23\n\t" \
  "v_perm_b32 v23, v15, v11, s24\n\t" \
  "v_accvgpr_write_b32 a112, v16\n\t" \
  "v_accvgpr_write_b32 a113, v17\n\t" \
  "v_accvgpr_write_b32 a114, v18\n\t" \
  "v_accvgpr_write_b32 a115, v19\n\t" \
  "v_accvgpr_write_b32 a116, v20\n\t" \
  "v_accvgpr_write_b32 a117, v21\n\t" \
  "v_accvgpr_write_b32 a118, v22\n\t" \
  "v_accvgpr_write_b32 a119, v23\n\t" \
  "s_add_u32 s22, s12, 688128\n\t" \
  "s_and_b32 s22, s22, 0xfffff\n\t" \
  "buffer_load_dword a168, v0, s[4:7], s22 offen offset:0\n\t" \
  "buffer_load_dword a169, v0, s[4:7], s22 offen offset:1024\n\t" \
  "buffer_load_dword a170, v0, s[4:7], s22 offen offset:2048\n\t" \
  "buffer_load_dword a171, v0, s[4:7], s22 offen offset:3072\n\t" \
  "buffer_load_dword a172, v1, s[4:7], s22 offen offset:0\n\t" \
  "buffer_load_dword a173, v1, s[4:7], s22 offen offset:1024\n\t" \
  "buffer_load_dword a174, v1, s[4:7], s22 offen offset:2048\n\t" \
  "buffer_load_dword a175, v1, s[4:7], s22 offen offset:3072\n\t" \
  "s_waitcnt vmcnt(48)\n\t" \
  "v_accvgpr_read_b32 v8, a120\n\t" \
  "v_accvgpr_read_b32 v9, a121\n\t" \
  "v_accvgpr_read_b32 v10, a122\n\t" \
  "v_accvgpr_read_b32 v11, a123\n\t" \
  "v_accvgpr_read_b32 v12, a124\n\t" \
  "v_accvgpr_read_b32 v13, a125\n\t" \
  "v_accvgpr_read_b32 v14, a126\n\t" \
  "v_accvgpr_read_b32 v15, a127\n\t" \
  "v_perm_b32 v16, v12, v8, s23\n\t" \
  "v_perm_b32 v20, v12, v8, s24\n\t" \
  "v_perm_b32 v17, v13, v9, s23\n\t" \
  "v_perm_b32 v21, v13, v9, s24\n\t" \
  "v_perm_b32 v18, v14, v10, s23\n\t" \
  "v_perm_b32 v22, v14, v10, s24\n\t" \
  "v_perm_b32 v19, v15, v11, s23\n\t" \
  "v_perm_b32 v23, v15, v11, s24\n\t" \
  "v_accvgpr_write_b32 a120, v16\n\t" \
  "v_accvgpr_write_b32 a121, v17\n\t" \
  "v_accvgpr_write_b32 a122, v18\n\t" \
  "v_accvgpr_write_b32 a123, v19\n\t" \
  "v_accvgpr_write_b32 a124, v20\n\t" \
  "v_accvgpr_write_b32 a125, v21\n\t" \
  "v_accvgpr_write_b32 a126, v22\n\t" \
  "v_accvgpr_write_b32 a127, v23\n\t" \
  "s_add_u32 s22, s12, 720896\n\t" \
  "s_and_b32 s22, s22, 0xfffff\n\t" \
  "buffer_load_dword a176, v0, s[4:7], s22 offen offset:0\n\t" \
  "buffer_load_dword a177, v0, s[4:7], s22 offen offset:1024\n\t" \
  "buffer_load_dword a178, v0, s[4:7], s22 offen offset:2048\n\t" \
  "buffer_load_dword a179, v0, s[4:7], s22 offen offset:3072\n\t" \
  "buffer_load_dword a180, v1, s[4:7], s22 offen offset:0\n\t" \
  "buffer_load_dword a181, v1, s[4:7], s22 offen offset:1024\n\t" \
  "buffer_load_dword a182, v1, s[4:7], s22 offen offset:2048\n\t" \
  "buffer_load_dword a183, v1, s[4:7], s22 offen offset:3072\n\t" \
  "s_waitcnt vmcnt(48)\n\t" \
  "v_accvgpr_read_b32 v8, a128\n\t" \
  "v_accvgpr_read_b32 v9, a129\n\t" \
  "v_accvgpr_read_b32 v10, a130\n\t" \
  "v_accvgpr_read_b32 v11, a131\n\t" \
  "v_accvgpr_read_b32 v12, a132\n\t" \
  "v_accvgpr_read_b32 v13, a133\n\t" \
  "v_accvgpr_read_b32 v14, a134\n\t" \
  "v_accvgpr_read_b32 v15, a135\n\t" \
  "v_perm_b32 v16, v12, v8, s23\n\t" \
  "v_perm_b32 v20, v12, v8, s24\n\t" \
  "v_perm_b32 v17, v13, v9, s23\n\t" \
  "v_perm_b32 v21, v13, v9, s24\n\t" \
  "v_perm_b32 v18, v14, v10, s23\n\t" \
  "v_perm_b32 v22, v14, v10, s24\n\t" \
  "v_perm_b32 v19, v15, v11, s23\n\t" \
  "v_perm_b32 v23, v15, v11, s24\n\t" \
  "v_accvgpr_write_b32 a128, v16\n\t" \
  "v_accvgpr_write_b32 a129, v17\n\t" \
  "v_accvgpr_write_b32 a130, v18\n\t" \
  "v_accvgpr_write_b32 a131, v19\n\t" \
  "v_accvgpr_write_b32 a132, v20\n\t" \
  "v_accvgpr_write_b32 a133, v21\n\t" \
  "v_accvgpr_write_b32 a134, v22\n\t" \
  "v_accvgpr_write_b32 a135, v23\n\t" \
  "s_add_u32 s22, s12, 753664\n\t" \
  "s_and_b32 s22, s22, 0xfffff\n\t" \
  "buffer_load_dword a184, v0, s[4:7], s22 offen offset:0\n\t" \
  "buffer_load_dword a185, v0, s[4:7], s22 offen offset:1024\n\t" \
  "buffer_load_dword a186, v0, s[4:7], s22 offen offset:2048\n\t" \
  "buffer_load_dword a187, v0, s[4:7], s22 offen offset:3072\n\t" \
  "buffer_load_dword a188, v1, s[4:7], s22 offen offset:0\n\t" \
  "buffer_load_dword a189, v1, s[4:7], s22 offen offset:1024\n\t" \
  "buffer_load_dword a190, v1, s[4:7], s22 offen offset:2048\n\t" \
  "buffer_load_dword a191, v1, s[4:7], s22 offen offset:3072\n\t" \
  "s_waitcnt vmcnt(48)\n\t" \
  "v_accvgpr_read_b32 v8, a136\n\t" \
  "v_accvgpr_read_b32 v9, a137\n\t" \
  "v_accvgpr_read_b32 v10, a138\n\t" \
  "v_accvgpr_read_b32 v11, a139\n\t" \
  "v_accvgpr_read_b32 v12, a140\n\t" \
  "v_accvgpr_read_b32 v13, a141\n\t" \
  "v_accvgpr_read_b32 v14, a142\n\t" \
  "v_accvgpr_read_b32 v15, a143\n\t" \
  "v_perm_b32 v16, v12, v8, s23\n\t" \
  "v_perm_b32 v20, v12, v8, s24\n\t" \
  "v_perm_b32 v17, v13, v9, s23\n\t" \
  "v_perm_b32 v21, v13, v9, s24\n\t" \
  "v_perm_b32 v18, v14, v10, s23\n\t" \
  "v_perm_b32 v22, v14, v10, s24\n\t" \
  "v_perm_b32 v19, v15, v11, s23\n\t" \
  "v_perm_b32 v23, v15, v11, s24\n\t" \
  "v_accvgpr_write_b32 a136, v16\n\t" \
  "v_accvgpr_write_b32 a137, v17\n\t" \
  "v_accvgpr_write_b32 a138, v18\n\t" \
  "v_accvgpr_write_b32 a139, v19\n\t" \
  "v_accvgpr_write_b32 a140, v20\n\t" \
  "v_accvgpr_write_b32 a141, v21\n\t" \
  "v_accvgpr_write_b32 a142, v22\n\t" \
  "v_accvgpr_write_b32 a143, v23\n\t" \
  "s_add_u32 s22, s12, 786432\n\t" \
  "s_and_b32 s22, s22, 0xfffff\n\t" \
  "buffer_load_dword a192, v0, s[4:7], s22 offen offset:0\n\t" \
  "buffer_load_dword a193, v0, s[4:7], s22 offen offset:1024\n\t" \
  "buffer_load_dword a194, v0, s[4:7], s22 offen offset:2048\n\t" \
  "buffer_load_dword a195, v0, s[4:7], s22 offen offset:3072\n\t" \
  "buffer_load_dword a196, v1, s[4:7], s22 offen offset:0\n\t" \
  "buffer_load_dword a197, v1, s[4:7], s22 offen offset:1024\n\t" \
  "buffer_load_dword a198, v1, s[4:7], s22 offen offset:2048\n\t" \
  "buffer_load_dword a199, v1, s[4:7], s22 offen offset:3072\n\t" \
  "s_waitcnt vmcnt(48)\n\t" \
  "v_accvgpr_read_b32 v8, a144\n\t" \
  "v_accvgpr_read_b32 v9, a145\n\t" \
  "v_accvgpr_read_b32 v10, a146\n\t" \
  "v_accvgpr_read_b32 v11, a147\n\t" \
  "v_accvgpr_read_b32 v12, a148\n\t" \
  "v_accvgpr_read_b32 v13, a149\n\t" \
  "v_accvgpr_read_b32 v14, a150\n\t" \
  "v_accvgpr_read_b32 v15, a151\n\t" \
  "v_perm_b32 v16, v12, v8, s23\n\t" \
  "v_perm_b32 v20, v12, v8, s24\n\t" \
  "v_perm_b32 v17, v13, v9, s23\n\t" \
  "v_perm_b32 v21, v13, v9, s24\n\t" \
  "v_perm_b32 v18, v14, v10, s23\n\t" \
  "v_perm_b32 v22, v14, v10, s24\n\t" \
  "v_perm_b32 v19, v15, v11, s23\n\t" \
  "v_perm_b32 v23, v15, v11, s24\n\t" \
  "v_accvgpr_write_b32 a144, v16\n\t" \
  "v_accvgpr_write_b32 a145, v17\n\t" \
  "v_accvgpr_write_b32 a146, v18\n\t" \
  "v_accvgpr_write_b32 a147, v19\n\t" \
  "v_accvgpr_write_b32 a148, v20\n\t" \
  "v_accvgpr_write_b32 a149, v21\n\t" \
  "v_accvgpr_write_b32 a150, v22\n\t" \
  "v_accvgpr_write_b32 a151, v23\n\t" \
  "s_add_u32 s22, s12, 819200\n\t" \
  "s_and_b32 s22, s22, 0xfffff\n\t" \
  "buffer_load_dword a200, v0, s[4:7], s22 offen offset:0\n\t" \
  "buffer_load_dword a201, v0, s[4:7], s22 offen offset:1024\n\t" \
  "buffer_load_dword a202, v0, s[4:7], s22 offen offset:2048\n\t" \
  "buffer_load_dword a203, v0, s[4:7], s22 offen offset:3072\n\t" \
  "buffer_load_dword a204, v1, s[4:7], s22 offen offset:0\n\t" \
  "buffer_load_dword a205, v1, s[4:7], s22 offen offset:1024\n\t" \
  "buffer_load_dword a206, v1, s[4:7], s22 offen offset:2048\n\t" \
  "buffer_load_dword a207, v1, s[4:7], s22 offen offset:3072\n\t" \
  "s_waitcnt vmcnt(48)\n\t" \
  "v_accvgpr_read_b32 v8, a152\n\t" \
  "v_accvgpr_read_b32 v9, a153\n\t" \
  "v_accvgpr_read_b32 v10, a154\n\t" \
  "v_accvgpr_read_b32 v11, a155\n\t" \
  "v_accvgpr_read_b32 v12, a156\n\t" \
  "v_accvgpr_read_b32 v13, a157\n\t" \
  "v_accvgpr_read_b32 v14, a158\n\t" \
  "v_accvgpr_read_b32 v15, a159\n\t" \
  "v_perm_b32 v16, v12, v8, s23\n\t" \
  "v_perm_b32 v20, v12, v8, s24\n\t" \
  "v_perm_b32 v17, v13, v9, s23\n\t" \
  "v_perm_b32 v21, v13, v9, s24\n\t" \
  "v_perm_b32 v18, v14, v10, s23\n\t" \
  "v_perm_b32 v22, v14, v10, s24\n\t" \
  "v_perm_b32 v19, v15, v11, s23\n\t" \
  "v_perm_b32 v23, v15, v11, s24\n\t" \
  "v_accvgpr_write_b32 a152, v16\n\t" \
  "v_accvgpr_write_b32 a153, v17\n\t" \
  "v_accvgpr_write_b32 a154, v18\n\t" \
  "v_accvgpr_write_b32 a155, v19\n\t" \
  "v_accvgpr_write_b32 a156, v20\n\t" \
  "v_accvgpr_write_b32 a157, v21\n\t" \
  "v_accvgpr_write_b32 a158, v22\n\t" \
  "v_accvgpr_write_b32 a159, v23\n\t" \
  "s_add_u32 s22, s12, 851968\n\t" \
  "s_and_b32 s22, s22, 0xfffff\n\t" \
  "buffer_load_dword a208, v0, s[4:7], s22 offen offset:0\n\t" \
  "buffer_load_dword a209, v0, s[4:7], s22 offen offset:1024\n\t" \
  "buffer_load_dword a210, v0, s[4:7], s22 offen offset:2048\n\t" \
  "buffer_load_dword a211, v0, s[4:7], s22 offen offset:3072\n\t" \
  "buffer_load_dword a212, v1, s[4:7], s22 offen offset:0\n\t" \
  "buffer_load_dword a213, v1, s[4:7], s22 offen offset:1024\n\t" \
  "buffer_load_dword a214, v1, s[4:7], s22 offen offset:2048\n\t" \
  "buffer_load_dword a215, v1, s[4:7], s22 offen offset:3072\n\t" \
  "s_waitcnt vmcnt(48)\n\t" \
  "v_accvgpr_read_b32 v8, a160\n\t" \
  "v_accvgpr_read_b32 v9, a161\n\t" \
  "v_accvgpr_read_b32 v10, a162\n\t" \
  "v_accvgpr_read_b32 v11, a163\n\t" \
  "v_accvgpr_read_b32 v12, a164\n\t" \
  "v_accvgpr_read_b32 v13, a165\n\t" \
  "v_accvgpr_read_b32 v14, a166\n\t" \
  "v_accvgpr_read_b32 v15, a167\n\t" \
  "v_perm_b32 v16, v12, v8, s23\n\t" \
  "v_perm_b32 v20, v12, v8, s24\n\t" \
  "v_perm_b32 v17, v13, v9, s23\n\t" \
  "v_perm_b32 v21, v13, v9, s24\n\t" \
  "v_perm_b32 v18, v14, v10, s23\n\t" \
  "v_perm_b32 v22, v14, v10, s24\n\t" \
  "v_perm_b32 v19, v15, v11, s23\n\t" \
  "v_perm_b32 v23, v15, v11, s24\n\t" \
  "v_accvgpr_write_b32 a160, v16\n\t" \
  "v_accvgpr_write_b32 a161, v17\n\t" \
  "v_accvgpr_write_b32 a162, v18\n\t" \
  "v_accvgpr_write_b32 a163, v19\n\t" \
  "v_accvgpr_write_b32 a164, v20\n\t" \
  "v_accvgpr_write_b32 a165, v21\n\t" \
  "v_accvgpr_write_b32 a166, v22\n\t" \
  "v_accvgpr_write_b32 a167, v23\n\t" \
  "s_add_u32 s22, s12, 884736\n\t" \
  "s_and_b32 s22, s22, 0xfffff\n\t" \
  "buffer_load_dword a216, v0, s[4:7], s22 offen offset:0\n\t" \
  "buffer_load_dword a217, v0, s[4:7], s22 offen offset:1024\n\t" \
  "buffer_load_dword a218, v0, s[4:7], s22 offen offset:2048\n\t" \
  "buffer_load_dword a219, v0, s[4:7], s22 offen offset:3072\n\t" \
  "buffer_load_dword a220, v1, s[4:7], s22 offen offset:0\n\t" \
  "buffer_load_dword a221, v1, s[4:7], s22 offen offset:1024\n\t" \
  "buffer_load_dword a222, v1, s[4:7], s22 offen offset:2048\n\t" \
  "buffer_load_dword a223, v1, s[4:7], s22 offen offset:3072\n\t" \
  "s_waitcnt vmcnt(48)\n\t" \
  "v_accvgpr_read_b32 v8, a168\n\t" \
  "v_accvgpr_read_b32 v9, a169\n\t" \
  "v_accvgpr_read_b32 v10, a170\n\t" \
  "v_accvgpr_read_b32 v11, a171\n\t" \
  "v_accvgpr_read_b32 v12, a172\n\t" \
  "v_accvgpr_read_b32 v13, a173\n\t" \
  "v_accvgpr_read_b32 v14, a174\n\t" \
  "v_accvgpr_read_b32 v15, a175\n\t" \
  "v_perm_b32 v16, v12, v8, s23\n\t" \
  "v_perm_b32 v20, v12, v8, s24\n\t" \
  "v_perm_b32 v17, v13, v9, s23\n\t" \
  "v_perm_b32 v21, v13, v9, s24\n\t" \
  "v_perm_b32 v18, v14, v10, s23\n\t" \
  "v_perm_b32 v22, v14, v10, s24\n\t" \
  "v_perm_b32 v19, v15, v11, s23\n\t" \
  "v_perm_b32 v23, v15, v11, s24\n\t" \
  "v_accvgpr_write_b32 a168, v16\n\t" \
  "v_accvgpr_write_b32 a169, v17\n\t" \
  "v_accvgpr_write_b32 a170, v18\n\t" \
  "v_accvgpr_write_b32 a171, v19\n\t" \
  "v_accvgpr_write_b32 a172, v20\n\t" \
  "v_accvgpr_write_b32 a173, v21\n\t" \
  "v_accvgpr_write_b32 a174, v22\n\t" \
  "v_accvgpr_write_b32 a175, v23\n\t" \
  "s_add_u32 s22, s12, 917504\n\t" \
  "s_and_b32 s22, s22, 0xfffff\n\t" \
  "buffer_load_dword a224, v0, s[4:7], s22 offen offset:0\n\t" \
  "buffer_load_dword a225, v0, s[4:7], s22 offen offset:1024\n\t" \
  "buffer_load_dword a226, v0, s[4:7], s22 offen offset:2048\n\t" \
  "buffer_load_dword a227, v0, s[4:7], s22 offen offset:3072\n\t" \
  "buffer_load_dword a228, v1, s[4:7], s22 offen offset:0\n\t" \
  "buffer_load_dword a229, v1, s[4:7], s22 offen offset:1024\n\t" \
  "buffer_load_dword a230, v1, s[4:7], s22 offen offset:2048\n\t" \
  "buffer_load_dword a231, v1, s[4:7], s22 offen offset:3072\n\t" \
  "s_waitcnt vmcnt(48)\n\t" \
  "v_accvgpr_read_b32 v8, a176\n\t" \
  "v_accvgpr_read_b32 v9, a177\n\t" \
  "v_accvgpr_read_b32 v10, a178\n\t" \
  "v_accvgpr_read_b32 v11, a179\n\t" \
  "v_accvgpr_read_b32 v12, a180\n\t" \
  "v_accvgpr_read_b32 v13, a181\n\t" \
  "v_accvgpr_read_b32 v14, a182\n\t" \
  "v_accvgpr_read_b32 v15, a183\n\t" \
  "v_perm_b32 v16, v12, v8, s23\n\t" \
  "v_perm_b32 v20, v12, v8, s24\n\t" \
  "v_perm_b32 v17, v13, v9, s23\n\t" \
  "v_perm_b32 v21, v13, v9, s24\n\t" \
  "v_perm_b32 v18, v14, v10, s23\n\t" \
  "v_perm_b32 v22, v14, v10, s24\n\t" \
  "v_perm_b32 v19, v15, v11, s23\n\t" \
  "v_perm_b32 v23, v15, v11, s24\n\t" \
  "v_accvgpr_write_b32 a176, v16\n\t" \
  "v_accvgpr_write_b32 a177, v17\n\t" \
  "v_accvgpr_write_b32 a178, v18\n\t" \
  "v_accvgpr_write_b32 a179, v19\n\t" \
  "v_accvgpr_write_b32 a180, v20\n\t" \
  "v_accvgpr_write_b32 a181, v21\n\t" \
  "v_accvgpr_write_b32 a182, v22\n\t" \
  "v_accvgpr_write_b32 a183, v23\n\t" \
  "s_add_u32 s22, s12, 950272\n\t" \
  "s_and_b32 s22, s22, 0xfffff\n\t" \
  "buffer_load_dword a232, v0, s[4:7], s22 offen offset:0\n\t" \
  "buffer_load_dword a233, v0, s[4:7], s22 offen offset:1024\n\t" \
  "buffer_load_dword a234, v0, s[4:7], s22 offen offset:2048\n\t" \
  "buffer_load_dword a235, v0, s[4:7], s22 offen offset:3072\n\t" \
  "buffer_load_dword a236, v1, s[4:7], s22 offen offset:0\n\t" \
  "buffer_load_dword a237, v1, s[4:7], s22 offen offset:1024\n\t" \
  "buffer_load_dword a238, v1, s[4:7], s22 offen offset:2048\n\t" \
  "buffer_load_dword a239, v1, s[4:7], s22 offen offset:3072\n\t" \
  "s_waitcnt vmcnt(48)\n\t" \
  "v_accvgpr_read_b32 v8, a184\n\t" \
  "v_accvgpr_read_b32 v9, a185\n\t" \
  "v_accvgpr_read_b32 v10, a186\n\t" \
  "v_accvgpr_read_b32 v11, a187\n\t" \
  "v_accvgpr_read_b32 v12, a188\n\t" \
  "v_accvgpr_read_b32 v13, a189\n\t" \
  "v_accvgpr_read_b32 v14, a190\n\t" \
  "v_accvgpr_read_b32 v15, a191\n\t" \
  "v_perm_b32 v16, v12, v8, s23\n\t" \
  "v_perm_b32 v20, v12, v8, s24\n\t" \
  "v_perm_b32 v17, v13, v9, s23\n\t" \
  "v_perm_b32 v21, v13, v9, s24\n\t" \
  "v_perm_b32 v18, v14, v10, s23\n\t" \
  "v_perm_b32 v22, v14, v10, s24\n\t" \
  "v_perm_b32 v19, v15, v11, s23\n\t" \
  "v_perm_b32 v23, v15, v11, s24\n\t" \
  "v_accvgpr_write_b32 a184, v16\n\t" \
  "v_accvgpr_write_b32 a185, v17\n\t" \
  "v_accvgpr_write_b32 a186, v18\n\t" \
  "v_accvgpr_write_b32 a187, v19\n\t" \
  "v_accvgpr_write_b32 a188, v20\n\t" \
  "v_accvgpr_write_b32 a189, v21\n\t" \
  "v_accvgpr_write_b32 a190, v22\n\t" \
  "v_accvgpr_write_b32 a191, v23\n\t" \
  "s_add_u32 s22, s12, 983040\n\t" \
  "s_and_b32 s22, s22, 0xfffff\n\t" \
  "buffer_load_dword a240, v0, s[4:7], s22 offen offset:0\n\t" \
  "buffer_load_dword a241, v0, s[4:7], s22 offen offset:1024\n\t" \
  "buffer_load_dword a242, v0, s[4:7], s22 offen offset:2048\n\t" \
  "buffer_load_dword a243, v0, s[4:7], s22 offen offset:3072\n\t" \
  "buffer_load_dword a244, v1, s[4:7], s22 offen offset:0\n\t" \
  "buffer_load_dword a245, v1, s[4:7], s22 offen offset:1024\n\t" \
  "buffer_load_dword a246, v1, s[4:7], s22 offen offset:2048\n\t" \
  "buffer_load_dword a247, v1, s[4:7], s22 offen offset:3072\n\t" \
  "s_waitcnt vmcnt(48)\n\t" \
  "v_accvgpr_read_b32 v8, a192\n\t" \
  "v_accvgpr_read_b32 v9, a193\n\t" \
  "v_accvgpr_read_b32 v10, a194\n\t" \
  "v_accvgpr_read_b32 v11, a195\n\t" \
  "v_accvgpr_read_b32 v12, a196\n\t" \
  "v_accvgpr_read_b32 v13, a197\n\t" \
  "v_accvgpr_read_b32 v14, a198\n\t" \
  "v_accvgpr_read_b32 v15, a199\n\t" \
  "v_perm_b32 v16, v12, v8, s23\n\t" \
  "v_perm_b32 v20, v12, v8, s24\n\t" \
  "v_perm_b32 v17, v13, v9, s23\n\t" \
  "v_perm_b32 v21, v13, v9, s24\n\t" \
  "v_perm_b32 v18, v14, v10, s23\n\t" \
  "v_perm_b32 v22, v14, v10, s24\n\t" \
  "v_perm_b32 v19, v15, v11, s23\n\t" \
  "v_perm_b32 v23, v15, v11, s24\n\t" \
  "v_accvgpr_write_b32 a192, v16\n\t" \
  "v_accvgpr_write_b32 a193, v17\n\t" \
  "v_accvgpr_write_b32 a194, v18\n\t" \
  "v_accvgpr_write_b32 a195, v19\n\t" \
  "v_accvgpr_write_b32 a196, v20\n\t" \
  "v_accvgpr_write_b32 a197, v21\n\t" \
  "v_accvgpr_write_b32 a198, v22\n\t" \
  "v_accvgpr_write_b32 a199, v23\n\t" \
  "s_add_u32 s22, s12, 1015808\n\t" \
  "s_and_b32 s22, s22, 0xfffff\n\t" \
  "buffer_load_dword a248, v0, s[4:7], s22 offen offset:0\n\t" \
  "buffer_load_dword a249, v0, s[4:7], s22 offen offset:1024\n\t" \
  "buffer_load_dword a250, v0, s[4:7], s22 offen offset:2048\n\t" \
  "buffer_load_dword a251, v0, s[4:7], s22 offen offset:3072\n\t" \
  "buffer_load_dword a252, v1, s[4:7], s22 offen offset:0\n\t" \
  "buffer_load_dword a253, v1, s[4:7], s22 offen offset:1024\n\t" \
  "buffer_load_dword a254, v1, s[4:7], s22 offen offset:2048\n\t" \
  "buffer_load_dword a255, v1, s[4:7], s22 offen offset:3072\n\t" \
  "s_waitcnt vmcnt(48)\n\t" \
  "v_accvgpr_read_b32 v8, a200\n\t" \
  "v_accvgpr_read_b32 v9, a201\n\t" \
  "v_accvgpr_read_b32 v10, a202\n\t" \
  "v_accvgpr_read_b32 v11, a203\n\t" \
  "v_accvgpr_read_b32 v12, a204\n\t" \
  "v_accvgpr_read_b32 v13, a205\n\t" \
  "v_accvgpr_read_b32 v14, a206\n\t" \
  "v_accvgpr_read_b32 v15, a207\n\t" \
  "v_perm_b32 v16, v12, v8, s23\n\t" \
  "v_perm_b32 v20, v12, v8, s24\n\t" \
  "v_perm_b32 v17, v13, v9, s23\n\t" \
  "v_perm_b32 v21, v13, v9, s24\n\t" \
  "v_perm_b32 v18, v14, v10, s23\n\t" \
  "v_perm_b32 v22, v14, v10, s24\n\t" \
  "v_perm_b32 v19, v15, v11, s23\n\t" \
  "v_perm_b32 v23, v15, v11, s24\n\t" \
  "v_accvgpr_write_b32 a200, v16\n\t" \
  "v_accvgpr_write_b32 a201, v17\n\t" \
  "v_accvgpr_write_b32 a202, v18\n\t" \
  "v_accvgpr_write_b32 a203, v19\n\t" \
  "v_accvgpr_write_b32 a204, v20\n\t" \
  "v_accvgpr_write_b32 a205, v21\n\t" \
  "v_accvgpr_write_b32 a206, v22\n\t" \
  "v_accvgpr_write_b32 a207, v23\n\t" \
  "s_mov_b32 s22, 0\n\t" \
  "buffer_load_dword v160, v2, s[8:11], s22 offen offset:0\n\t" \
  "buffer_load_dword v161, v2, s[8:11], s22 offen offset:1024\n\t" \
  "buffer_load_dword v162, v2, s[8:11], s22 offen offset:2048\n\t" \
  "buffer_load_dword v163, v2, s[8:11], s22 offen offset:3072\n\t" \
  "buffer_load_dword v164, v3, s[8:11], s22 offen offset:0\n\t" \
  "buffer_load_dword v165, v3, s[8:11], s22 offen offset:1024\n\t" \
  "buffer_load_dword v166, v3, s[8:11], s22 offen offset:2048\n\t" \
  "buffer_load_dword v167, v3, s[8:11], s22 offen offset:3072\n\t" \
  "s_waitcnt vmcnt(48)\n\t" \
  "v_accvgpr_read_b32 v8, a208\n\t" \
  "v_accvgpr_read_b32 v9, a209\n\t" \
  "v_accvgpr_read_b32 v10, a210\n\t" \
  "v_accvgpr_read_b32 v11, a211\n\t" \
  "v_accvgpr_read_b32 v12, a212\n\t" \
  "v_accvgpr_read_b32 v13, a213\n\t" \
  "v_accvgpr_read_b32 v14, a214\n\t" \
  "v_accvgpr_read_b32 v15, a215\n\t" \
  "v_perm_b32 v16, v12, v8, s23\n\t" \
  "v_perm_b32 v20, v12, v8, s24\n\t" \
  "v_perm_b32 v17, v13, v9, s23\n\t" \
  "v_perm_b32 v21, v13, v9, s24\n\t" \
  "v_perm_b32 v18, v14, v10, s23\n\t" \
  "v_perm_b32 v22, v14, v10, s24\n\t" \
  "v_perm_b32 v19, v15, v11, s23\n\t" \
  "v_perm_b32 v23, v15, v11, s24\n\t" \
  "v_accvgpr_write_b32 a208, v16\n\t" \
  "v_accvgpr_write_b32 a209, v17\n\t" \
  "v_accvgpr_write_b32 a210, v18\n\t" \
  "v_accvgpr_write_b32 a211, v19\n\t" \
  "v_accvgpr_write_b32 a212, v20\n\t" \
  "v_accvgpr_write_b32 a213, v21\n\t" \
  "v_accvgpr_write_b32 a214, v22\n\t" \
  "v_accvgpr_write_b32 a215, v23\n\t" \
  "s_mov_b32 s22, 32768\n\t" \
  "buffer_load_dword v168, v2, s[8:11], s22 offen offset:0\n\t" \
  "buffer_load_dword v169, v2, s[8:11], s22 offen offset:1024\n\t" \
  "buffer_load_dword v170, v2, s[8:11], s22 offen offset:2048\n\t" \
  "buffer_load_dword v171, v2, s[8:11], s22 offen offset:3072\n\t" \
  "buffer_load_dword v172, v3, s[8:11], s22 offen offset:0\n\t" \
  "buffer_load_dword v173, v3, s[8:11], s22 offen offset:1024\n\t" \
  "buffer_load_dword v174, v3, s[8:11], s22 offen offset:2048\n\t" \
  "buffer_load_dword v175, v3, s[8:11], s22 offen offset:3072\n\t" \
  "s_waitcnt vmcnt(48)\n\t" \
  "v_accvgpr_read_b32 v8, a216\n\t" \
  "v_accvgpr_read_b32 v9, a217\n\t" \
  "v_accvgpr_read_b32 v10, a218\n\t" \
  "v_accvgpr_read_b32 v11, a219\n\t" \
  "v_accvgpr_read_b32 v12, a220\n\t" \
  "v_accvgpr_read_b32 v13, a221\n\t" \
  "v_accvgpr_read_b32 v14, a222\n\t" \
  "v_accvgpr_read_b32 v15, a223\n\t" \
  "v_perm_b32 v16, v12, v8, s23\n\t" \
  "v_perm_b32 v20, v12, v8, s24\n\t" \
  "v_perm_b32 v17, v13, v9, s23\n\t" \
  "v_perm_b32 v21, v13, v9, s24\n\t" \
  "v_perm_b32 v18, v14, v10, s23\n\t" \
  "v_perm_b32 v22, v14, v10, s24\n\t" \
  "v_perm_b32 v19, v15, v11, s23\n\t" \
  "v_perm_b32 v23, v15, v11, s24\n\t" \
  "v_accvgpr_write_b32 a216, v16\n\t" \
  "v_accvgpr_write_b32 a217, v17\n\t" \
  "v_accvgpr_write_b32 a218, v18\n\t" \
  "v_accvgpr_write_b32 a219, v19\n\t" \
  "v_accvgpr_write_b32 a220, v20\n\t" \
  "v_accvgpr_write_b32 a221, v21\n\t" \
  "v_accvgpr_write_b32 a222, v22\n\t" \
  "v_accvgpr_write_b32 a223, v23\n\t" \
  "s_mov_b32 s22, 65536\n\t" \
  "buffer_load_dword v176, v2, s[8:11], s22 offen offset:0\n\t" \
  "buffer_load_dword v177, v2, s[8:11], s22 offen offset:1024\n\t" \
  "buffer_load_dword v178, v2, s[8:11], s22 offen offset:2048\n\t" \
  "buffer_load_dword v179, v2, s[8:11], s22 offen offset:3072\n\t" \
  "buffer_load_dword v180, v3, s[8:11], s22 offen offset:0\n\t" \
  "buffer_load_dword v181, v3, s[8:11], s22 offen offset:1024\n\t" \
  "buffer_load_dword v182, v3, s[8:11], s22 offen offset:2048\n\t" \
  "buffer_load_dword v183, v3, s[8:11], s22 offen offset:3072\n\t" \
  "s_waitcnt vmcnt(48)\n\t" \
  "v_accvgpr_read_b32 v8, a224\n\t" \
  "v_accvgpr_read_b32 v9, a225\n\t" \
  "v_accvgpr_read_b32 v10, a226\n\t" \
  "v_accvgpr_read_b32 v11, a227\n\t" \
  "v_accvgpr_read_b32 v12, a228\n\t" \
  "v_accvgpr_read_b32 v13, a229\n\t" \
  "v_accvgpr_read_b32 v14, a230\n\t" \
  "v_accvgpr_read_b32 v15, a231\n\t" \
  "v_perm_b32 v16, v12, v8, s23\n\t" \
  "v_perm_b32 v20, v12, v8, s24\n\t" \
  "v_perm_b32 v17, v13, v9, s23\n\t" \
  "v_perm_b32 v21, v13, v9, s24\n\t" \
  "v_perm_b32 v18, v14, v10, s23\n\t" \
  "v_perm_b32 v22, v14, v10, s24\n\t" \
  "v_perm_b32 v19, v15, v11, s23\n\t" \
  "v_perm_b32 v23, v15, v11, s24\n\t" \
  "v_accvgpr_write_b32 a224, v16\n\t" \
  "v_accvgpr_write_b32 a225, v17\n\t" \
  "v_accvgpr_write_b32 a226, v18\n\t" \
  "v_accvgpr_write_b32 a227, v19\n\t" \
  "v_accvgpr_write_b32 a228, v20\n\t" \
  "v_accvgpr_write_b32 a229, v21\n\t" \
  "v_accvgpr_write_b32 a230, v22\n\t" \
  "v_accvgpr_write_b32 a231, v23\n\t" \
  "s_mov_b32 s22, 98304\n\t" \
  "buffer_load_dword v184, v2, s[8:11], s22 offen offset:0\n\t" \
  "buffer_load_dword v185, v2, s[8:11], s22 offen offset:1024\n\t" \
  "buffer_load_dword v186, v2, s[8:11], s22 offen offset:2048\n\t" \
  "buffer_load_dword v187, v2, s[8:11], s22 offen offset:3072\n\t" \
  "buffer_load_dword v188, v3, s[8:11], s22 offen offset:0\n\t" \
  "buffer_load_dword v189, v3, s[8:11], s22 offen offset:1024\n\t" \
  "buffer_load_dword v190, v3, s[8:11], s22 offen offset:2048\n\t" \
  "buffer_load_dword v191, v3, s[8:11], s22 offen offset:3072\n\t" \
  "s_waitcnt vmcnt(48)\n\t" \
  "v_accvgpr_read_b32 v8, a232\n\t" \
  "v_accvgpr_read_b32 v9, a233\n\t" \
  "v_accvgpr_read_b32 v10, a234\n\t" \
  "v_accvgpr_read_b32 v11, a235\n\t" \
  "v_accvgpr_read_b32 v12, a236\n\t" \
  "v_accvgpr_read_b32 v13, a237\n\t" \
  "v_accvgpr_read_b32 v14, a238\n\t" \
  "v_accvgpr_read_b32 v15, a239\n\t" \
  "v_perm_b32 v16, v12, v8, s23\n\t" \
  "v_perm_b32 v20, v12, v8, s24\n\t" \
  "v_perm_b32 v17, v13, v9, s23\n\t" \
  "v_perm_b32 v21, v13, v9, s24\n\t" \
  "v_perm_b32 v18, v14, v10, s23\n\t" \
  "v_perm_b32 v22, v14, v10, s24\n\t" \
  "v_perm_b32 v19, v15, v11, s23\n\t" \
  "v_perm_b32 v23, v15, v11, s24\n\t" \
  "v_accvgpr_write_b32 a232, v16\n\t" \
  "v_accvgpr_write_b32 a233, v17\n\t" \
  "v_accvgpr_write_b32 a234, v18\n\t" \
  "v_accvgpr_write_b32 a235, v19\n\t" \
  "v_accvgpr_write_b32 a236, v20\n\t" \
  "v_accvgpr_write_b32 a237, v21\n\t" \
  "v_accvgpr_write_b32 a238, v22\n\t" \
  "v_accvgpr_write_b32 a239, v23\n\t" \
  "s_mov_b32 s22, 131072\n\t" \
  "buffer_load_dword v192, v2, s[8:11], s22 offen offset:0\n\t" \
  "buffer_load_dword v193, v2, s[8:11], s22 offen offset:1024\n\t" \
  "buffer_load_dword v194, v2, s[8:11], s22 offen offset:2048\n\t" \
  "buffer_load_dword v195, v2, s[8:11], s22 offen offset:3072\n\t" \
  "buffer_load_dword v196, v3, s[8:11], s22 offen offset:0\n\t" \
  "buffer_load_dword v197, v3, s[8:11], s22 offen offset:1024\n\t" \
  "buffer_load_dword v198, v3, s[8:11], s22 offen offset:2048\n\t" \
  "buffer_load_dword v199, v3, s[8:11], s22 offen offset:3072\n\t" \
  "s_waitcnt vmcnt(48)\n\t" \
  "v_accvgpr_read_b32 v8, a240\n\t" \
  "v_accvgpr_read_b32 v9, a241\n\t" \
  "v_accvgpr_read_b32 v10, a242\n\t" \
  "v_accvgpr_read_b32 v11, a243\n\t" \
  "v_accvgpr_read_b32 v12, a244\n\t" \
  "v_accvgpr_read_b32 v13, a245\n\t" \
  "v_accvgpr_read_b32 v14, a246\n\t" \
  "v_accvgpr_read_b32 v15, a247\n\t" \
  "v_perm_b32 v16, v12, v8, s23\n\t" \
  "v_perm_b32 v20, v12, v8, s24\n\t" \
  "v_perm_b32 v17, v13, v9, s23\n\t" \
  "v_perm_b32 v21, v13, v9, s24\n\t" \
  "v_perm_b32 v18, v14, v10, s23\n\t" \
  "v_perm_b32 v22, v14, v10, s24\n\t" \
  "v_perm_b32 v19, v15, v11, s23\n\t" \
  "v_perm_b32 v23, v15, v11, s24\n\t" \
  "v_accvgpr_write_b32 a240, v16\n\t" \
  "v_accvgpr_write_b32 a241, v17\n\t" \
  "v_accvgpr_write_b32 a242, v18\n\t" \
  "v_accvgpr_write_b32 a243, v19\n\t" \
  "v_accvgpr_write_b32 a244, v20\n\t" \
  "v_accvgpr_write_b32 a245, v21\n\t" \
  "v_accvgpr_write_b32 a246, v22\n\t" \
  "v_accvgpr_write_b32 a247, v23\n\t" \
  "s_mov_b32 s22, 163840\n\t" \
  "buffer_load_dword v200, v2, s[8:11], s22 offen offset:0\n\t" \
  "buffer_load_dword v201, v2, s[8:11], s22 offen offset:1024\n\t" \
  "buffer_load_dword v202, v2, s[8:11], s22 offen offset:2048\n\t" \
  "buffer_load_dword v203, v2, s[8:11], s22 offen offset:3072\n\t" \
  "buffer_load_dword v204, v3, s[8:11], s22 offen offset:0\n\t" \
  "buffer_load_dword v205, v3, s[8:11], s22 offen offset:1024\n\t" \
  "buffer_load_dword v206, v3, s[8:11], s22 offen offset:2048\n\t" \
  "buffer_load_dword v207, v3, s[8:11], s22 offen offset:3072\n\t" \
  "s_waitcnt vmcnt(48)\n\t" \
  "v_accvgpr_read_b32 v8, a248\n\t" \
  "v_accvgpr_read_b32 v9, a249\n\t" \
  "v_accvgpr_read_b32 v10, a250\n\t" \
  "v_accvgpr_read_b32 v11, a251\n\t" \
  "v_accvgpr_read_b32 v12, a252\n\t" \
  "v_accvgpr_read_b32 v13, a253\n\t" \
  "v_accvgpr_read_b32 v14, a254\n\t" \
  "v_accvgpr_read_b32 v15, a255\n\t" \
  "v_perm_b32 v16, v12, v8, s23\n\t" \
  "v_perm_b32 v20, v12, v8, s24\n\t" \
  "v_perm_b32 v17, v13, v9, s23\n\t" \
  "v_perm_b32 v21, v13, v9, s24\n\t" \
  "v_perm_b32 v18, v14, v10, s23\n\t" \
  "v_perm_b32 v22, v14, v10, s24\n\t" \
  "v_perm_b32 v19, v15, v11, s23\n\t" \
  "v_perm_b32 v23, v15, v11, s24\n\t" \
  "v_accvgpr_write_b32 a248, v16\n\t" \
  "v_accvgpr_write_b32 a249, v17\n\t" \
  "v_accvgpr_write_b32 a250, v18\n\t" \
  "v_accvgpr_write_b32 a251, v19\n\t" \
  "v_accvgpr_write_b32 a252, v20\n\t" \
  "v_accvgpr_write_b32 a253, v21\n\t" \
  "v_accvgpr_write_b32 a254, v22\n\t" \
  "v_accvgpr_write_b32 a255, v23\n\t" \
  "s_mov_b32 s22, 196608\n\t" \
  "buffer_load_dword v208, v2, s[8:11], s22 offen offset:0\n\t" \
  "buffer_load_dword v209, v2, s[8:11], s22 offen offset:1024\n\t" \
  "buffer_load_dword v210, v2, s[8:11], s22 offen offset:2048\n\t" \
  "buffer_load_dword v211, v2, s[8:11], s22 offen offset:3072\n\t" \
  "buffer_load_dword v212, v3, s[8:11], s22 offen offset:0\n\t" \
  "buffer_load_dword v213, v3, s[8:11], s22 offen offset:1024\n\t" \
  "buffer_load_dword v214, v3, s[8:11], s22 offen offset:2048\n\t" \
  "buffer_load_dword v215, v3, s[8:11], s22 offen offset:3072\n\t" \
  "s_waitcnt vmcnt(48)\n\t" \
  "v_perm_b32 v16, v164, v160, s23\n\t" \
  "v_perm_b32 v164, v164, v160, s24\n\t" \
  "v_mov_b32 v160, v16\n\t" \
  "v_perm_b32 v17, v165, v161, s23\n\t" \
  "v_perm_b32 v165, v165, v161, s24\n\t" \
  "v_mov_b32 v161, v17\n\t" \
  "v_perm_b32 v18, v166, v162, s23\n\t" \
  "v_perm_b32 v166, v166, v162, s24\n\t" \
  "v_mov_b32 v162, v18\n\t" \
  "v_perm_b32 v19, v167, v163, s23\n\t" \
  "v_perm_b32 v167, v167, v163, s24\n\t" \
  "v_mov_b32 v163, v19\n\t" \
  "s_mov_b32 s22, 229376\n\t" \
  "buffer_load_dword v216, v2, s[8:11], s22 offen offset:0\n\t" \
  "buffer_load_dword v217, v2, s[8:11], s22 offen offset:1024\n\t" \
  "buffer_load_dword v218, v2, s[8:11], s22 offen offset:2048\n\t" \
  "buffer_load_dword v219, v2, s[8:11], s22 offen offset:3072\n\t" \
  "buffer_load_dword v220, v3, s[8:11], s22 offen offset:0\n\t" \
  "buffer_load_dword v221, v3, s[8:11], s22 offen offset:1024\n\t" \
  "buffer_load_dword v222, v3, s[8:11], s22 offen offset:2048\n\t" \
  "buffer_load_dword v223, v3, s[8:11], s22 offen offset:3072\n\t" \
  "s_waitcnt vmcnt(48)\n\t" \
  "v_perm_b32 v16, v172, v168, s23\n\t" \
  "v_perm_b32 v172, v172, v168, s24\n\t" \
  "v_mov_b32 v168, v16\n\t" \
  "v_perm_b32 v17, v173, v169, s23\n\t" \
  "v_perm_b32 v173, v173, v169, s24\n\t" \
  "v_mov_b32 v169, v17\n\t" \
  "v_perm_b32 v18, v174, v170, s23\n\t" \
  "v_perm_b32 v174, v174, v170, s24\n\t" \
  "v_mov_b32 v170, v18\n\t" \
  "v_perm_b32 v19, v175, v171, s23\n\t" \
  "v_perm_b32 v175, v175, v171, s24\n\t" \
  "v_mov_b32 v171, v19\n\t" \
  "s_mov_b32 s22, 262144\n\t" \
  "buffer_load_dword v224, v2, s[8:11], s22 offen offset:0\n\t" \
  "buffer_load_dword v225, v2, s[8:11], s22 offen offset:1024\n\t" \
  "buffer_load_dword v226, v2, s[8:11], s22 offen offset:2048\n\t" \
  "buffer_load_dword v227, v2, s[8:11], s22 offen offset:3072\n\t" \
  "buffer_load_dword v228, v3, s[8:11], s22 offen offset:0\n\t" \
  "buffer_load_dword v229, v3, s[8:11], s22 offen offset:1024\n\t" \
  "buffer_load_dword v230, v3, s[8:11], s22 offen offset:2048\n\t" \
  "buffer_load_dword v231, v3, s[8:11], s22 offen offset:3072\n\t" \
  "s_waitcnt vmcnt(48)\n\t" \
  "v_perm_b32 v16, v180, v176, s23\n\t" \
  "v_perm_b32 v180, v180, v176, s24\n\t" \
  "v_mov_b32 v176, v16\n\t" \
  "v_perm_b32 v17, v181, v177, s23\n\t" \
  "v_perm_b32 v181, v181, v177, s24\n\t" \
  "v_mov_b32 v177, v17\n\t" \
  "v_perm_b32 v18, v182, v178, s23\n\t" \
  "v_perm_b32 v182, v182, v178, s24\n\t" \
  "v_mov_b32 v178, v18\n\t" \
  "v_perm_b32 v19, v183, v179, s23\n\t" \
  "v_perm_b32 v183, v183, v179, s24\n\t" \
  "v_mov_b32 v179, v19\n\t" \
  "s_mov_b32 s22, 294912\n\t" \
  "buffer_load_dword v232, v2, s[8:11], s22 offen offset:0\n\t" \
  "buffer_load_dword v233, v2, s[8:11], s22 offen offset:1024\n\t" \
  "buffer_load_dword v234, v2, s[8:11], s22 offen offset:2048\n\t" \
  "buffer_load_dword v235, v2, s[8:11], s22 offen offset:3072\n\t" \
  "buffer_load_dword v236, v3, s[8:11], s22 offen offset:0\n\t" \
  "buffer_load_dword v237, v3, s[8:11], s22 offen offset:1024\n\t" \
  "buffer_load_dword v238, v3, s[8:11], s22 offen offset:2048\n\t" \
  "buffer_load_dword v239, v3, s[8:11], s22 offen offset:3072\n\t" \
  "s_waitcnt vmcnt(48)\n\t" \
  "v_perm_b32 v16, v188, v184, s23\n\t" \
  "v_perm_b32 v188, v188, v184, s24\n\t" \
  "v_mov_b32 v184, v16\n\t" \
  "v_perm_b32 v17, v189, v185, s23\n\t" \
  "v_perm_b32 v189, v189, v185, s24\n\t" \
  "v_mov_b32 v185, v17\n\t" \
  "v_perm_b32 v18, v190, v186, s23\n\t" \
  "v_perm_b32 v190, v190, v186, s24\n\t" \
  "v_mov_b32 v186, v18\n\t" \
  "v_perm_b32 v19, v191, v187, s23\n\t" \
  "v_perm_b32 v191, v191, v187, s24\n\t" \
  "v_mov_b32 v187, v19\n\t" \
  "s_mov_b32 s22, 327680\n\t" \
  "buffer_load_dword v240, v2, s[8:11], s22 offen offset:0\n\t" \
  "buffer_load_dword v241, v2, s[8:11], s22 offen offset:1024\n\t" \
  "buffer_load_dword v242, v2, s[8:11], s22 offen offset:2048\n\t" \
  "buffer_load_dword v243, v2, s[8:11], s22 offen offset:3072\n\t" \
  "buffer_load_dword v244, v3, s[8:11], s22 offen offset:0\n\t" \
  "buffer_load_dword v245, v3, s[8:11], s22 offen offset:1024\n\t" \
  "buffer_load_dword v246, v3, s[8:11], s22 offen offset:2048\n\t" \
  "buffer_load_dword v247, v3, s[8:11], s22 offen offset:3072\n\t" \
  "s_waitcnt vmcnt(48)\n\t" \
  "v_perm_b32 v16, v196, v192, s23\n\t" \
  "v_perm_b32 v196, v196, v192, s24\n\t" \
  "v_mov_b32 v192, v16\n\t" \
  "v_perm_b32 v17, v197, v193, s23\n\t" \
  "v_perm_b32 v197, v197, v193, s24\n\t" \
  "v_mov_b32 v193, v17\n\t" \
  "v_perm_b32 v18, v198, v194, s23\n\t" \
  "v_perm_b32 v198, v198, v194, s24\n\t" \
  "v_mov_b32 v194, v18\n\t" \
  "v_perm_b32 v19, v199, v195, s23\n\t" \
  "v_perm_b32 v199, v199, v195, s24\n\t" \
  "v_mov_b32 v195, v19\n\t" \
  "s_waitcnt vmcnt(40)\n\t" \
  "v_perm_b32 v16, v204, v200, s23\n\t" \
  "v_perm_b32 v204, v204, v200, s24\n\t" \
  "v_mov_b32 v200, v16\n\t" \
  "v_perm_b32 v17, v205, v201, s23\n\t" \
  "v_perm_b32 v205, v205, v201, s24\n\t" \
  "v_mov_b32 v201, v17\n\t" \
  "v_perm_b32 v18, v206, v202, s23\n\t" \
  "v_perm_b32 v206, v206, v202, s24\n\t" \
  "v_mov_b32 v202, v18\n\t" \
  "v_perm_b32 v19, v207, v203, s23\n\t" \
  "v_perm_b32 v207, v207, v203, s24\n\t" \
  "v_mov_b32 v203, v19\n\t" \
  "s_waitcnt vmcnt(32)\n\t" \
  "v_perm_b32 v16, v212, v208, s23\n\t" \
  "v_perm_b32 v212, v212, v208, s24\n\t" \
  "v_mov_b32 v208, v16\n\t" \
  "v_perm_b32 v17, v213, v209, s23\n\t" \
  "v_perm_b32 v213, v213, v209, s24\n\t" \
  "v_mov_b32 v209, v17\n\t" \
  "v_perm_b32 v18, v214, v210, s23\n\t" \
  "v_perm_b32 v214, v214, v210, s24\n\t" \
  "v_mov_b32 v210, v18\n\t" \
  "v_perm_b32 v19, v215, v211, s23\n\t" \
  "v_perm_b32 v215, v215, v211, s24\n\t" \
  "v_mov_b32 v211, v19\n\t" \
  "s_waitcnt vmcnt(24)\n\t" \
  "v_perm_b32 v16, v220, v216, s23\n\t" \
  "v_perm_b32 v220, v220, v216, s24\n\t" \
  "v_mov_b32 v216, v16\n\t" \
  "v_perm_b32 v17, v221, v217, s23\n\t" \
  "v_perm_b32 v221, v221, v217, s24\n\t" \
  "v_mov_b32 v217, v17\n\t" \
  "v_perm_b32 v18, v222, v218, s23\n\t" \
  "v_perm_b32 v222, v222, v218, s24\n\t" \
  "v_mov_b32 v218, v18\n\t" \
  "v_perm_b32 v19, v223, v219, s23\n\t" \
  "v_perm_b32 v223, v223, v219, s24\n\t" \
  "v_mov_b32 v219, v19\n\t" \
  "s_waitcnt vmcnt(16)\n\t" \
  "v_perm_b32 v16, v228, v224, s23\n\t" \
  "v_perm_b32 v228, v228, v224, s24\n\t" \
  "v_mov_b32 v224, v16\n\t" \
  "v_perm_b32 v17, v229, v225, s23\n\t" \
  "v_perm_b32 v229, v229, v225, s24\n\t" \
  "v_mov_b32 v225, v17\n\t" \
  "v_perm_b32 v18, v230, v226, s23\n\t" \
  "v_perm_b32 v230, v230, v226, s24\n\t" \
  "v_mov_b32 v226, v18\n\t" \
  "v_perm_b32 v19, v231, v227, s23\n\t" \
  "v_perm_b32 v231, v231, v227, s24\n\t" \
  "v_mov_b32 v227, v19\n\t" \
  "s_waitcnt vmcnt(8)\n\t" \
  "v_perm_b32 v16, v236, v232, s23\n\t" \
  "v_perm_b32 v236, v236, v232, s24\n\t" \
  "v_mov_b32 v232, v16\n\t" \
  "v_perm_b32 v17, v237, v233, s23\n\t" \
  "v_perm_b32 v237, v237, v233, s24\n\t" \
  "v_mov_b32 v233, v17\n\t" \
  "v_perm_b32 v18, v238, v234, s23\n\t" \
  "v_perm_b32 v238, v238, v234, s24\n\t" \
  "v_mov_b32 v234, v18\n\t" \
  "v_perm_b32 v19, v239, v235, s23\n\t" \
  "v_perm_b32 v239, v239, v235, s24\n\t" \
  "v_mov_b32 v235, v19\n\t" \
  "s_waitcnt vmcnt(0)\n\t" \
  "v_perm_b32 v16, v244, v240, s23\n\t" \
  "v_perm_b32 v244, v244, v240, s24\n\t" \
  "v_mov_b32 v240, v16\n\t" \
  "v_perm_b32 v17, v245, v241, s23\n\t" \
  "v_perm_b32 v245, v245, v241, s24\n\t" \
  "v_mov_b32 v241, v17\n\t" \
  "v_perm_b32 v18, v246, v242, s23\n\t" \
  "v_perm_b32 v246, v246, v242, s24\n\t" \
  "v_mov_b32 v242, v18\n\t" \
  "v_perm_b32 v19, v247, v243, s23\n\t" \
  "v_perm_b32 v247, v247, v243, s24\n\t" \
  "v_mov_b32 v243, v19\n\t"
#define CLOB_OL "memory", "s22", "s23", "s24", "v8", "v9", "v10", "v11", "v12", "v13", "v14", "v15", "v16", "v17", "v18", "v19", "v20", "v21", "v22", "v23"
#define A_LOOP3 \
  "s_nop 4\n\t" \
  "s_mov_b32 s57, m0\n\t" \
  "L_seg_%=:\n\t" \
  "ds_read_b128 v[40:43], v39 offset:0\n\t" \
  "ds_read_b128 v[44:47], v39 offset:16384\n\t" \
  "ds_read_b128 v[48:51], v39 offset:1024\n\t" \
  "ds_read_b128 v[52:55], v39 offset:17408\n\t" \
  "s_waitcnt lgkmcnt(2)\n\t" \
  "v_mfma_f32_16x16x32_bf16 v[64:67], a[0:3], v[40:43], 0\n\t" \
  "ds_read_b128 v[56:59], v39 offset:2048\n\t" \
  "v_mfma_f32_16x16x32_bf16 v[68:71], a[0:3], v[44:47], 0\n\t" \
  "ds_read_b128 v[60:63], v39 offset:18432\n\t" \
  "v_mfma_f32_16x16x32_bf16 v[72:75], a[64:67], v[40:43], 0\n\t" \
  "buffer_store_dwordx4 v[112:115], v37, s[40:43], s46 offen sc1\n\t" \
  "v_mfma_f32_16x16x32_bf16 v[76:79], a[64:67], v[44:47], 0\n\t" \
  "v_mfma_f32_16x16x32_bf16 v[80:83], a[128:131], v[40:43], 0\n\t" \
  "v_mfma_f32_16x16x32_bf16 v[84:87], a[128:131], v[44:47], 0\n\t" \
  "v_mfma_f32_16x16x32_bf16 v[88:91], a[192:195], v[40:43], 0\n\t" \
  "v_mfma_f32_16x16x32_bf16 v[92:95], a[192:195], v[44:47], 0\n\t" \
  "v_mfma_f32_16x16x32_bf16 v[96:99], v[40:43], v[128:131], 0\n\t" \
  "v_mfma_f32_16x16x32_bf16 v[100:103], v[44:47], v[128:131], 0\n\t" \
  "v_mfma_f32_16x16x32_bf16 v[104:107], v[40:43], v[192:195], 0\n\t" \
  "v_mfma_f32_16x16x32_bf16 v[108:111], v[44:47], v[192:195], 0\n\t" \
  "s_waitcnt lgkmcnt(2)\n\t" \
  "v_mfma_f32_16x16x32_bf16 v[64:67], a[4:7], v[48:51], v[64:67]\n\t" \
  "ds_read_b128 v[40:43], v39 offset:3072\n\t" \
  "buffer_store_dwordx4 v[116:119], v37, s[40:43], s47 offen sc1\n\t" \
  "v_mfma_f32_16x16x32_bf16 v[68:71], a[4:7], v[52:55], v[68:71]\n\t" \
  "ds_read_b128 v[44:47], v39 offset:19456\n\t" \
  "v_mfma_f32_16x16x32_bf16 v[72:75], a[68:71], v[48:51], v[72:75]\n\t" \
  "v_mfma_f32_16x16x32_bf16 v[76:79], a[68:71], v[52:55], v[76:79]\n\t" \
  "v_mfma_f32_16x16x32_bf16 v[80:83], a[132:135], v[48:51], v[80:83]\n\t" \
  "v_mfma_f32_16x16x32_bf16 v[84:87], a[132:135], v[52:55], v[84:87]\n\t" \
  "v_mfma_f32_16x16x32_bf16 v[88:91], a[196:199], v[48:51], v[88:91]\n\t" \
  "v_mfma_f32_16x16x32_bf16 v[92:95], a[196:199], v[52:55], v[92:95]\n\t" \
  "v_mfma_f32_16x16x32_bf16 v[96:99], v[48:51], v[132:135], v[96:99]\n\t" \
  "v_mfma_f32_16x16x32_bf16 v[100:103], v[52:55], v[132:135], v[100:103]\n\t" \
  "v_mfma_f32_16x16x32_bf16 v[104:107], v[48:51], v[196:199], v[104:107]\n\t" \
  "buffer_store_dwordx4 v[120:123], v36, s[40:43], s46 offen sc1\n\t" \
  "v_mfma_f32_16x16x32_bf16 v[108:111], v[52:55], v[196:199], v[108:111]\n\t" \
  "s_waitcnt lgkmcnt(2)\n\t" \
  "v_mfma_f32_16x16x32_bf16 v[64:67], a[8:11], v[56:59], v[64:67]\n\t" \
  "ds_read_b128 v[48:51], v39 offset:4096\n\t" \
  "v_mfma_f32_16x16x32_bf16 v[68:71], a[8:11], v[60:63], v[68:71]\n\t" \
  "ds_read_b128 v[52:55], v39 offset:20480\n\t" \
  "v_mfma_f32_16x16x32_bf16 v[72:75], a[72:75], v[56:59], v[72:75]\n\t" \
  "v_mfma_f32_16x16x32_bf16 v[76:79], a[72:75], v[60:63], v[76:79]\n\t" \
  "v_mfma_f32_16x16x32_bf16 v[80:83], a[136:139], v[56:59], v[80:83]\n\t" \
  "v_mfma_f32_16x16x32_bf16 v[84:87], a[136:139], v[60:63], v[84:87]\n\t" \
  "v_mfma_f32_16x16x32_bf16 v[88:91], a[200:203], v[56:59], v[88:91]\n\t" \
  "v_mfma_f32_16x16x32_bf16 v[92:95], a[200:203], v[60:63], v[92:95]\n\t" \
  "v_mfma_f32_16x16x32_bf16 v[96:99], v[56:59], v[136:139], v[96:99]\n\t" \
  "buffer_store_dwordx4 v[124:127], v36, s[40:43], s46 offen offset:1024 sc1\n\t" \
  "v_mfma_f32_16x16x32_bf16 v[100:103], v[60:63], v[136:139], v[100:103]\n\t" \
  "v_mfma_f32_16x16x32_bf16 v[104:107], v[56:59], v[200:203], v[104:107]\n\t" \
  "v_mfma_f32_16x16x32_bf16 v[108:111], v[60:63], v[200:203], v[108:111]\n\t" \
  "s_waitcnt lgkmcnt(2)\n\t" \
  "v_mfma_f32_16x16x32_bf16 v[64:67], a[12:15], v[40:43], v[64:67]\n\t" \
  "ds_read_b128 v[56:59], v39 offset:5120\n\t" \
  "v_mfma_f32_16x16x32_bf16 v[68:71], a[12:15], v[44:47], v[68:71]\n\t" \
  "ds_read_b128 v[60:63], v39 offset:21504\n\t" \
  "v_mfma_f32_16x16x32_bf16 v[72:75], a[76:79], v[40:43], v[72:75]\n\t" \
  "v_mfma_f32_16x16x32_bf16 v[76:79], a[76:79], v[44:47], v[76:79]\n\t" \
  "v_mfma_f32_16x16x32_bf16 v[80:83], a[140:143], v[40:43], v[80:83]\n\t" \
  "v_mfma_f32_16x16x32_bf16 v[84:87], a[140:143], v[44:47], v[84:87]\n\t" \
  "v_mfma_f32_16x16x32_bf16 v[88:91], a[204:207], v[40:43], v[88:91]\n\t" \
  "buffer_store_dwordx4 v[24:27], v37, s[40:43], s48 offen sc1\n\t" \
  "v_mfma_f32_16x16x32_bf16 v[92:95], a[204:207], v[44:47], v[92:95]\n\t" \
  "v_mfma_f32_16x16x32_bf16 v[96:99], v[40:43], v[140:143], v[96:99]\n\t" \
  "v_mfma_f32_16x16x32_bf16 v[100:103], v[44:47], v[140:143], v[100:103]\n\t" \
  "v_mfma_f32_16x16x32_bf16 v[104:107], v[40:43], v[204:207], v[104:107]\n\t" \
  "v_mfma_f32_16x16x32_bf16 v[108:111], v[44:47], v[204:207], v[108:111]\n\t" \
  "s_waitcnt lgkmcnt(2)\n\t" \
  "v_mfma_f32_16x16x32_bf16 v[64:67], a[16:19], v[48:51], v[64:67]\n\t" \
  "ds_read_b128 v[40:43], v39 offset:6144\n\t" \
  "v_mfma_f32_16x16x32_bf16 v[68:71], a[16:19], v[52:55], v[68:71]\n\t" \
  "ds_read_b128 v[44:47], v39 offset:22528\n\t" \
  "v_mfma_f32_16x16x32_bf16 v[72:75], a[80:83], v[48:51], v[72:75]\n\t" \
  "v_mfma_f32_16x16x32_bf16 v[76:79], a[80:83], v[52:55], v[76:79]\n\t" \
  "v_mfma_f32_16x16x32_bf16 v[80:83], a[144:147], v[48:51], v[80:83]\n\t" \
  "buffer_store_dwordx4 v[28:31], v37, s[40:43], s49 offen sc1\n\t" \
  "v_mfma_f32_16x16x32_bf16 v[84:87], a[144:147], v[52:55], v[84:87]\n\t" \
  "v_mfma_f32_16x16x32_bf16 v[88:91], a[208:211], v[48:51], v[88:91]\n\t" \
  "v_mfma_f32_16x16x32_bf16 v[92:95], a[208:211], v[52:55], v[92:95]\n\t" \
  "v_mfma_f32_16x16x32_bf16 v[96:99], v[48:51], v[144:147], v[96:99]\n\t" \
  "v_mfma_f32_16x16x32_bf16 v[100:103], v[52:55], v[144:147], v[100:103]\n\t" \
  "v_mfma_f32_16x16x32_bf16 v[104:107], v[48:51], v[208:211], v[104:107]\n\t" \
  "v_mfma_f32_16x16x32_bf16 v[108:111], v[52:55], v[208:211], v[108:111]\n\t" \
  "s_waitcnt lgkmcnt(2)\n\t" \
  "v_mfma_f32_16x16x32_bf16 v[64:67], a[20:23], v[56:59], v[64:67]\n\t" \
  "ds_read_b128 v[48:51], v39 offset:7168\n\t" \
  "v_mfma_f32_16x16x32_bf16 v[68:71], a[20:23], v[60:63], v[68:71]\n\t" \
  "ds_read_b128 v[52:55], v39 offset:23552\n\t" \
  "v_mfma_f32_16x16x32_bf16 v[72:75], a[84:87], v[56:59], v[72:75]\n\t" \
  "s_mov_b32 m0, s45\n\t" \
  "s_add_u32 s53, s44, 4096\n\t" \
  "buffer_load_dwordx4 v38, s[40:43], s44 offen sc1 lds\n\t" \
  "v_mfma_f32_16x16x32_bf16 v[76:79], a[84:87], v[60:63], v[76:79]\n\t" \
  "v_mfma_f32_16x16x32_bf16 v[80:83], a[148:151], v[56:59], v[80:83]\n\t" \
  "v_mfma_f32_16x16x32_bf16 v[84:87], a[148:151], v[60:63], v[84:87]\n\t" \
  "v_mfma_f32_16x16x32_bf16 v[88:91], a[212:215], v[56:59], v[88:91]\n\t" \
  "v_mfma_f32_16x16x32_bf16 v[92:95], a[212:215], v[60:63], v[92:95]\n\t" \
  "v_mfma_f32_16x16x32_bf16 v[96:99], v[56:59], v[148:151], v[96:99]\n\t" \
  "v_mfma_f32_16x16x32_bf16 v[100:103], v[60:63], v[148:151], v[100:103]\n\t" \
  "v_mfma_f32_16x16x32_bf16 v[104:107], v[56:59], v[212:215], v[104:107]\n\t" \
  "v_mfma_f32_16x16x32_bf16 v[108:111], v[60:63], v[212:215], v[108:111]\n\t" \
  "s_waitcnt lgkmcnt(2)\n\t" \
  "v_mfma_f32_16x16x32_bf16 v[64:67], a[24:27], v[40:43], v[64:67]\n\t" \
  "ds_read_b128 v[56:59], v39 offset:8192\n\t" \
  "buffer_load_dwordx4 v38, s[40:43], s44 offen offset:1024 sc1 lds\n\t" \
  "v_mfma_f32_16x16x32_bf16 v[68:71], a[24:27], v[44:47], v[68:71]\n\t" \
  "ds_read_b128 v[60:63], v39 offset:24576\n\t" \
  "v_mfma_f32_16x16x32_bf16 v[72:75], a[88:91], v[40:43], v[72:75]\n\t" \
  "v_mfma_f32_16x16x32_bf16 v[76:79], a[88:91], v[44:47], v[76:79]\n\t" \
  "v_mfma_f32_16x16x32_bf16 v[80:83], a[152:155], v[40:43], v[80:83]\n\t" \
  "v_mfma_f32_16x16x32_bf16 v[84:87], a[152:155], v[44:47], v[84:87]\n\t" \
  "v_mfma_f32_16x16x32_bf16 v[88:91], a[216:219], v[40:43], v[88:91]\n\t" \
  "v_mfma_f32_16x16x32_bf16 v[92:95], a[216:219], v[44:47], v[92:95]\n\t" \
  "v_mfma_f32_16x16x32_bf16 v[96:99], v[40:43], v[152:155], v[96:99]\n\t" \
  "v_mfma_f32_16x16x32_bf16 v[100:103], v[44:47], v[152:155], v[100:103]\n\t" \
  "v_mfma_f32_16x16x32_bf16 v[104:107], v[40:43], v[216:219], v[104:107]\n\t" \
  "buffer_load_dwordx4 v38, s[40:43], s44 offen offset:2048 sc1 lds\n\t" \
  "v_mfma_f32_16x16x32_bf16 v[108:111], v[44:47], v[216:219], v[108:111]\n\t" \
  "s_waitcnt lgkmcnt(2)\n\t" \
  "v_mfma_f32_16x16x32_bf16 v[64:67], a[28:31], v[48:51], v[64:67]\n\t" \
  "ds_read_b128 v[40:43], v39 offset:9216\n\t" \
  "v_mfma_f32_16x16x32_bf16 v[68:71], a[28:31], v[52:55], v[68:71]\n\t" \
  "ds_read_b128 v[44:47], v39 offset:25600\n\t" \
  "v_mfma_f32_16x16x32_bf16 v[72:75], a[92:95], v[48:51], v[72:75]\n\t" \
  "v_mfma_f32_16x16x32_bf16 v[76:79], a[92:95], v[52:55], v[76:79]\n\t" \
  "v_mfma_f32_16x16x32_bf16 v[80:83], a[156:159], v[48:51], v[80:83]\n\t" \
  "v_mfma_f32_16x16x32_bf16 v[84:87], a[156:159], v[52:55], v[84:87]\n\t" \
  "v_mfma_f32_16x16x32_bf16 v[88:91], a[220:223], v[48:51], v[88:91]\n\t" \
  "v_mfma_f32_16x16x32_bf16 v[92:95], a[220:223], v[52:55], v[92:95]\n\t" \
  "v_mfma_f32_16x16x32_bf16 v[96:99], v[48:51], v[156:159], v[96:99]\n\t" \
  "buffer_load_dwordx4 v38, s[40:43], s44 offen offset:3072 sc1 lds\n\t" \
  "v_mfma_f32_16x16x32_bf16 v[100:103], v[52:55], v[156:159], v[100:103]\n\t" \
  "v_mfma_f32_16x16x32_bf16 v[104:107], v[48:51], v[220:223], v[104:107]\n\t" \
  "v_mfma_f32_16x16x32_bf16 v[108:111], v[52:55], v[220:223], v[108:111]\n\t" \
  "s_waitcnt lgkmcnt(2)\n\t" \
  "v_mfma_f32_16x16x32_bf16 v[64:67], a[32:35], v[56:59], v[64:67]\n\t" \
  "ds_read_b128 v[48:51], v39 offset:10240\n\t" \
  "v_mfma_f32_16x16x32_bf16 v[68:71], a[32:35], v[60:63], v[68:71]\n\t" \
  "ds_read_b128 v[52:55], v39 offset:26624\n\t" \
  "v_mfma_f32_16x16x32_bf16 v[72:75], a[96:99], v[56:59], v[72:75]\n\t" \
  "v_mfma_f32_16x16x32_bf16 v[76:79], a[96:99], v[60:63], v[76:79]\n\t" \
  "v_mfma_f32_16x16x32_bf16 v[80:83], a[160:163], v[56:59], v[80:83]\n\t" \
  "v_mfma_f32_16x16x32_bf16 v[84:87], a[160:163], v[60:63], v[84:87]\n\t" \
  "v_mfma_f32_16x16x32_bf16 v[88:91], a[224:227], v[56:59], v[88:91]\n\t" \
  "s_add_u32 m0, s45, 4096\n\t" \
  "s_nop 0\n\t" \
  "buffer_load_dwordx4 v38, s[40:43], s53 offen sc1 lds\n\t" \
  "v_mfma_f32_16x16x32_bf16 v[92:95], a[224:227], v[60:63], v[92:95]\n\t" \
  "v_mfma_f32_16x16x32_bf16 v[96:99], v[56:59], v[160:163], v[96:99]\n\t" \
  "v_mfma_f32_16x16x32_bf16 v[100:103], v[60:63], v[160:163], v[100:103]\n\t" \
  "v_mfma_f32_16x16x32_bf16 v[104:107], v[56:59], v[224:227], v[104:107]\n\t" \
  "v_mfma_f32_16x16x32_bf16 v[108:111], v[60:63], v[224:227], v[108:111]\n\t" \
  "s_waitcnt lgkmcnt(2)\n\t" \
  "v_mfma_f32_16x16x32_bf16 v[64:67], a[36:39], v[40:43], v[64:67]\n\t" \
  "ds_read_b128 v[56:59], v39 offset:11264\n\t" \
  "v_mfma_f32_16x16x32_bf16 v[68:71], a[36:39], v[44:47], v[68:71]\n\t" \
  "ds_read_b128 v[60:63], v39 offset:27648\n\t" \
  "v_mfma_f32_16x16x32_bf16 v[72:75], a[100:103], v[40:43], v[72:75]\n\t" \
  "v_mfma_f32_16x16x32_bf16 v[76:79], a[100:103], v[44:47], v[76:79]\n\t" \
  "v_mfma_f32_16x16x32_bf16 v[80:83], a[164:167], v[40:43], v[80:83]\n\t" \
  "buffer_load_dwordx4 v38, s[40:43], s53 offen offset:1024 sc1 lds\n\t" \
  "v_mfma_f32_16x16x32_bf16 v[84:87], a[164:167], v[44:47], v[84:87]\n\t" \
  "v_mfma_f32_16x16x32_bf16 v[88:91], a[228:231], v[40:43], v[88:91]\n\t" \
  "v_mfma_f32_16x16x32_bf16 v[92:95], a[228:231], v[44:47], v[92:95]\n\t" \
  "v_mfma_f32_16x16x32_bf16 v[96:99], v[40:43], v[164:167], v[96:99]\n\t" \
  "v_mfma_f32_16x16x32_bf16 v[100:103], v[44:47], v[164:167], v[100:103]\n\t" \
  "v_mfma_f32_16x16x32_bf16 v[104:107], v[40:43], v[228:231], v[104:107]\n\t" \
  "v_mfma_f32_16x16x32_bf16 v[108:111], v[44:47], v[228:231], v[108:111]\n\t" \
  "s_waitcnt lgkmcnt(2)\n\t" \
  "v_mfma_f32_16x16x32_bf16 v[64:67], a[40:43], v[48:51], v[64:67]\n\t" \
  "ds_read_b128 v[40:43], v39 offset:12288\n\t" \
  "v_mfma_f32_16x16x32_bf16 v[68:71], a[40:43], v[52:55], v[68:71]\n\t" \
  "ds_read_b128 v[44:47], v39 offset:28672\n\t" \
  "v_mfma_f32_16x16x32_bf16 v[72:75], a[104:107], v[48:51], v[72:75]\n\t" \
  "buffer_load_dwordx4 v38, s[40:43], s53 offen offset:2048 sc1 lds\n\t" \
  "v_mfma_f32_16x16x32_bf16 v[76:79], a[104:107], v[52:55], v[76:79]\n\t" \
  "v_mfma_f32_16x16x32_bf16 v[80:83], a[168:171], v[48:51], v[80:83]\n\t" \
  "v_mfma_f32_16x16x32_bf16 v[84:87], a[168:171], v[52:55], v[84:87]\n\t" \
  "v_mfma_f32_16x16x32_bf16 v[88:91], a[232:235], v[48:51], v[88:91]\n\t" \
  "v_mfma_f32_16x16x32_bf16 v[92:95], a[232:235], v[52:55], v[92:95]\n\t" \
  "v_mfma_f32_16x16x32_bf16 v[96:99], v[48:51], v[168:171], v[96:99]\n\t" \
  "v_mfma_f32_16x16x32_bf16 v[100:103], v[52:55], v[168:171], v[100:103]\n\t" \
  "v_mfma_f32_16x16x32_bf16 v[104:107], v[48:51], v[232:235], v[104:107]\n\t" \
  "v_mfma_f32_16x16x32_bf16 v[108:111], v[52:55], v[232:235], v[108:111]\n\t" \
  "s_waitcnt lgkmcnt(2)\n\t" \
  "v_mfma_f32_16x16x32_bf16 v[64:67], a[44:47], v[56:59], v[64:67]\n\t" \
  "ds_read_b128 v[48:51], v39 offset:13312\n\t" \
  "buffer_load_dwordx4 v38, s[40:43], s53 offen offset:3072 sc1 lds\n\t" \
  "v_mfma_f32_16x16x32_bf16 v[68:71], a[44:47], v[60:63], v[68:71]\n\t" \
  "ds_read_b128 v[52:55], v39 offset:29696\n\t" \
  "v_mfma_f32_16x16x32_bf16 v[72:75], a[108:111], v[56:59], v[72:75]\n\t" \
  "v_mfma_f32_16x16x32_bf16 v[76:79], a[108:111], v[60:63], v[76:79]\n\t" \
  "v_mfma_f32_16x16x32_bf16 v[80:83], a[172:175], v[56:59], v[80:83]\n\t" \
  "v_mfma_f32_16x16x32_bf16 v[84:87], a[172:175], v[60:63], v[84:87]\n\t" \
  "v_mfma_f32_16x16x32_bf16 v[88:91], a[236:239], v[56:59], v[88:91]\n\t" \
  "v_mfma_f32_16x16x32_bf16 v[92:95], a[236:239], v[60:63], v[92:95]\n\t" \
  "v_mfma_f32_16x16x32_bf16 v[96:99], v[56:59], v[172:175], v[96:99]\n\t" \
  "v_mfma_f32_16x16x32_bf16 v[100:103], v[60:63], v[172:175], v[100:103]\n\t" \
  "v_mfma_f32_16x16x32_bf16 v[104:107], v[56:59], v[236:239], v[104:107]\n\t" \
  "v_mfma_f32_16x16x32_bf16 v[108:111], v[60:63], v[236:239], v[108:111]\n\t" \
  "s_waitcnt lgkmcnt(2)\n\t" \
  "v_mfma_f32_16x16x32_bf16 v[64:67], a[48:51], v[40:43], v[64:67]\n\t" \
  "ds_read_b128 v[56:59], v39 offset:14336\n\t" \
  "v_mfma_f32_16x16x32_bf16 v[68:71], a[48:51], v[44:47], v[68:71]\n\t" \
  "ds_read_b128 v[60:63], v39 offset:30720\n\t" \
  "v_mfma_f32_16x16x32_bf16 v[72:75], a[112:115], v[40:43], v[72:75]\n\t" \
  "v_mfma_f32_16x16x32_bf16 v[76:79], a[112:115], v[44:47], v[76:79]\n\t" \
  "v_mfma_f32_16x16x32_bf16 v[80:83], a[176:179], v[40:43], v[80:83]\n\t" \
  "v_mfma_f32_16x16x32_bf16 v[84:87], a[176:179], v[44:47], v[84:87]\n\t" \
  "v_mfma_f32_16x16x32_bf16 v[88:91], a[240:243], v[40:43], v[88:91]\n\t" \
  "v_mfma_f32_16x16x32_bf16 v[92:95], a[240:243], v[44:47], v[92:95]\n\t" \
  "v_mfma_f32_16x16x32_bf16 v[96:99], v[40:43], v[176:179], v[96:99]\n\t" \
  "v_mfma_f32_16x16x32_bf16 v[100:103], v[44:47], v[176:179], v[100:103]\n\t" \
  "v_mfma_f32_16x16x32_bf16 v[104:107], v[40:43], v[240:243], v[104:107]\n\t" \
  "v_mfma_f32_16x16x32_bf16 v[108:111], v[44:47], v[240:243], v[108:111]\n\t" \
  "s_waitcnt lgkmcnt(2)\n\t" \
  "v_mfma_f32_16x16x32_bf16 v[64:67], a[52:55], v[48:51], v[64:67]\n\t" \
  "ds_read_b128 v[40:43], v39 offset:15360\n\t" \
  "v_mfma_f32_16x16x32_bf16 v[68:71], a[52:55], v[52:55], v[68:71]\n\t" \
  "ds_read_b128 v[44:47], v39 offset:31744\n\t" \
  "v_mfma_f32_16x16x32_bf16 v[72:75], a[116:119], v[48:51], v[72:75]\n\t" \
  "v_mfma_f32_16x16x32_bf16 v[76:79], a[116:119], v[52:55], v[76:79]\n\t" \
  "v_mfma_f32_16x16x32_bf16 v[80:83], a[180:183], v[48:51], v[80:83]\n\t" \
  "v_mfma_f32_16x16x32_bf16 v[84:87], a[180:183], v[52:55], v[84:87]\n\t" \
  "v_mfma_f32_16x16x32_bf16 v[88:91], a[244:247], v[48:51], v[88:91]\n\t" \
  "v_mfma_f32_16x16x32_bf16 v[92:95], a[244:247], v[52:55], v[92:95]\n\t" \
  "v_mfma_f32_16x16x32_bf16 v[96:99], v[48:51], v[180:183], v[96:99]\n\t" \
  "v_mfma_f32_16x16x32_bf16 v[100:103], v[52:55], v[180:183], v[100:103]\n\t" \
  "v_mfma_f32_16x16x32_bf16 v[104:107], v[48:51], v[244:247], v[104:107]\n\t" \
  "v_mfma_f32_16x16x32_bf16 v[108:111], v[52:55], v[244:247], v[108:111]\n\t" \
  "s_waitcnt lgkmcnt(2)\n\t" \
  "v_mfma_f32_16x16x32_bf16 v[64:67], a[56:59], v[56:59], v[64:67]\n\t" \
  "v_mfma_f32_16x16x32_bf16 v[68:71], a[56:59], v[60:63], v[68:71]\n\t" \
  "v_mfma_f32_16x16x32_bf16 v[72:75], a[120:123], v[56:59], v[72:75]\n\t" \
  "v_mfma_f32_16x16x32_bf16 v[76:79], a[120:123], v[60:63], v[76:79]\n\t" \
  "v_mfma_f32_16x16x32_bf16 v[80:83], a[184:187], v[56:59], v[80:83]\n\t" \
  "v_mfma_f32_16x16x32_bf16 v[84:87], a[184:187], v[60:63], v[84:87]\n\t" \
  "v_mfma_f32_16x16x32_bf16 v[88:91], a[248:251], v[56:59], v[88:91]\n\t" \
  "v_mfma_f32_16x16x32_bf16 v[92:95], a[248:251], v[60:63], v[92:95]\n\t" \
  "v_mfma_f32_16x16x32_bf16 v[96:99], v[56:59], v[184:187], v[96:99]\n\t" \
  "v_mfma_f32_16x16x32_bf16 v[100:103], v[60:63], v[184:187], v[100:103]\n\t" \
  "v_mfma_f32_16x16x32_bf16 v[104:107], v[56:59], v[248:251], v[104:107]\n\t" \
  "v_mfma_f32_16x16x32_bf16 v[108:111], v[60:63], v[248:251], v[108:111]\n\t" \
  "s_waitcnt lgkmcnt(0)\n\t" \
  "v_mfma_f32_16x16x32_bf16 v[64:67], a[60:63], v[40:43], v[64:67]\n\t" \
  "v_mfma_f32_16x16x32_bf16 v[68:71], a[60:63], v[44:47], v[68:71]\n\t" \
  "v_mfma_f32_16x16x32_bf16 v[72:75], a[124:127], v[40:43], v[72:75]\n\t" \
  "v_mfma_f32_16x16x32_bf16 v[76:79], a[124:127], v[44:47], v[76:79]\n\t" \
  "v_mfma_f32_16x16x32_bf16 v[80:83], a[188:191], v[40:43], v[80:83]\n\t" \
  "v_mfma_f32_16x16x32_bf16 v[84:87], a[188:191], v[44:47], v[84:87]\n\t" \
  "v_mfma_f32_16x16x32_bf16 v[88:91], a[252:255], v[40:43], v[88:91]\n\t" \
  "v_mfma_f32_16x16x32_bf16 v[92:95], a[252:255], v[44:47], v[92:95]\n\t" \
  "v_mfma_f32_16x16x32_bf16 v[96:99], v[40:43], v[188:191], v[96:99]\n\t" \
  "v_mfma_f32_16x16x32_bf16 v[100:103], v[44:47], v[188:191], v[100:103]\n\t" \
  "v_mfma_f32_16x16x32_bf16 v[104:107], v[40:43], v[252:255], v[104:107]\n\t" \
  "v_mfma_f32_16x16x32_bf16 v[108:111], v[44:47], v[252:255], v[108:111]\n\t" \
  "s_add_u32 s44, s44, 98304\n\t" \
  "s_xor_b32 s45, s45, 131072\n\t" \
  "s_sub_u32 s52, s52, 1\n\t" \
  "s_add_u32 s46, s58, 0\n\t" \
  "s_add_u32 s58, s58, 1572864\n\t" \
  "s_add_u32 s47, s59, 0\n\t" \
  "s_add_u32 s59, s59, 1572864\n\t" \
  "s_add_u32 s48, s60, 0\n\t" \
  "s_add_u32 s60, s60, 786432\n\t" \
  "s_add_u32 s49, s61, 0\n\t" \
  "s_add_u32 s61, s61, 786432\n\t" \
  "s_cmp_lt_u32 s52, 2\n\t" \
  "s_cselect_b32 s44, 0x80000000, s44\n\t" \
  "v_cvt_pk_bf16_f32 v24, v64, v72\n\t" \
  "v_cvt_pk_bf16_f32 v25, v65, v73\n\t" \
  "v_cvt_pk_bf16_f32 v26, v66, v74\n\t" \
  "v_cvt_pk_bf16_f32 v27, v67, v75\n\t" \
  "v_cvt_pk_bf16_f32 v112, v80, v88\n\t" \
  "v_cvt_pk_bf16_f32 v113, v81, v89\n\t" \
  "v_cvt_pk_bf16_f32 v114, v82, v90\n\t" \
  "v_cvt_pk_bf16_f32 v115, v83, v91\n\t" \
  "v_cvt_pk_bf16_f32 v28, v68, v76\n\t" \
  "v_cvt_pk_bf16_f32 v29, v69, v77\n\t" \
  "v_cvt_pk_bf16_f32 v30, v70, v78\n\t" \
  "v_cvt_pk_bf16_f32 v31, v71, v79\n\t" \
  "v_cvt_pk_bf16_f32 v116, v84, v92\n\t" \
  "v_cvt_pk_bf16_f32 v117, v85, v93\n\t" \
  "v_cvt_pk_bf16_f32 v118, v86, v94\n\t" \
  "v_cvt_pk_bf16_f32 v119, v87, v95\n\t" \
  "v_cvt_pk_bf16_f32 v120, v96, v97\n\t" \
  "v_cvt_pk_bf16_f32 v121, v98, v99\n\t" \
  "v_cvt_pk_bf16_f32 v122, v100, v101\n\t" \
  "v_cvt_pk_bf16_f32 v123, v102, v103\n\t" \
  "v_cvt_pk_bf16_f32 v124, v104, v105\n\t" \
  "v_cvt_pk_bf16_f32 v125, v106, v107\n\t" \
  "v_cvt_pk_bf16_f32 v126, v108, v109\n\t" \
  "v_cvt_pk_bf16_f32 v127, v110, v111\n\t" \
  "v_xor_b32 v39, 131072, v39\n\t" \
  "v_mov_b32 v34, s51\n\t" \
  "s_waitcnt vmcnt(0)\n\t" \
  "s_barrier\n\t" \
  "buffer_store_dwordx2 v[34:35], off, s[40:43], s50 sc1\n\t" \
  "s_add_u32 s51, s51, 1\n\t" \
  "s_cmp_lg_u32 s52, 0\n\t" \
  "s_cbranch_scc1 L_seg_%=\n\t" \
  "buffer_store_dwordx4 v[112:115], v37, s[40:43], s46 offen sc1\n\t" \
  "buffer_store_dwordx4 v[116:119], v37, s[40:43], s47 offen sc1\n\t" \
  "buffer_store_dwordx4 v[120:123], v36, s[40:43], s46 offen sc1\n\t" \
  "buffer_store_dwordx4 v[124:127], v36, s[40:43], s46 offen offset:1024 sc1\n\t" \
  "buffer_store_dwordx4 v[24:27], v37, s[40:43], s48 offen sc1\n\t" \
  "buffer_store_dwordx4 v[28:31], v37, s[40:43], s49 offen sc1\n\t" \
  "v_mov_b32 v34, s51\n\t" \
  "s_waitcnt vmcnt(0)\n\t" \
  "s_barrier\n\t" \
  "buffer_store_dwordx2 v[34:35], off, s[40:43], s50 sc1\n\t" \
  "s_mov_b32 m0, s57\n\t"
#define A_LOOP2 \
  "s_nop 4\n\t" \
  "s_mov_b32 s57, m0\n\t" \
  "L_seg_%=:\n\t" \
  "ds_read_b128 v[40:43], v39 offset:0\n\t" \
  "ds_read_b128 v[44:47], v39 offset:16384\n\t" \
  "ds_read_b128 v[48:51], v39 offset:1024\n\t" \
  "ds_read_b128 v[52:55], v39 offset:17408\n\t" \
  "s_waitcnt lgkmcnt(2)\n\t" \
  "v_mfma_f32_16x16x32_bf16 v[64:67], a[0:3], v[40:43], 0\n\t" \
  "ds_read_b128 v[56:59], v39 offset:2048\n\t" \
  "v_mfma_f32_16x16x32_bf16 v[68:71], a[0:3], v[44:47], 0\n\t" \
  "ds_read_b128 v[60:63], v39 offset:18432\n\t" \
  "v_mfma_f32_16x16x32_bf16 v[72:75], a[64:67], v[40:43], 0\n\t" \
  "buffer_store_dwordx4 v[112:115], v37, s[40:43], s46 offen sc1\n\t" \
  "v_mfma_f32_16x16x32_bf16 v[76:79], a[64:67], v[44:47], 0\n\t" \
  "v_mfma_f32_16x16x32_bf16 v[80:83], a[128:131], v[40:43], 0\n\t" \
  "v_mfma_f32_16x16x32_bf16 v[84:87], a[128:131], v[44:47], 0\n\t" \
  "v_mfma_f32_16x16x32_bf16 v[88:91], a[192:195], v[40:43], 0\n\t" \
  "v_mfma_f32_16x16x32_bf16 v[92:95], a[192:195], v[44:47], 0\n\t" \
  "s_waitcnt lgkmcnt(2)\n\t" \
  "v_mfma_f32_16x16x32_bf16 v[64:67], a[4:7], v[48:51], v[64:67]\n\t" \
  "ds_read_b128 v[40:43], v39 offset:3072\n\t" \
  "v_mfma_f32_16x16x32_bf16 v[68:71], a[4:7], v[52:55], v[68:71]\n\t" \
  "ds_read_b128 v[44:47], v39 offset:19456\n\t" \
  "v_mfma_f32_16x16x32_bf16 v[72:75], a[68:71], v[48:51], v[72:75]\n\t" \
  "v_mfma_f32_16x16x32_bf16 v[76:79], a[68:71], v[52:55], v[76:79]\n\t" \
  "buffer_store_dwordx4 v[116:119], v37, s[40:43], s47 offen sc1\n\t" \
  "v_mfma_f32_16x16x32_bf16 v[80:83], a[132:135], v[48:51], v[80:83]\n\t" \
  "v_mfma_f32_16x16x32_bf16 v[84:87], a[132:135], v[52:55], v[84:87]\n\t" \
  "v_mfma_f32_16x16x32_bf16 v[88:91], a[196:199], v[48:51], v[88:91]\n\t" \
  "v_mfma_f32_16x16x32_bf16 v[92:95], a[196:199], v[52:55], v[92:95]\n\t" \
  "s_waitcnt lgkmcnt(2)\n\t" \
  "v_mfma_f32_16x16x32_bf16 v[64:67], a[8:11], v[56:59], v[64:67]\n\t" \
  "ds_read_b128 v[48:51], v39 offset:4096\n\t" \
  "v_mfma_f32_16x16x32_bf16 v[68:71], a[8:11], v[60:63], v[68:71]\n\t" \
  "ds_read_b128 v[52:55], v39 offset:20480\n\t" \
  "v_mfma_f32_16x16x32_bf16 v[72:75], a[72:75], v[56:59], v[72:75]\n\t" \
  "v_mfma_f32_16x16x32_bf16 v[76:79], a[72:75], v[60:63], v[76:79]\n\t" \
  "v_mfma_f32_16x16x32_bf16 v[80:83], a[136:139], v[56:59], v[80:83]\n\t" \
  "s_mov_b32 m0, s45\n\t" \
  "s_add_u32 s53, s44, 4096\n\t" \
  "buffer_load_dwordx4 v38, s[40:43], s44 offen sc1 lds\n\t" \
  "v_mfma_f32_16x16x32_bf16 v[84:87], a[136:139], v[60:63], v[84:87]\n\t" \
  "v_mfma_f32_16x16x32_bf16 v[88:91], a[200:203], v[56:59], v[88:91]\n\t" \
  "v_mfma_f32_16x16x32_bf16 v[92:95], a[200:203], v[60:63], v[92:95]\n\t" \
  "s_waitcnt lgkmcnt(2)\n\t" \
  "v_mfma_f32_16x16x32_bf16 v[64:67], a[12:15], v[40:43], v[64:67]\n\t" \
  "ds_read_b128 v[56:59], v39 offset:5120\n\t" \
  "v_mfma_f32_16x16x32_bf16 v[68:71], a[12:15], v[44:47], v[68:71]\n\t" \
  "ds_read_b128 v[60:63], v39 offset:21504\n\t" \
  "v_mfma_f32_16x16x32_bf16 v[72:75], a[76:79], v[40:43], v[72:75]\n\t" \
  "v_mfma_f32_16x16x32_bf16 v[76:79], a[76:79], v[44:47], v[76:79]\n\t" \
  "v_mfma_f32_16x16x32_bf16 v[80:83], a[140:143], v[40:43], v[80:83]\n\t" \
  "v_mfma_f32_16x16x32_bf16 v[84:87], a[140:143], v[44:47], v[84:87]\n\t" \
  "buffer_load_dwordx4 v38, s[40:43], s44 offen offset:1024 sc1 lds\n\t" \
  "v_mfma_f32_16x16x32_bf16 v[88:91], a[204:207], v[40:43], v[88:91]\n\t" \
  "v_mfma_f32_16x16x32_bf16 v[92:95], a[204:207], v[44:47], v[92:95]\n\t" \
  "s_waitcnt lgkmcnt(2)\n\t" \
  "v_mfma_f32_16x16x32_bf16 v[64:67], a[16:19], v[48:51], v[64:67]\n\t" \
  "ds_read_b128 v[40:43], v39 offset:6144\n\t" \
  "v_mfma_f32_16x16x32_bf16 v[68:71], a[16:19], v[52:55], v[68:71]\n\t" \
  "ds_read_b128 v[44:47], v39 offset:22528\n\t" \
  "v_mfma_f32_16x16x32_bf16 v[72:75], a[80:83], v[48:51], v[72:75]\n\t" \
  "v_mfma_f32_16x16x32_bf16 v[76:79], a[80:83], v[52:55], v[76:79]\n\t" \
  "v_mfma_f32_16x16x32_bf16 v[80:83], a[144:147], v[48:51], v[80:83]\n\t" \
  "v_mfma_f32_16x16x32_bf16 v[84:87], a[144:147], v[52:55], v[84:87]\n\t" \
  "v_mfma_f32_16x16x32_bf16 v[88:91], a[208:211], v[48:51], v[88:91]\n\t" \
  "buffer_load_dwordx4 v38, s[40:43], s44 offen offset:2048 sc1 lds\n\t" \
  "v_mfma_f32_16x16x32_bf16 v[92:95], a[208:211], v[52:55], v[92:95]\n\t" \
  "s_waitcnt lgkmcnt(2)\n\t" \
  "v_mfma_f32_16x16x32_bf16 v[64:67], a[20:23], v[56:59], v[64:67]\n\t" \
  "ds_read_b128 v[48:51], v39 offset:7168\n\t" \
  "v_mfma_f32_16x16x32_bf16 v[68:71], a[20:23], v[60:63], v[68:71]\n\t" \
  "ds_read_b128 v[52:55], v39 offset:23552\n\t" \
  "v_mfma_f32_16x16x32_bf16 v[72:75], a[84:87], v[56:59], v[72:75]\n\t" \
  "v_mfma_f32_16x16x32_bf16 v[76:79], a[84:87], v[60:63], v[76:79]\n\t" \
  "v_mfma_f32_16x16x32_bf16 v[80:83], a[148:151], v[56:59], v[80:83]\n\t" \
  "v_mfma_f32_16x16x32_bf16 v[84:87], a[148:151], v[60:63], v[84:87]\n\t" \
  "v_mfma_f32_16x16x32_bf16 v[88:91], a[212:215], v[56:59], v[88:91]\n\t" \
  "v_mfma_f32_16x16x32_bf16 v[92:95], a[212:215], v[60:63], v[92:95]\n\t" \
  "buffer_load_dwordx4 v38, s[40:43], s44 offen offset:3072 sc1 lds\n\t" \
  "s_waitcnt lgkmcnt(2)\n\t" \
  "v_mfma_f32_16x16x32_bf16 v[64:67], a[24:27], v[40:43], v[64:67]\n\t" \
  "ds_read_b128 v[56:59], v39 offset:8192\n\t" \
  "v_mfma_f32_16x16x32_bf16 v[68:71], a[24:27], v[44:47], v[68:71]\n\t" \
  "ds_read_b128 v[60:63], v39 offset:24576\n\t" \
  "v_mfma_f32_16x16x32_bf16 v[72:75], a[88:91], v[40:43], v[72:75]\n\t" \
  "v_mfma_f32_16x16x32_bf16 v[76:79], a[88:91], v[44:47], v[76:79]\n\t" \
  "v_mfma_f32_16x16x32_bf16 v[80:83], a[152:155], v[40:43], v[80:83]\n\t" \
  "v_mfma_f32_16x16x32_bf16 v[84:87], a[152:155], v[44:47], v[84:87]\n\t" \
  "v_mfma_f32_16x16x32_bf16 v[88:91], a[216:219], v[40:43], v[88:91]\n\t" \
  "v_mfma_f32_16x16x32_bf16 v[92:95], a[216:219], v[44:47], v[92:95]\n\t" \
  "s_waitcnt lgkmcnt(2)\n\t" \
  "v_mfma_f32_16x16x32_bf16 v[64:67], a[28:31], v[48:51], v[64:67]\n\t" \
  "ds_read_b128 v[40:43], v39 offset:9216\n\t" \
  "s_add_u32 m0, s45, 4096\n\t" \
  "s_nop 0\n\t" \
  "buffer_load_dwordx4 v38, s[40:43], s53 offen sc1 lds\n\t" \
  "v_mfma_f32_16x16x32_bf16 v[68:71], a[28:31], v[52:55], v[68:71]\n\t" \
  "ds_read_b128 v[44:47], v39 offset:25600\n\t" \
  "v_mfma_f32_16x16x32_bf16 v[72:75], a[92:95], v[48:51], v[72:75]\n\t" \
  "v_mfma_f32_16x16x32_bf16 v[76:79], a[92:95], v[52:55], v[76:79]\n\t" \
  "v_mfma_f32_16x16x32_bf16 v[80:83], a[156:159], v[48:51], v[80:83]\n\t" \
  "v_mfma_f32_16x16x32_bf16 v[84:87], a[156:159], v[52:55], v[84:87]\n\t" \
  "v_mfma_f32_16x16x32_bf16 v[88:91], a[220:223], v[48:51], v[88:91]\n\t" \
  "v_mfma_f32_16x16x32_bf16 v[92:95], a[220:223], v[52:55], v[92:95]\n\t" \
  "s_waitcnt lgkmcnt(2)\n\t" \
  "v_mfma_f32_16x16x32_bf16 v[64:67], a[32:35], v[56:59], v[64:67]\n\t" \
  "ds_read_b128 v[48:51], v39 offset:10240\n\t" \
  "v_mfma_f32_16x16x32_bf16 v[68:71], a[32:35], v[60:63], v[68:71]\n\t" \
  "ds_read_b128 v[52:55], v39 offset:26624\n\t" \
  "buffer_load_dwordx4 v38, s[40:43], s53 offen offset:1024 sc1 lds\n\t" \
  "v_mfma_f32_16x16x32_bf16 v[72:75], a[96:99], v[56:59], v[72:75]\n\t" \
  "v_mfma_f32_16x16x32_bf16 v[76:79], a[96:99], v[60:63], v[76:79]\n\t" \
  "v_mfma_f32_16x16x32_bf16 v[80:83], a[160:163], v[56:59], v[80:83]\n\t" \
  "v_mfma_f32_16x16x32_bf16 v[84:87], a[160:163], v[60:63], v[84:87]\n\t" \
  "v_mfma_f32_16x16x32_bf16 v[88:91], a[224:227], v[56:59], v[88:91]\n\t" \
  "v_mfma_f32_16x16x32_bf16 v[92:95], a[224:227], v[60:63], v[92:95]\n\t" \
  "s_waitcnt lgkmcnt(2)\n\t" \
  "v_mfma_f32_16x16x32_bf16 v[64:67], a[36:39], v[40:43], v[64:67]\n\t" \
  "ds_read_b128 v[56:59], v39 offset:11264\n\t" \
  "v_mfma_f32_16x16x32_bf16 v[68:71], a[36:39], v[44:47], v[68:71]\n\t" \
  "ds_read_b128 v[60:63], v39 offset:27648\n\t" \
  "v_mfma_f32_16x16x32_bf16 v[72:75], a[100:103], v[40:43], v[72:75]\n\t" \
  "buffer_load_dwordx4 v38, s[40:43], s53 offen offset:2048 sc1 lds\n\t" \
  "v_mfma_f32_16x16x32_bf16 v[76:79], a[100:103], v[44:47], v[76:79]\n\t" \
  "v_mfma_f32_16x16x32_bf16 v[80:83], a[164:167], v[40:43], v[80:83]\n\t" \
  "v_mfma_f32_16x16x32_bf16 v[84:87], a[164:167], v[44:47], v[84:87]\n\t" \
  "v_mfma_f32_16x16x32_bf16 v[88:91], a[228:231], v[40:43], v[88:91]\n\t" \
  "v_mfma_f32_16x16x32_bf16 v[92:95], a[228:231], v[44:47], v[92:95]\n\t" \
  "s_waitcnt lgkmcnt(2)\n\t" \
  "v_mfma_f32_16x16x32_bf16 v[64:67], a[40:43], v[48:51], v[64:67]\n\t" \
  "ds_read_b128 v[40:43], v39 offset:12288\n\t" \
  "v_mfma_f32_16x16x32_bf16 v[68:71], a[40:43], v[52:55], v[68:71]\n\t" \
  "ds_read_b128 v[44:47], v39 offset:28672\n\t" \
  "v_mfma_f32_16x16x32_bf16 v[72:75], a[104:107], v[48:51], v[72:75]\n\t" \
  "v_mfma_f32_16x16x32_bf16 v[76:79], a[104:107], v[52:55], v[76:79]\n\t" \
  "buffer_load_dwordx4 v38, s[40:43], s53 offen offset:3072 sc1 lds\n\t" \
  "v_mfma_f32_16x16x32_bf16 v[80:83], a[168:171], v[48:51], v[80:83]\n\t" \
  "v_mfma_f32_16x16x32_bf16 v[84:87], a[168:171], v[52:55], v[84:87]\n\t" \
  "v_mfma_f32_16x16x32_bf16 v[88:91], a[232:235], v[48:51], v[88:91]\n\t" \
  "v_mfma_f32_16x16x32_bf16 v[92:95], a[232:235], v[52:55], v[92:95]\n\t" \
  "s_waitcnt lgkmcnt(2)\n\t" \
  "v_mfma_f32_16x16x32_bf16 v[64:67], a[44:47], v[56:59], v[64:67]\n\t" \
  "ds_read_b128 v[48:51], v39 offset:13312\n\t" \
  "v_mfma_f32_16x16x32_bf16 v[68:71], a[44:47], v[60:63], v[68:71]\n\t" \
  "ds_read_b128 v[52:55], v39 offset:29696\n\t" \
  "v_mfma_f32_16x16x32_bf16 v[72:75], a[108:111], v[56:59], v[72:75]\n\t" \
  "v_mfma_f32_16x16x32_bf16 v[76:79], a[108:111], v[60:63], v[76:79]\n\t" \
  "v_mfma_f32_16x16x32_bf16 v[80:83], a[172:175], v[56:59], v[80:83]\n\t" \
  "v_mfma_f32_16x16x32_bf16 v[84:87], a[172:175], v[60:63], v[84:87]\n\t" \
  "v_mfma_f32_16x16x32_bf16 v[88:91], a[236:239], v[56:59], v[88:91]\n\t" \
  "v_mfma_f32_16x16x32_bf16 v[92:95], a[236:239], v[60:63], v[92:95]\n\t" \
  "s_waitcnt lgkmcnt(2)\n\t" \
  "v_mfma_f32_16x16x32_bf16 v[64:67], a[48:51], v[40:43], v[64:67]\n\t" \
  "ds_read_b128 v[56:59], v39 offset:14336\n\t" \
  "v_mfma_f32_16x16x32_bf16 v[68:71], a[48:51], v[44:47], v[68:71]\n\t" \
  "ds_read_b128 v[60:63], v39 offset:30720\n\t" \
  "v_mfma_f32_16x16x32_bf16 v[72:75], a[112:115], v[40:43], v[72:75]\n\t" \
  "v_mfma_f32_16x16x32_bf16 v[76:79], a[112:115], v[44:47], v[76:79]\n\t" \
  "v_mfma_f32_16x16x32_bf16 v[80:83], a[176:179], v[40:43], v[80:83]\n\t" \
  "v_mfma_f32_16x16x32_bf16 v[84:87], a[176:179], v[44:47], v[84:87]\n\t" \
  "v_mfma_f32_16x16x32_bf16 v[88:91], a[240:243], v[40:43], v[88:91]\n\t" \
  "v_mfma_f32_16x16x32_bf16 v[92:95], a[240:243], v[44:47], v[92:95]\n\t" \
  "s_waitcnt lgkmcnt(2)\n\t" \
  "v_mfma_f32_16x16x32_bf16 v[64:67], a[52:55], v[48:51], v[64:67]\n\t" \
  "ds_read_b128 v[40:43], v39 offset:15360\n\t" \
  "v_mfma_f32_16x16x32_bf16 v[68:71], a[52:55], v[52:55], v[68:71]\n\t" \
  "ds_read_b128 v[44:47], v39 offset:31744\n\t" \
  "v_mfma_f32_16x16x32_bf16 v[72:75], a[116:119], v[48:51], v[72:75]\n\t" \
  "v_mfma_f32_16x16x32_bf16 v[76:79], a[116:119], v[52:55], v[76:79]\n\t" \
  "v_mfma_f32_16x16x32_bf16 v[80:83], a[180:183], v[48:51], v[80:83]\n\t" \
  "v_mfma_f32_16x16x32_bf16 v[84:87], a[180:183], v[52:55], v[84:87]\n\t" \
  "v_mfma_f32_16x16x32_bf16 v[88:91], a[244:247], v[48:51], v[88:91]\n\t" \
  "v_mfma_f32_16x16x32_bf16 v[92:95], a[244:247], v[52:55], v[92:95]\n\t" \
  "s_waitcnt lgkmcnt(2)\n\t" \
  "v_mfma_f32_16x16x32_bf16 v[64:67], a[56:59], v[56:59], v[64:67]\n\t" \
  "v_mfma_f32_16x16x32_bf16 v[68:71], a[56:59], v[60:63], v[68:71]\n\t" \
  "v_mfma_f32_16x16x32_bf16 v[72:75], a[120:123], v[56:59], v[72:75]\n\t" \
  "v_mfma_f32_16x16x32_bf16 v[76:79], a[120:123], v[60:63], v[76:79]\n\t" \
  "v_mfma_f32_16x16x32_bf16 v[80:83], a[184:187], v[56:59], v[80:83]\n\t" \
  "v_mfma_f32_16x16x32_bf16 v[84:87], a[184:187], v[60:63], v[84:87]\n\t" \
  "v_mfma_f32_16x16x32_bf16 v[88:91], a[248:251], v[56:59], v[88:91]\n\t" \
  "v_mfma_f32_16x16x32_bf16 v[92:95], a[248:251], v[60:63], v[92:95]\n\t" \
  "s_waitcnt lgkmcnt(0)\n\t" \
  "v_mfma_f32_16x16x32_bf16 v[64:67], a[60:63], v[40:43], v[64:67]\n\t" \
  "v_mfma_f32_16x16x32_bf16 v[68:71], a[60:63], v[44:47], v[68:71]\n\t" \
  "v_mfma_f32_16x16x32_bf16 v[72:75], a[124:127], v[40:43], v[72:75]\n\t" \
  "v_mfma_f32_16x16x32_bf16 v[76:79], a[124:127], v[44:47], v[76:79]\n\t" \
  "v_mfma_f32_16x16x32_bf16 v[80:83], a[188:191], v[40:43], v[80:83]\n\t" \
  "v_mfma_f32_16x16x32_bf16 v[84:87], a[188:191], v[44:47], v[84:87]\n\t" \
  "v_mfma_f32_16x16x32_bf16 v[88:91], a[252:255], v[40:43], v[88:91]\n\t" \
  "v_mfma_f32_16x16x32_bf16 v[92:95], a[252:255], v[44:47], v[92:95]\n\t" \
  "s_add_u32 s44, s44, 65536\n\t" \
  "s_xor_b32 s45, s45, 131072\n\t" \
  "s_sub_u32 s52, s52, 1\n\t" \
  "s_add_u32 s46, s58, 0\n\t" \
  "s_add_u32 s58, s58, 176128\n\t" \
  "s_add_u32 s47, s59, 0\n\t" \
  "s_add_u32 s59, s59, 176128\n\t" \
  "s_cmp_lt_u32 s52, 2\n\t" \
  "s_cselect_b32 s44, 0x80000000, s44\n\t" \
  "v_cvt_pk_bf16_f32 v64, v64, v72\n\t" \
  "v_cvt_pk_bf16_f32 v80, v80, v88\n\t" \
  "v_lshlrev_b32 v24, 16, v64\n\t" \
  "v_and_b32 v25, 0xffff0000, v64\n\t" \
  "v_lshlrev_b32 v26, 16, v80\n\t" \
  "v_and_b32 v27, 0xffff0000, v80\n\t" \
  "v_mul_f32 v24, v24, v26\n\t" \
  "v_mul_f32 v25, v25, v27\n\t" \
  "v_cvt_pk_bf16_f32 v112, v24, v25\n\t" \
  "v_cvt_pk_bf16_f32 v65, v65, v73\n\t" \
  "v_cvt_pk_bf16_f32 v81, v81, v89\n\t" \
  "v_lshlrev_b32 v24, 16, v65\n\t" \
  "v_and_b32 v25, 0xffff0000, v65\n\t" \
  "v_lshlrev_b32 v26, 16, v81\n\t" \
  "v_and_b32 v27, 0xffff0000, v81\n\t" \
  "v_mul_f32 v24, v24, v26\n\t" \
  "v_mul_f32 v25, v25, v27\n\t" \
  "v_cvt_pk_bf16_f32 v113, v24, v25\n\t" \
  "v_cvt_pk_bf16_f32 v66, v66, v74\n\t" \
  "v_cvt_pk_bf16_f32 v82, v82, v90\n\t" \
  "v_lshlrev_b32 v24, 16, v66\n\t" \
  "v_and_b32 v25, 0xffff0000, v66\n\t" \
  "v_lshlrev_b32 v26, 16, v82\n\t" \
  "v_and_b32 v27, 0xffff0000, v82\n\t" \
  "v_mul_f32 v24, v24, v26\n\t" \
  "v_mul_f32 v25, v25, v27\n\t" \
  "v_cvt_pk_bf16_f32 v114, v24, v25\n\t" \
  "v_cvt_pk_bf16_f32 v67, v67, v75\n\t" \
  "v_cvt_pk_bf16_f32 v83, v83, v91\n\t" \
  "v_lshlrev_b32 v24, 16, v67\n\t" \
  "v_and_b32 v25, 0xffff0000, v67\n\t" \
  "v_lshlrev_b32 v26, 16, v83\n\t" \
  "v_and_b32 v27, 0xffff0000, v83\n\t" \
  "v_mul_f32 v24, v24, v26\n\t" \
  "v_mul_f32 v25, v25, v27\n\t" \
  "v_cvt_pk_bf16_f32 v115, v24, v25\n\t" \
  "v_cvt_pk_bf16_f32 v68, v68, v76\n\t" \
  "v_cvt_pk_bf16_f32 v84, v84, v92\n\t" \
  "v_lshlrev_b32 v24, 16, v68\n\t" \
  "v_and_b32 v25, 0xffff0000, v68\n\t" \
  "v_lshlrev_b32 v26, 16, v84\n\t" \
  "v_and_b32 v27, 0xffff0000, v84\n\t" \
  "v_mul_f32 v24, v24, v26\n\t" \
  "v_mul_f32 v25, v25, v27\n\t" \
  "v_cvt_pk_bf16_f32 v116, v24, v25\n\t" \
  "v_cvt_pk_bf16_f32 v69, v69, v77\n\t" \
  "v_cvt_pk_bf16_f32 v85, v85, v93\n\t" \
  "v_lshlrev_b32 v24, 16, v69\n\t" \
  "v_and_b32 v25, 0xffff0000, v69\n\t" \
  "v_lshlrev_b32 v26, 16, v85\n\t" \
  "v_and_b32 v27, 0xffff0000, v85\n\t" \
  "v_mul_f32 v24, v24, v26\n\t" \
  "v_mul_f32 v25, v25, v27\n\t" \
  "v_cvt_pk_bf16_f32 v117, v24, v25\n\t" \
  "v_cvt_pk_bf16_f32 v70, v70, v78\n\t" \
  "v_cvt_pk_bf16_f32 v86, v86, v94\n\t" \
  "v_lshlrev_b32 v24, 16, v70\n\t" \
  "v_and_b32 v25, 0xffff0000, v70\n\t" \
  "v_lshlrev_b32 v26, 16, v86\n\t" \
  "v_and_b32 v27, 0xffff0000, v86\n\t" \
  "v_mul_f32 v24, v24, v26\n\t" \
  "v_mul_f32 v25, v25, v27\n\t" \
  "v_cvt_pk_bf16_f32 v118, v24, v25\n\t" \
  "v_cvt_pk_bf16_f32 v71, v71, v79\n\t" \
  "v_cvt_pk_bf16_f32 v87, v87, v95\n\t" \
  "v_lshlrev_b32 v24, 16, v71\n\t" \
  "v_and_b32 v25, 0xffff0000, v71\n\t" \
  "v_lshlrev_b32 v26, 16, v87\n\t" \
  "v_and_b32 v27, 0xffff0000, v87\n\t" \
  "v_mul_f32 v24, v24, v26\n\t" \
  "v_mul_f32 v25, v25, v27\n\t" \
  "v_cvt_pk_bf16_f32 v119, v24, v25\n\t" \
  "v_xor_b32 v39, 131072, v39\n\t" \
  "v_mov_b32 v34, s51\n\t" \
  "s_waitcnt vmcnt(0)\n\t" \
  "s_barrier\n\t" \
  "buffer_store_dwordx2 v[34:35], off, s[40:43], s50 sc1\n\t" \
  "s_add_u32 s51, s51, 1\n\t" \
  "s_cmp_lg_u32 s52, 0\n\t" \
  "s_cbranch_scc1 L_seg_%=\n\t" \
  "buffer_store_dwordx4 v[112:115], v37, s[40:43], s46 offen sc1\n\t" \
  "buffer_store_dwordx4 v[116:119], v37, s[40:43], s47 offen sc1\n\t" \
  "v_mov_b32 v34, s51\n\t" \
  "s_waitcnt vmcnt(0)\n\t" \
  "s_barrier\n\t" \
  "buffer_store_dwordx2 v[34:35], off, s[40:43], s50 sc1\n\t" \
  "s_mov_b32 m0, s57\n\t"
#define CLOB_AL "memory", "scc", "v34", "v24", "v25", "v26", "v27", "v28", "v29", "v30", "v31", "v40", "v41", "v42", "v43", "v44", "v45", "v46", "v47", "v48", "v49", "v50", "v51", "v52", "v53", "v54", "v55", "v56", "v57", "v58", "v59", "v60", "v61", "v62", "v63", "v64", "v65", "v66", "v67", "v68", "v69", "v70", "v71", "v72", "v73", "v74", "v75", "v76", "v77", "v78", "v79", "v80", "v81", "v82", "v83", "v84", "v85", "v86", "v87", "v88", "v89", "v90", "v91", "v92", "v93", "v94", "v95", "v96", "v97", "v98", "v99", "v100", "v101", "v102", "v103", "v104", "v105", "v106", "v107", "v108", "v109", "v110", "v111", "v112", "v113", "v114", "v115", "v116", "v117", "v118", "v119", "v120", "v121", "v122", "v123", "v124", "v125", "v126", "v127"
#define OUT_H_ASM \
  "s_nop 4\n\t" \
  "buffer_load_dwordx4 v[64:67], v31, s[40:43], s44 offen offset:0 sc1\n\t" \
  "buffer_load_dwordx4 v[68:71], v31, s[40:43], s44 offen offset:1024 sc1\n\t" \
  "buffer_load_dwordx4 v[72:75], v31, s[40:43], s44 offen offset:2048 sc1\n\t" \
  "buffer_load_dwordx4 v[76:79], v31, s[40:43], s44 offen offset:3072 sc1\n\t" \
  "buffer_load_dwordx4 v[80:83], v30, s[40:43], s44 offen offset:0 sc1\n\t" \
  "buffer_load_dwordx4 v[84:87], v30, s[40:43], s44 offen offset:1024 sc1\n\t" \
  "buffer_load_dwordx4 v[88:91], v30, s[40:43], s44 offen offset:2048 sc1\n\t" \
  "buffer_load_dwordx4 v[92:95], v30, s[40:43], s44 offen offset:3072 sc1\n\t" \
  "buffer_load_dwordx4 v[96:99], v29, s[40:43], s44 offen offset:0 sc1\n\t" \
  "buffer_load_dwordx4 v[100:103], v29, s[40:43], s44 offen offset:1024 sc1\n\t" \
  "buffer_load_dwordx4 v[104:107], v28, s[40:43], s44 offen offset:2048 sc1\n\t" \
  "s_waitcnt vmcnt(10)\n\t" \
  "v_mfma_f32_16x16x32_bf16 v[32:35], v[160:163], v[64:67], 0\n\t" \
  "v_mfma_f32_16x16x32_bf16 v[36:39], v[164:167], v[64:67], 0\n\t" \
  "s_waitcnt vmcnt(9)\n\t" \
  "v_mfma_f32_16x16x32_bf16 v[32:35], v[168:171], v[68:71], v[32:35]\n\t" \
  "v_mfma_f32_16x16x32_bf16 v[36:39], v[172:175], v[68:71], v[36:39]\n\t" \
  "s_waitcnt vmcnt(8)\n\t" \
  "v_mfma_f32_16x16x32_bf16 v[32:35], v[176:179], v[72:75], v[32:35]\n\t" \
  "v_mfma_f32_16x16x32_bf16 v[36:39], v[180:183], v[72:75], v[36:39]\n\t" \
  "s_waitcnt vmcnt(7)\n\t" \
  "v_mfma_f32_16x16x32_bf16 v[32:35], v[184:187], v[76:79], v[32:35]\n\t" \
  "v_mfma_f32_16x16x32_bf16 v[36:39], v[188:191], v[76:79], v[36:39]\n\t" \
  "s_waitcnt vmcnt(6)\n\t" \
  "v_mfma_f32_16x16x32_bf16 v[32:35], v[192:195], v[80:83], v[32:35]\n\t" \
  "v_mfma_f32_16x16x32_bf16 v[36:39], v[196:199], v[80:83], v[36:39]\n\t" \
  "s_waitcnt vmcnt(5)\n\t" \
  "v_mfma_f32_16x16x32_bf16 v[32:35], v[200:203], v[84:87], v[32:35]\n\t" \
  "v_mfma_f32_16x16x32_bf16 v[36:39], v[204:207], v[84:87], v[36:39]\n\t" \
  "s_waitcnt vmcnt(4)\n\t" \
  "v_mfma_f32_16x16x32_bf16 v[32:35], v[208:211], v[88:91], v[32:35]\n\t" \
  "v_mfma_f32_16x16x32_bf16 v[36:39], v[212:215], v[88:91], v[36:39]\n\t" \
  "s_waitcnt vmcnt(3)\n\t" \
  "v_mfma_f32_16x16x32_bf16 v[32:35], v[216:219], v[92:95], v[32:35]\n\t" \
  "v_mfma_f32_16x16x32_bf16 v[36:39], v[220:223], v[92:95], v[36:39]\n\t" \
  "s_waitcnt vmcnt(2)\n\t" \
  "v_mfma_f32_16x16x32_bf16 v[32:35], v[224:227], v[96:99], v[32:35]\n\t" \
  "v_mfma_f32_16x16x32_bf16 v[36:39], v[228:231], v[96:99], v[36:39]\n\t" \
  "s_waitcnt vmcnt(1)\n\t" \
  "v_mfma_f32_16x16x32_bf16 v[32:35], v[232:235], v[100:103], v[32:35]\n\t" \
  "v_mfma_f32_16x16x32_bf16 v[36:39], v[236:239], v[100:103], v[36:39]\n\t" \
  "s_waitcnt vmcnt(0)\n\t" \
  "v_mfma_f32_16x16x32_bf16 v[32:35], v[240:243], v[104:107], v[32:35]\n\t" \
  "v_mfma_f32_16x16x32_bf16 v[36:39], v[244:247], v[104:107], v[36:39]\n\t" \
  "s_nop 7\n\t"
#define OUT_O_ASM \
  "s_nop 4\n\t" \
  "s_mov_b32 s54, m0\n\t" \
  "s_add_u32 s52, s56, 0\n\t" \
  "s_and_b32 s52, s52, 0x7fff\n\t" \
  "s_add_u32 s52, s52, s44\n\t" \
  "s_add_u32 m0, s53, 12288\n\t" \
  "s_nop 0\n\t" \
  "buffer_load_dwordx4 v27, s[40:43], s52 offen offset:0 sc1 lds\n\t" \
  "s_add_u32 m0, s53, 12288\n\t" \
  "s_nop 0\n\t" \
  "buffer_load_dwordx4 v27, s[40:43], s52 offen offset:1024 sc1 lds\n\t" \
  "s_add_u32 m0, s53, 12288\n\t" \
  "s_nop 0\n\t" \
  "buffer_load_dwordx4 v27, s[40:43], s52 offen offset:2048 sc1 lds\n\t" \
  "s_add_u32 m0, s53, 12288\n\t" \
  "s_nop 0\n\t" \
  "buffer_load_dwordx4 v27, s[40:43], s52 offen offset:3072 sc1 lds\n\t" \
  "s_add_u32 s52, s56, 4096\n\t" \
  "s_and_b32 s52, s52, 0x7fff\n\t" \
  "s_add_u32 s52, s52, s44\n\t" \
  "s_add_u32 m0, s53, 16384\n\t" \
  "s_nop 0\n\t" \
  "buffer_load_dwordx4 v27, s[40:43], s52 offen offset:0 sc1 lds\n\t" \
  "s_add_u32 m0, s53, 16384\n\t" \
  "s_nop 0\n\t" \
  "buffer_load_dwordx4 v27, s[40:43], s52 offen offset:1024 sc1 lds\n\t" \
  "s_add_u32 m0, s53, 16384\n\t" \
  "s_nop 0\n\t" \
  "buffer_load_dwordx4 v27, s[40:43], s52 offen offset:2048 sc1 lds\n\t" \
  "s_add_u32 m0, s53, 16384\n\t" \
  "s_nop 0\n\t" \
  "buffer_load_dwordx4 v27, s[40:43], s52 offen offset:3072 sc1 lds\n\t" \
  "s_add_u32 s52, s56, 8192\n\t" \
  "s_and_b32 s52, s52, 0x7fff\n\t" \
  "s_add_u32 s52, s52, s44\n\t" \
  "s_add_u32 m0, s53, 20480\n\t" \
  "s_nop 0\n\t" \
  "buffer_load_dwordx4 v27, s[40:43], s52 offen offset:0 sc1 lds\n\t" \
  "s_add_u32 m0, s53, 20480\n\t" \
  "s_nop 0\n\t" \
  "buffer_load_dwordx4 v27, s[40:43], s52 offen offset:1024 sc1 lds\n\t" \
  "s_add_u32 m0, s53, -2048\n\t" \
  "s_nop 0\n\t" \
  "buffer_load_dwordx4 v27, s[40:43], s52 offen offset:2048 sc1 lds\n\t" \
  "s_add_u32 m0, s53, -2048\n\t" \
  "s_nop 0\n\t" \
  "buffer_load_dwordx4 v27, s[40:43], s52 offen offset:3072 sc1 lds\n\t" \
  "s_add_u32 s52, s56, 12288\n\t" \
  "s_and_b32 s52, s52, 0x7fff\n\t" \
  "s_add_u32 s52, s52, s44\n\t" \
  "s_add_u32 m0, s53, 2048\n\t" \
  "s_nop 0\n\t" \
  "buffer_load_dwordx4 v27, s[40:43], s52 offen offset:0 sc1 lds\n\t" \
  "s_add_u32 m0, s53, 2048\n\t" \
  "s_nop 0\n\t" \
  "buffer_load_dwordx4 v27, s[40:43], s52 offen offset:1024 sc1 lds\n\t" \
  "s_add_u32 m0, s53, 2048\n\t" \
  "s_nop 0\n\t" \
  "buffer_load_dwordx4 v27, s[40:43], s52 offen offset:2048 sc1 lds\n\t" \
  "s_add_u32 m0, s53, 2048\n\t" \
  "s_nop 0\n\t" \
  "buffer_load_dwordx4 v27, s[40:43], s52 offen offset:3072 sc1 lds\n\t" \
  "s_add_u32 s52, s56, 16384\n\t" \
  "s_and_b32 s52, s52, 0x7fff\n\t" \
  "s_add_u32 s52, s52, s44\n\t" \
  "s_add_u32 m0, s53, 6144\n\t" \
  "s_nop 0\n\t" \
  "buffer_load_dwordx4 v27, s[40:43], s52 offen offset:0 sc1 lds\n\t" \
  "s_add_u32 m0, s53, 6144\n\t" \
  "s_nop 0\n\t" \
  "buffer_load_dwordx4 v27, s[40:43], s52 offen offset:1024 sc1 lds\n\t" \
  "s_add_u32 m0, s53, 6144\n\t" \
  "s_nop 0\n\t" \
  "buffer_load_dwordx4 v27, s[40:43], s52 offen offset:2048 sc1 lds\n\t" \
  "s_add_u32 m0, s53, 6144\n\t" \
  "s_nop 0\n\t" \
  "buffer_load_dwordx4 v27, s[40:43], s52 offen offset:3072 sc1 lds\n\t" \
  "s_add_u32 s52, s56, 20480\n\t" \
  "s_and_b32 s52, s52, 0x7fff\n\t" \
  "s_add_u32 s52, s52, s44\n\t" \
  "s_add_u32 m0, s53, 10240\n\t" \
  "s_nop 0\n\t" \
  "buffer_load_dwordx4 v27, s[40:43], s52 offen offset:0 sc1 lds\n\t" \
  "s_add_u32 m0, s53, 10240\n\t" \
  "s_nop 0\n\t" \
  "buffer_load_dwordx4 v27, s[40:43], s52 offen offset:1024 sc1 lds\n\t" \
  "L_run_%=:\n\t" \
  "s_waitcnt vmcnt(21)\n\t" \
  "ds_read_b128 v[48:51], v25 offset:12288\n\t" \
  "s_waitcnt vmcnt(20)\n\t" \
  "ds_read_b128 v[52:55], v25 offset:13312\n\t" \
  "s_waitcnt vmcnt(19)\n\t" \
  "ds_read_b128 v[56:59], v25 offset:14336\n\t" \
  "s_waitcnt lgkmcnt(2)\n\t" \
  "v_mfma_f32_16x16x32_bf16 v[40:43], a[0:3], v[48:51], 0\n\t" \
  "v_mfma_f32_16x16x32_bf16 v[44:47], a[4:7], v[48:51], 0\n\t" \
  "s_add_u32 m0, s53, 10240\n\t" \
  "s_nop 0\n\t" \
  "buffer_load_dwordx4 v27, s[40:43], s52 offen offset:2048 sc1 lds\n\t" \
  "s_waitcnt vmcnt(19)\n\t" \
  "ds_read_b128 v[60:63], v25 offset:15360\n\t" \
  "s_waitcnt lgkmcnt(2)\n\t" \
  "v_mfma_f32_16x16x32_bf16 v[40:43], a[8:11], v[52:55], v[40:43]\n\t" \
  "v_mfma_f32_16x16x32_bf16 v[44:47], a[12:15], v[52:55], v[44:47]\n\t" \
  "s_add_u32 m0, s53, 10240\n\t" \
  "s_nop 0\n\t" \
  "buffer_load_dwordx4 v27, s[40:43], s52 offen offset:3072 sc1 lds\n\t" \
  "s_waitcnt vmcnt(19)\n\t" \
  "ds_read_b128 v[48:51], v25 offset:16384\n\t" \
  "s_waitcnt lgkmcnt(2)\n\t" \
  "v_mfma_f32_16x16x32_bf16 v[40:43], a[16:19], v[56:59], v[40:43]\n\t" \
  "v_mfma_f32_16x16x32_bf16 v[44:47], a[20:23], v[56:59], v[44:47]\n\t" \
  "s_add_u32 s52, s56, 24576\n\t" \
  "s_and_b32 s52, s52, 0x7fff\n\t" \
  "s_add_u32 s52, s52, s44\n\t" \
  "s_add_u32 m0, s53, 14336\n\t" \
  "s_nop 0\n\t" \
  "buffer_load_dwordx4 v27, s[40:43], s52 offen offset:0 sc1 lds\n\t" \
  "s_waitcnt vmcnt(19)\n\t" \
  "ds_read_b128 v[52:55], v25 offset:17408\n\t" \
  "s_waitcnt lgkmcnt(2)\n\t" \
  "v_mfma_f32_16x16x32_bf16 v[40:43], a[24:27], v[60:63], v[40:43]\n\t" \
  "v_mfma_f32_16x16x32_bf16 v[44:47], a[28:31], v[60:63], v[44:47]\n\t" \
  "s_add_u32 m0, s53, 14336\n\t" \
  "s_nop 0\n\t" \
  "buffer_load_dwordx4 v27, s[40:43], s52 offen offset:1024 sc1 lds\n\t" \
  "s_waitcnt vmcnt(19)\n\t" \
  "ds_read_b128 v[56:59], v25 offset:18432\n\t" \
  "s_waitcnt lgkmcnt(2)\n\t" \
  "v_mfma_f32_16x16x32_bf16 v[40:43], a[32:35], v[48:51], v[40:43]\n\t" \
  "v_mfma_f32_16x16x32_bf16 v[44:47], a[36:39], v[48:51], v[44:47]\n\t" \
  "s_add_u32 m0, s53, 14336\n\t" \
  "s_nop 0\n\t" \
  "buffer_load_dwordx4 v27, s[40:43], s52 offen offset:2048 sc1 lds\n\t" \
  "s_waitcnt vmcnt(19)\n\t" \
  "ds_read_b128 v[60:63], v25 offset:19456\n\t" \
  "s_waitcnt lgkmcnt(2)\n\t" \
  "v_mfma_f32_16x16x32_bf16 v[40:43], a[40:43], v[52:55], v[40:43]\n\t" \
  "v_mfma_f32_16x16x32_bf16 v[44:47], a[44:47], v[52:55], v[44:47]\n\t" \
  "s_add_u32 m0, s53, 14336\n\t" \
  "s_nop 0\n\t" \
  "buffer_load_dwordx4 v27, s[40:43], s52 offen offset:3072 sc1 lds\n\t" \
  "s_waitcnt vmcnt(19)\n\t" \
  "ds_read_b128 v[48:51], v25 offset:20480\n\t" \
  "s_waitcnt lgkmcnt(2)\n\t" \
  "v_mfma_f32_16x16x32_bf16 v[40:43], a[48:51], v[56:59], v[40:43]\n\t" \
  "v_mfma_f32_16x16x32_bf16 v[44:47], a[52:55], v[56:59], v[44:47]\n\t" \
  "s_add_u32 s52, s56, 28672\n\t" \
  "s_and_b32 s52, s52, 0x7fff\n\t" \
  "s_add_u32 s52, s52, s44\n\t" \
  "s_add_u32 m0, s53, 18432\n\t" \
  "s_nop 0\n\t" \
  "buffer_load_dwordx4 v27, s[40:43], s52 offen offset:0 sc1 lds\n\t" \
  "s_waitcnt vmcnt(19)\n\t" \
  "ds_read_b128 v[52:55], v25 offset:21504\n\t" \
  "s_waitcnt lgkmcnt(2)\n\t" \
  "v_mfma_f32_16x16x32_bf16 v[40:43], a[56:59], v[60:63], v[40:43]\n\t" \
  "v_mfma_f32_16x16x32_bf16 v[44:47], a[60:63], v[60:63], v[44:47]\n\t" \
  "s_add_u32 m0, s53, 18432\n\t" \
  "s_nop 0\n\t" \
  "buffer_load_dwordx4 v27, s[40:43], s52 offen offset:1024 sc1 lds\n\t" \
  "s_waitcnt vmcnt(19)\n\t" \
  "ds_read_b128 v[56:59], v25 offset:0\n\t" \
  "s_waitcnt lgkmcnt(2)\n\t" \
  "v_mfma_f32_16x16x32_bf16 v[40:43], a[64:67], v[48:51], v[40:43]\n\t" \
  "v_mfma_f32_16x16x32_bf16 v[44:47], a[68:71], v[48:51], v[44:47]\n\t" \
  "s_add_u32 m0, s53, 18432\n\t" \
  "s_nop 0\n\t" \
  "buffer_load_dwordx4 v27, s[40:43], s52 offen offset:2048 sc1 lds\n\t" \
  "s_waitcnt vmcnt(19)\n\t" \
  "ds_read_b128 v[60:63], v25 offset:1024\n\t" \
  "s_waitcnt lgkmcnt(2)\n\t" \
  "v_mfma_f32_16x16x32_bf16 v[40:43], a[72:75], v[52:55], v[40:43]\n\t" \
  "v_mfma_f32_16x16x32_bf16 v[44:47], a[76:79], v[52:55], v[44:47]\n\t" \
  "s_add_u32 m0, s53, 18432\n\t" \
  "s_nop 0\n\t" \
  "buffer_load_dwordx4 v27, s[40:43], s52 offen offset:3072 sc1 lds\n\t" \
  "s_waitcnt vmcnt(19)\n\t" \
  "ds_read_b128 v[48:51], v25 offset:2048\n\t" \
  "s_waitcnt lgkmcnt(2)\n\t" \
  "v_mfma_f32_16x16x32_bf16 v[40:43], a[80:83], v[56:59], v[40:43]\n\t" \
  "v_mfma_f32_16x16x32_bf16 v[44:47], a[84:87], v[56:59], v[44:47]\n\t" \
  "s_waitcnt vmcnt(18)\n\t" \
  "ds_read_b128 v[52:55], v25 offset:3072\n\t" \
  "s_waitcnt lgkmcnt(2)\n\t" \
  "v_mfma_f32_16x16x32_bf16 v[40:43], a[88:91], v[60:63], v[40:43]\n\t" \
  "v_mfma_f32_16x16x32_bf16 v[44:47], a[92:95], v[60:63], v[44:47]\n\t" \
  "s_waitcnt vmcnt(17)\n\t" \
  "ds_read_b128 v[56:59], v25 offset:4096\n\t" \
  "s_waitcnt lgkmcnt(2)\n\t" \
  "v_mfma_f32_16x16x32_bf16 v[40:43], a[96:99], v[48:51], v[40:43]\n\t" \
  "v_mfma_f32_16x16x32_bf16 v[44:47], a[100:103], v[48:51], v[44:47]\n\t" \
  "s_waitcnt vmcnt(16)\n\t" \
  "ds_read_b128 v[60:63], v25 offset:5120\n\t" \
  "s_waitcnt lgkmcnt(2)\n\t" \
  "v_mfma_f32_16x16x32_bf16 v[40:43], a[104:107], v[52:55], v[40:43]\n\t" \
  "v_mfma_f32_16x16x32_bf16 v[44:47], a[108:111], v[52:55], v[44:47]\n\t" \
  "s_waitcnt vmcnt(15)\n\t" \
  "ds_read_b128 v[48:51], v25 offset:6144\n\t" \
  "s_waitcnt lgkmcnt(2)\n\t" \
  "v_mfma_f32_16x16x32_bf16 v[40:43], a[112:115], v[56:59], v[40:43]\n\t" \
  "v_mfma_f32_16x16x32_bf16 v[44:47], a[116:119], v[56:59], v[44:47]\n\t" \
  "s_waitcnt vmcnt(14)\n\t" \
  "ds_read_b128 v[52:55], v25 offset:7168\n\t" \
  "s_waitcnt lgkmcnt(2)\n\t" \
  "v_mfma_f32_16x16x32_bf16 v[40:43], a[120:123], v[60:63], v[40:43]\n\t" \
  "v_mfma_f32_16x16x32_bf16 v[44:47], a[124:127], v[60:63], v[44:47]\n\t" \
  "s_waitcnt vmcnt(13)\n\t" \
  "ds_read_b128 v[56:59], v25 offset:8192\n\t" \
  "s_waitcnt lgkmcnt(2)\n\t" \
  "v_mfma_f32_16x16x32_bf16 v[40:43], a[128:131], v[48:51], v[40:43]\n\t" \
  "v_mfma_f32_16x16x32_bf16 v[44:47], a[132:135], v[48:51], v[44:47]\n\t" \
  "s_waitcnt vmcnt(12)\n\t" \
  "ds_read_b128 v[60:63], v25 offset:9216\n\t" \
  "s_waitcnt lgkmcnt(2)\n\t" \
  "v_mfma_f32_16x16x32_bf16 v[40:43], a[136:139], v[52:55], v[40:43]\n\t" \
  "v_mfma_f32_16x16x32_bf16 v[44:47], a[140:143], v[52:55], v[44:47]\n\t" \
  "s_waitcnt vmcnt(11)\n\t" \
  "ds_read_b128 v[48:51], v25 offset:10240\n\t" \
  "s_waitcnt lgkmcnt(2)\n\t" \
  "v_mfma_f32_16x16x32_bf16 v[40:43], a[144:147], v[56:59], v[40:43]\n\t" \
  "v_mfma_f32_16x16x32_bf16 v[44:47], a[148:151], v[56:59], v[44:47]\n\t" \
  "s_waitcnt vmcnt(10)\n\t" \
  "ds_read_b128 v[52:55], v25 offset:11264\n\t" \
  "s_waitcnt lgkmcnt(2)\n\t" \
  "v_mfma_f32_16x16x32_bf16 v[40:43], a[152:155], v[60:63], v[40:43]\n\t" \
  "v_mfma_f32_16x16x32_bf16 v[44:47], a[156:159], v[60:63], v[44:47]\n\t" \
  "s_waitcnt vmcnt(9)\n\t" \
  "ds_read_b128 v[56:59], v25 offset:12288\n\t" \
  "s_waitcnt lgkmcnt(2)\n\t" \
  "v_mfma_f32_16x16x32_bf16 v[40:43], a[160:163], v[48:51], v[40:43]\n\t" \
  "v_mfma_f32_16x16x32_bf16 v[44:47], a[164:167], v[48:51], v[44:47]\n\t" \
  "s_waitcnt vmcnt(8)\n\t" \
  "ds_read_b128 v[60:63], v25 offset:13312\n\t" \
  "s_waitcnt lgkmcnt(2)\n\t" \
  "v_mfma_f32_16x16x32_bf16 v[40:43], a[168:171], v[52:55], v[40:43]\n\t" \
  "v_mfma_f32_16x16x32_bf16 v[44:47], a[172:175], v[52:55], v[44:47]\n\t" \
  "s_waitcnt vmcnt(7)\n\t" \
  "ds_read_b128 v[48:51], v25 offset:14336\n\t" \
  "s_waitcnt lgkmcnt(2)\n\t" \
  "v_mfma_f32_16x16x32_bf16 v[40:43], a[176:179], v[56:59], v[40:43]\n\t" \
  "v_mfma_f32_16x16x32_bf16 v[44:47], a[180:183], v[56:59], v[44:47]\n\t" \
  "s_waitcnt vmcnt(6)\n\t" \
  "ds_read_b128 v[52:55], v25 offset:15360\n\t" \
  "s_waitcnt lgkmcnt(2)\n\t" \
  "v_mfma_f32_16x16x32_bf16 v[40:43], a[184:187], v[60:63], v[40:43]\n\t" \
  "v_mfma_f32_16x16x32_bf16 v[44:47], a[188:191], v[60:63], v[44:47]\n\t" \
  "s_waitcnt vmcnt(5)\n\t" \
  "ds_read_b128 v[56:59], v25 offset:16384\n\t" \
  "s_waitcnt lgkmcnt(2)\n\t" \
  "v_mfma_f32_16x16x32_bf16 v[40:43], a[192:195], v[48:51], v[40:43]\n\t" \
  "v_mfma_f32_16x16x32_bf16 v[44:47], a[196:199], v[48:51], v[44:47]\n\t" \
  "s_waitcnt vmcnt(4)\n\t" \
  "ds_read_b128 v[60:63], v25 offset:17408\n\t" \
  "s_waitcnt lgkmcnt(2)\n\t" \
  "v_mfma_f32_16x16x32_bf16 v[40:43], a[200:203], v[52:55], v[40:43]\n\t" \
  "v_mfma_f32_16x16x32_bf16 v[44:47], a[204:207], v[52:55], v[44:47]\n\t" \
  "s_waitcnt vmcnt(3)\n\t" \
  "ds_read_b128 v[48:51], v25 offset:18432\n\t" \
  "s_waitcnt lgkmcnt(2)\n\t" \
  "v_mfma_f32_16x16x32_bf16 v[40:43], a[208:211], v[56:59], v[40:43]\n\t" \
  "v_mfma_f32_16x16x32_bf16 v[44:47], a[212:215], v[56:59], v[44:47]\n\t" \
  "s_waitcnt vmcnt(2)\n\t" \
  "ds_read_b128 v[52:55], v25 offset:19456\n\t" \
  "s_waitcnt lgkmcnt(2)\n\t" \
  "v_mfma_f32_16x16x32_bf16 v[40:43], a[216:219], v[60:63], v[40:43]\n\t" \
  "v_mfma_f32_16x16x32_bf16 v[44:47], a[220:223], v[60:63], v[44:47]\n\t" \
  "s_waitcnt vmcnt(1)\n\t" \
  "ds_read_b128 v[56:59], v25 offset:20480\n\t" \
  "s_waitcnt lgkmcnt(2)\n\t" \
  "v_mfma_f32_16x16x32_bf16 v[40:43], a[224:227], v[48:51], v[40:43]\n\t" \
  "v_mfma_f32_16x16x32_bf16 v[44:47], a[228:231], v[48:51], v[44:47]\n\t" \
  "s_waitcnt vmcnt(0)\n\t" \
  "ds_read_b128 v[60:63], v25 offset:21504\n\t" \
  "s_waitcnt lgkmcnt(2)\n\t" \
  "v_mfma_f32_16x16x32_bf16 v[40:43], a[232:235], v[52:55], v[40:43]\n\t" \
  "v_mfma_f32_16x16x32_bf16 v[44:47], a[236:239], v[52:55], v[44:47]\n\t" \
  "s_waitcnt lgkmcnt(1)\n\t" \
  "v_mfma_f32_16x16x32_bf16 v[40:43], a[240:243], v[56:59], v[40:43]\n\t" \
  "v_mfma_f32_16x16x32_bf16 v[44:47], a[244:247], v[56:59], v[44:47]\n\t" \
  "s_waitcnt lgkmcnt(0)\n\t" \
  "v_mfma_f32_16x16x32_bf16 v[40:43], a[248:251], v[60:63], v[40:43]\n\t" \
  "v_mfma_f32_16x16x32_bf16 v[44:47], a[252:255], v[60:63], v[44:47]\n\t" \
  "L_done_%=:\n\t" \
  "s_mov_b32 m0, s54\n\t" \
  "s_nop 6\n\t"
#define OUT_M_ASM \
  "s_nop 4\n\t" \
  "s_mov_b32 s54, m0\n\t" \
  "s_bitcmp1_b32 s48, 0\n\t" \
  "s_cbranch_scc1 L_run_%=\n\t" \
  "s_add_u32 m0, s53, 0\n\t" \
  "s_nop 0\n\t" \
  "buffer_load_dwordx4 v26, s[8:11], s47 offen lds\n\t" \
  "s_add_u32 m0, s53, 1024\n\t" \
  "s_nop 0\n\t" \
  "buffer_load_dwordx4 v31, s[40:43], s45 offen offset:0 sc1 lds\n\t" \
  "s_add_u32 m0, s53, 1024\n\t" \
  "s_nop 0\n\t" \
  "buffer_load_dwordx4 v31, s[40:43], s45 offen offset:1024 sc1 lds\n\t" \
  "s_add_u32 m0, s53, 1024\n\t" \
  "s_nop 0\n\t" \
  "buffer_load_dwordx4 v31, s[40:43], s45 offen offset:2048 sc1 lds\n\t" \
  "s_add_u32 m0, s53, 1024\n\t" \
  "s_nop 0\n\t" \
  "buffer_load_dwordx4 v31, s[40:43], s45 offen offset:3072 sc1 lds\n\t" \
  "s_add_u32 m0, s53, 5120\n\t" \
  "s_nop 0\n\t" \
  "buffer_load_dwordx4 v30, s[40:43], s45 offen offset:0 sc1 lds\n\t" \
  "s_add_u32 m0, s53, 5120\n\t" \
  "s_nop 0\n\t" \
  "buffer_load_dwordx4 v30, s[40:43], s45 offen offset:1024 sc1 lds\n\t" \
  "s_add_u32 m0, s53, 5120\n\t" \
  "s_nop 0\n\t" \
  "buffer_load_dwordx4 v30, s[40:43], s45 offen offset:2048 sc1 lds\n\t" \
  "s_add_u32 m0, s53, 5120\n\t" \
  "s_nop 0\n\t" \
  "buffer_load_dwordx4 v30, s[40:43], s45 offen offset:3072 sc1 lds\n\t" \
  "s_add_u32 m0, s53, 9216\n\t" \
  "s_nop 0\n\t" \
  "buffer_load_dwordx4 v29, s[40:43], s45 offen offset:0 sc1 lds\n\t" \
  "s_add_u32 m0, s53, 9216\n\t" \
  "s_nop 0\n\t" \
  "buffer_load_dwordx4 v29, s[40:43], s45 offen offset:1024 sc1 lds\n\t" \
  "s_add_u32 m0, s53, 9216\n\t" \
  "s_nop 0\n\t" \
  "buffer_load_dwordx4 v28, s[40:43], s45 offen offset:2048 sc1 lds\n\t" \
  "s_add_u32 s52, s56, 0\n\t" \
  "s_and_b32 s52, s52, 0x7fff\n\t" \
  "s_add_u32 s52, s52, s44\n\t" \
  "s_add_u32 m0, s53, 12288\n\t" \
  "s_nop 0\n\t" \
  "buffer_load_dwordx4 v27, s[40:43], s52 offen offset:0 sc1 lds\n\t" \
  "s_add_u32 m0, s53, 12288\n\t" \
  "s_nop 0\n\t" \
  "buffer_load_dwordx4 v27, s[40:43], s52 offen offset:1024 sc1 lds\n\t" \
  "s_add_u32 m0, s53, 12288\n\t" \
  "s_nop 0\n\t" \
  "buffer_load_dwordx4 v27, s[40:43], s52 offen offset:2048 sc1 lds\n\t" \
  "s_add_u32 m0, s53, 12288\n\t" \
  "s_nop 0\n\t" \
  "buffer_load_dwordx4 v27, s[40:43], s52 offen offset:3072 sc1 lds\n\t" \
  "s_add_u32 s52, s56, 4096\n\t" \
  "s_and_b32 s52, s52, 0x7fff\n\t" \
  "s_add_u32 s52, s52, s44\n\t" \
  "s_add_u32 m0, s53, 16384\n\t" \
  "s_nop 0\n\t" \
  "buffer_load_dwordx4 v27, s[40:43], s52 offen offset:0 sc1 lds\n\t" \
  "s_add_u32 m0, s53, 16384\n\t" \
  "s_nop 0\n\t" \
  "buffer_load_dwordx4 v27, s[40:43], s52 offen offset:1024 sc1 lds\n\t" \
  "s_add_u32 m0, s53, 16384\n\t" \
  "s_nop 0\n\t" \
  "buffer_load_dwordx4 v27, s[40:43], s52 offen offset:2048 sc1 lds\n\t" \
  "s_add_u32 m0, s53, 16384\n\t" \
  "s_nop 0\n\t" \
  "buffer_load_dwordx4 v27, s[40:43], s52 offen offset:3072 sc1 lds\n\t" \
  "s_add_u32 s52, s56, 8192\n\t" \
  "s_and_b32 s52, s52, 0x7fff\n\t" \
  "s_add_u32 s52, s52, s44\n\t" \
  "s_add_u32 m0, s53, 20480\n\t" \
  "s_nop 0\n\t" \
  "buffer_load_dwordx4 v27, s[40:43], s52 offen offset:0 sc1 lds\n\t" \
  "s_add_u32 m0, s53, 20480\n\t" \
  "s_nop 0\n\t" \
  "buffer_load_dwordx4 v27, s[40:43], s52 offen offset:1024 sc1 lds\n\t" \
  "L_run_%=:\n\t" \
  "s_add_u32 s52, s56, 8192\n\t" \
  "s_and_b32 s52, s52, 0x7fff\n\t" \
  "s_add_u32 s52, s52, s44\n\t" \
  "s_add_u32 s49, s44, 786432\n\t" \
  "s_add_u32 s50, s45, 264192\n\t" \
  "s_add_u32 s51, s47, 98304\n\t" \
  "s_waitcnt vmcnt(21)\n\t" \
  "ds_read_b128 v[20:23], v25 offset:0\n\t" \
  "s_waitcnt vmcnt(20)\n\t" \
  "ds_read_b128 v[52:55], v25 offset:1024\n\t" \
  "s_waitcnt vmcnt(19)\n\t" \
  "ds_read_b128 v[56:59], v25 offset:2048\n\t" \
  "s_waitcnt lgkmcnt(2)\n\t" \
  "s_add_u32 m0, s53, -2048\n\t" \
  "s_nop 0\n\t" \
  "buffer_load_dwordx4 v27, s[40:43], s52 offen offset:2048 sc1 lds\n\t" \
  "s_waitcnt vmcnt(19)\n\t" \
  "ds_read_b128 v[60:63], v25 offset:3072\n\t" \
  "s_waitcnt lgkmcnt(2)\n\t" \
  "v_mfma_f32_16x16x32_bf16 v[32:35], v[160:163], v[52:55], 0\n\t" \
  "v_mfma_f32_16x16x32_bf16 v[36:39], v[164:167], v[52:55], 0\n\t" \
  "s_add_u32 m0, s53, -2048\n\t" \
  "s_nop 0\n\t" \
  "buffer_load_dwordx4 v27, s[40:43], s52 offen offset:3072 sc1 lds\n\t" \
  "s_waitcnt vmcnt(19)\n\t" \
  "ds_read_b128 v[48:51], v25 offset:4096\n\t" \
  "s_waitcnt lgkmcnt(2)\n\t" \
  "v_mfma_f32_16x16x32_bf16 v[32:35], v[168:171], v[56:59], v[32:35]\n\t" \
  "v_mfma_f32_16x16x32_bf16 v[36:39], v[172:175], v[56:59], v[36:39]\n\t" \
  "s_add_u32 s52, s56, 12288\n\t" \
  "s_and_b32 s52, s52, 0x7fff\n\t" \
  "s_add_u32 s52, s52, s44\n\t" \
  "s_add_u32 m0, s53, 2048\n\t" \
  "s_nop 0\n\t" \
  "buffer_load_dwordx4 v27, s[40:43], s52 offen offset:0 sc1 lds\n\t" \
  "s_waitcnt vmcnt(19)\n\t" \
  "ds_read_b128 v[52:55], v25 offset:5120\n\t" \
  "s_waitcnt lgkmcnt(2)\n\t" \
  "v_mfma_f32_16x16x32_bf16 v[32:35], v[176:179], v[60:63], v[32:35]\n\t" \
  "v_mfma_f32_16x16x32_bf16 v[36:39], v[180:183], v[60:63], v[36:39]\n\t" \
  "s_add_u32 m0, s53, 2048\n\t" \
  "s_nop 0\n\t" \
  "buffer_load_dwordx4 v27, s[40:43], s52 offen offset:1024 sc1 lds\n\t" \
  "s_waitcnt vmcnt(19)\n\t" \
  "ds_read_b128 v[56:59], v25 offset:6144\n\t" \
  "s_waitcnt lgkmcnt(2)\n\t" \
  "v_mfma_f32_16x16x32_bf16 v[32:35], v[184:187], v[48:51], v[32:35]\n\t" \
  "v_mfma_f32_16x16x32_bf16 v[36:39], v[188:191], v[48:51], v[36:39]\n\t" \
  "s_add_u32 m0, s53, 2048\n\t" \
  "s_nop 0\n\t" \
  "buffer_load_dwordx4 v27, s[40:43], s52 offen offset:2048 sc1 lds\n\t" \
  "s_waitcnt vmcnt(19)\n\t" \
  "ds_read_b128 v[60:63], v25 offset:7168\n\t" \
  "s_waitcnt lgkmcnt(2)\n\t" \
  "v_mfma_f32_16x16x32_bf16 v[32:35], v[192:195], v[52:55], v[32:35]\n\t" \
  "v_mfma_f32_16x16x32_bf16 v[36:39], v[196:199], v[52:55], v[36:39]\n\t" \
  "s_add_u32 m0, s53, 2048\n\t" \
  "s_nop 0\n\t" \
  "buffer_load_dwordx4 v27, s[40:43], s52 offen offset:3072 sc1 lds\n\t" \
  "s_waitcnt vmcnt(19)\n\t" \
  "ds_read_b128 v[48:51], v25 offset:8192\n\t" \
  "s_waitcnt lgkmcnt(2)\n\t" \
  "v_mfma_f32_16x16x32_bf16 v[32:35], v[200:203], v[56:59], v[32:35]\n\t" \
  "v_mfma_f32_16x16x32_bf16 v[36:39], v[204:207], v[56:59], v[36:39]\n\t" \
  "s_add_u32 s52, s56, 16384\n\t" \
  "s_and_b32 s52, s52, 0x7fff\n\t" \
  "s_add_u32 s52, s52, s44\n\t" \
  "s_add_u32 m0, s53, 6144\n\t" \
  "s_nop 0\n\t" \
  "buffer_load_dwordx4 v27, s[40:43], s52 offen offset:0 sc1 lds\n\t" \
  "s_waitcnt vmcnt(19)\n\t" \
  "ds_read_b128 v[52:55], v25 offset:9216\n\t" \
  "s_waitcnt lgkmcnt(2)\n\t" \
  "v_mfma_f32_16x16x32_bf16 v[32:35], v[208:211], v[60:63], v[32:35]\n\t" \
  "v_mfma_f32_16x16x32_bf16 v[36:39], v[212:215], v[60:63], v[36:39]\n\t" \
  "s_add_u32 m0, s53, 6144\n\t" \
  "s_nop 0\n\t" \
  "buffer_load_dwordx4 v27, s[40:43], s52 offen offset:1024 sc1 lds\n\t" \
  "s_waitcnt vmcnt(19)\n\t" \
  "ds_read_b128 v[56:59], v25 offset:10240\n\t" \
  "s_waitcnt lgkmcnt(2)\n\t" \
  "v_mfma_f32_16x16x32_bf16 v[32:35], v[216:219], v[48:51], v[32:35]\n\t" \
  "v_mfma_f32_16x16x32_bf16 v[36:39], v[220:223], v[48:51], v[36:39]\n\t" \
  "s_add_u32 m0, s53, 6144\n\t" \
  "s_nop 0\n\t" \
  "buffer_load_dwordx4 v27, s[40:43], s52 offen offset:2048 sc1 lds\n\t" \
  "s_waitcnt vmcnt(19)\n\t" \
  "ds_read_b128 v[60:63], v25 offset:11264\n\t" \
  "s_waitcnt lgkmcnt(2)\n\t" \
  "v_mfma_f32_16x16x32_bf16 v[32:35], v[224:227], v[52:55], v[32:35]\n\t" \
  "v_mfma_f32_16x16x32_bf16 v[36:39], v[228:231], v[52:55], v[36:39]\n\t" \
  "s_add_u32 m0, s53, 6144\n\t" \
  "s_nop 0\n\t" \
  "buffer_load_dwordx4 v27, s[40:43], s52 offen offset:3072 sc1 lds\n\t" \
  "s_waitcnt vmcnt(19)\n\t" \
  "ds_read_b128 v[48:51], v25 offset:12288\n\t" \
  "s_waitcnt lgkmcnt(2)\n\t" \
  "v_mfma_f32_16x16x32_bf16 v[32:35], v[232:235], v[56:59], v[32:35]\n\t" \
  "v_mfma_f32_16x16x32_bf16 v[36:39], v[236:239], v[56:59], v[36:39]\n\t" \
  "s_add_u32 s52, s56, 20480\n\t" \
  "s_and_b32 s52, s52, 0x7fff\n\t" \
  "s_add_u32 s52, s52, s44\n\t" \
  "s_add_u32 m0, s53, 10240\n\t" \
  "s_nop 0\n\t" \
  "buffer_load_dwordx4 v27, s[40:43], s52 offen offset:0 sc1 lds\n\t" \
  "s_waitcnt vmcnt(19)\n\t" \
  "ds_read_b128 v[52:55], v25 offset:13312\n\t" \
  "s_waitcnt lgkmcnt(2)\n\t" \
  "v_mfma_f32_16x16x32_bf16 v[32:35], v[240:243], v[60:63], v[32:35]\n\t" \
  "v_mfma_f32_16x16x32_bf16 v[36:39], v[244:247], v[60:63], v[36:39]\n\t" \
  "s_add_u32 m0, s53, 10240\n\t" \
  "s_nop 0\n\t" \
  "buffer_load_dwordx4 v27, s[40:43], s52 offen offset:1024 sc1 lds\n\t" \
  "s_waitcnt vmcnt(19)\n\t" \
  "ds_read_b128 v[56:59], v25 offset:14336\n\t" \
  "s_waitcnt lgkmcnt(2)\n\t" \
  "v_mfma_f32_16x16x32_bf16 v[40:43], a[0:3], v[48:51], 0\n\t" \
  "v_mfma_f32_16x16x32_bf16 v[44:47], a[4:7], v[48:51], 0\n\t" \
  "s_add_u32 m0, s53, 10240\n\t" \
  "s_nop 0\n\t" \
  "buffer_load_dwordx4 v27, s[40:43], s52 offen offset:2048 sc1 lds\n\t" \
  "s_waitcnt vmcnt(19)\n\t" \
  "ds_read_b128 v[60:63], v25 offset:15360\n\t" \
  "s_waitcnt lgkmcnt(2)\n\t" \
  "v_mfma_f32_16x16x32_bf16 v[40:43], a[8:11], v[52:55], v[40:43]\n\t" \
  "v_mfma_f32_16x16x32_bf16 v[44:47], a[12:15], v[52:55], v[44:47]\n\t" \
  "s_add_u32 m0, s53, 10240\n\t" \
  "s_nop 0\n\t" \
  "buffer_load_dwordx4 v27, s[40:43], s52 offen offset:3072 sc1 lds\n\t" \
  "s_waitcnt vmcnt(19)\n\t" \
  "ds_read_b128 v[48:51], v25 offset:16384\n\t" \
  "s_waitcnt lgkmcnt(2)\n\t" \
  "v_mfma_f32_16x16x32_bf16 v[40:43], a[16:19], v[56:59], v[40:43]\n\t" \
  "v_mfma_f32_16x16x32_bf16 v[44:47], a[20:23], v[56:59], v[44:47]\n\t" \
  "s_add_u32 s52, s56, 24576\n\t" \
  "s_and_b32 s52, s52, 0x7fff\n\t" \
  "s_add_u32 s52, s52, s44\n\t" \
  "s_add_u32 m0, s53, 14336\n\t" \
  "s_nop 0\n\t" \
  "buffer_load_dwordx4 v27, s[40:43], s52 offen offset:0 sc1 lds\n\t" \
  "s_waitcnt vmcnt(19)\n\t" \
  "ds_read_b128 v[52:55], v25 offset:17408\n\t" \
  "s_waitcnt lgkmcnt(2)\n\t" \
  "v_mfma_f32_16x16x32_bf16 v[40:43], a[24:27], v[60:63], v[40:43]\n\t" \
  "v_mfma_f32_16x16x32_bf16 v[44:47], a[28:31], v[60:63], v[44:47]\n\t" \
  "s_add_u32 m0, s53, 14336\n\t" \
  "s_nop 0\n\t" \
  "buffer_load_dwordx4 v27, s[40:43], s52 offen offset:1024 sc1 lds\n\t" \
  "s_waitcnt vmcnt(19)\n\t" \
  "ds_read_b128 v[56:59], v25 offset:18432\n\t" \
  "s_waitcnt lgkmcnt(2)\n\t" \
  "v_mfma_f32_16x16x32_bf16 v[40:43], a[32:35], v[48:51], v[40:43]\n\t" \
  "v_mfma_f32_16x16x32_bf16 v[44:47], a[36:39], v[48:51], v[44:47]\n\t" \
  "s_add_u32 m0, s53, 14336\n\t" \
  "s_nop 0\n\t" \
  "buffer_load_dwordx4 v27, s[40:43], s52 offen offset:2048 sc1 lds\n\t" \
  "s_waitcnt vmcnt(19)\n\t" \
  "ds_read_b128 v[60:63], v25 offset:19456\n\t" \
  "s_waitcnt lgkmcnt(2)\n\t" \
  "v_mfma_f32_16x16x32_bf16 v[40:43], a[40:43], v[52:55], v[40:43]\n\t" \
  "v_mfma_f32_16x16x32_bf16 v[44:47], a[44:47], v[52:55], v[44:47]\n\t" \
  "s_add_u32 m0, s53, 14336\n\t" \
  "s_nop 0\n\t" \
  "buffer_load_dwordx4 v27, s[40:43], s52 offen offset:3072 sc1 lds\n\t" \
  "s_waitcnt vmcnt(19)\n\t" \
  "ds_read_b128 v[48:51], v25 offset:20480\n\t" \
  "s_waitcnt lgkmcnt(2)\n\t" \
  "v_mfma_f32_16x16x32_bf16 v[40:43], a[48:51], v[56:59], v[40:43]\n\t" \
  "v_mfma_f32_16x16x32_bf16 v[44:47], a[52:55], v[56:59], v[44:47]\n\t" \
  "s_add_u32 s52, s56, 28672\n\t" \
  "s_and_b32 s52, s52, 0x7fff\n\t" \
  "s_add_u32 s52, s52, s44\n\t" \
  "s_add_u32 m0, s53, 18432\n\t" \
  "s_nop 0\n\t" \
  "buffer_load_dwordx4 v27, s[40:43], s52 offen offset:0 sc1 lds\n\t" \
  "s_waitcnt vmcnt(19)\n\t" \
  "ds_read_b128 v[52:55], v25 offset:21504\n\t" \
  "s_waitcnt lgkmcnt(2)\n\t" \
  "v_mfma_f32_16x16x32_bf16 v[40:43], a[56:59], v[60:63], v[40:43]\n\t" \
  "v_mfma_f32_16x16x32_bf16 v[44:47], a[60:63], v[60:63], v[44:47]\n\t" \
  "s_add_u32 m0, s53, 18432\n\t" \
  "s_nop 0\n\t" \
  "buffer_load_dwordx4 v27, s[40:43], s52 offen offset:1024 sc1 lds\n\t" \
  "s_waitcnt vmcnt(19)\n\t" \
  "ds_read_b128 v[56:59], v25 offset:0\n\t" \
  "s_waitcnt lgkmcnt(2)\n\t" \
  "v_mfma_f32_16x16x32_bf16 v[40:43], a[64:67], v[48:51], v[40:43]\n\t" \
  "v_mfma_f32_16x16x32_bf16 v[44:47], a[68:71], v[48:51], v[44:47]\n\t" \
  "s_add_u32 m0, s53, 18432\n\t" \
  "s_nop 0\n\t" \
  "buffer_load_dwordx4 v27, s[40:43], s52 offen offset:2048 sc1 lds\n\t" \
  "s_waitcnt vmcnt(19)\n\t" \
  "ds_read_b128 v[60:63], v25 offset:1024\n\t" \
  "s_waitcnt lgkmcnt(2)\n\t" \
  "v_mfma_f32_16x16x32_bf16 v[40:43], a[72:75], v[52:55], v[40:43]\n\t" \
  "v_mfma_f32_16x16x32_bf16 v[44:47], a[76:79], v[52:55], v[44:47]\n\t" \
  "s_add_u32 m0, s53, 18432\n\t" \
  "s_nop 0\n\t" \
  "buffer_load_dwordx4 v27, s[40:43], s52 offen offset:3072 sc1 lds\n\t" \
  "s_bitcmp1_b32 s48, 1\n\t" \
  "s_cbranch_scc0 L_last_%=\n\t" \
  "s_waitcnt vmcnt(19)\n\t" \
  "ds_read_b128 v[48:51], v25 offset:2048\n\t" \
  "s_waitcnt lgkmcnt(2)\n\t" \
  "v_mfma_f32_16x16x32_bf16 v[40:43], a[80:83], v[56:59], v[40:43]\n\t" \
  "v_mfma_f32_16x16x32_bf16 v[44:47], a[84:87], v[56:59], v[44:47]\n\t" \
  "s_add_u32 m0, s53, 0\n\t" \
  "s_nop 0\n\t" \
  "buffer_load_dwordx4 v26, s[8:11], s51 offen lds\n\t" \
  "s_waitcnt vmcnt(19)\n\t" \
  "ds_read_b128 v[52:55], v25 offset:3072\n\t" \
  "s_waitcnt lgkmcnt(2)\n\t" \
  "v_mfma_f32_16x16x32_bf16 v[40:43], a[88:91], v[60:63], v[40:43]\n\t" \
  "v_mfma_f32_16x16x32_bf16 v[44:47], a[92:95], v[60:63], v[44:47]\n\t" \
  "s_add_u32 m0, s53, 1024\n\t" \
  "s_nop 0\n\t" \
  "buffer_load_dwordx4 v31, s[40:43], s50 offen offset:0 sc1 lds\n\t" \
  "s_waitcnt vmcnt(19)\n\t" \
  "ds_read_b128 v[56:59], v25 offset:4096\n\t" \
  "s_waitcnt lgkmcnt(2)\n\t" \
  "v_mfma_f32_16x16x32_bf16 v[40:43], a[96:99], v[48:51], v[40:43]\n\t" \
  "v_mfma_f32_16x16x32_bf16 v[44:47], a[100:103], v[48:51], v[44:47]\n\t" \
  "s_add_u32 m0, s53, 1024\n\t" \
  "s_nop 0\n\t" \
  "buffer_load_dwordx4 v31, s[40:43], s50 offen offset:1024 sc1 lds\n\t" \
  "s_waitcnt vmcnt(19)\n\t" \
  "ds_read_b128 v[60:63], v25 offset:5120\n\t" \
  "s_waitcnt lgkmcnt(2)\n\t" \
  "v_mfma_f32_16x16x32_bf16 v[40:43], a[104:107], v[52:55], v[40:43]\n\t" \
  "v_mfma_f32_16x16x32_bf16 v[44:47], a[108:111], v[52:55], v[44:47]\n\t" \
  "s_add_u32 m0, s53, 1024\n\t" \
  "s_nop 0\n\t" \
  "buffer_load_dwordx4 v31, s[40:43], s50 offen offset:2048 sc1 lds\n\t" \
  "s_waitcnt vmcnt(19)\n\t" \
  "ds_read_b128 v[48:51], v25 offset:6144\n\t" \
  "s_waitcnt lgkmcnt(2)\n\t" \
  "v_mfma_f32_16x16x32_bf16 v[40:43], a[112:115], v[56:59], v[40:43]\n\t" \
  "v_mfma_f32_16x16x32_bf16 v[44:47], a[116:119], v[56:59], v[44:47]\n\t" \
  "s_add_u32 m0, s53, 1024\n\t" \
  "s_nop 0\n\t" \
  "buffer_load_dwordx4 v31, s[40:43], s50 offen offset:3072 sc1 lds\n\t" \
  "s_waitcnt vmcnt(19)\n\t" \
  "ds_read_b128 v[52:55], v25 offset:7168\n\t" \
  "s_waitcnt lgkmcnt(2)\n\t" \
  "v_mfma_f32_16x16x32_bf16 v[40:43], a[120:123], v[60:63], v[40:43]\n\t" \
  "v_mfma_f32_16x16x32_bf16 v[44:47], a[124:127], v[60:63], v[44:47]\n\t" \
  "s_add_u32 m0, s53, 5120\n\t" \
  "s_nop 0\n\t" \
  "buffer_load_dwordx4 v30, s[40:43], s50 offen offset:0 sc1 lds\n\t" \
  "s_waitcnt vmcnt(19)\n\t" \
  "ds_read_b128 v[56:59], v25 offset:8192\n\t" \
  "s_waitcnt lgkmcnt(2)\n\t" \
  "v_mfma_f32_16x16x32_bf16 v[40:43], a[128:131], v[48:51], v[40:43]\n\t" \
  "v_mfma_f32_16x16x32_bf16 v[44:47], a[132:135], v[48:51], v[44:47]\n\t" \
  "s_add_u32 m0, s53, 5120\n\t" \
  "s_nop 0\n\t" \
  "buffer_load_dwordx4 v30, s[40:43], s50 offen offset:1024 sc1 lds\n\t" \
  "s_waitcnt vmcnt(19)\n\t" \
  "ds_read_b128 v[60:63], v25 offset:9216\n\t" \
  "s_waitcnt lgkmcnt(2)\n\t" \
  "v_mfma_f32_16x16x32_bf16 v[40:43], a[136:139], v[52:55], v[40:43]\n\t" \
  "v_mfma_f32_16x16x32_bf16 v[44:47], a[140:143], v[52:55], v[44:47]\n\t" \
  "s_add_u32 m0, s53, 5120\n\t" \
  "s_nop 0\n\t" \
  "buffer_load_dwordx4 v30, s[40:43], s50 offen offset:2048 sc1 lds\n\t" \
  "s_waitcnt vmcnt(19)\n\t" \
  "ds_read_b128 v[48:51], v25 offset:10240\n\t" \
  "s_waitcnt lgkmcnt(2)\n\t" \
  "v_mfma_f32_16x16x32_bf16 v[40:43], a[144:147], v[56:59], v[40:43]\n\t" \
  "v_mfma_f32_16x16x32_bf16 v[44:47], a[148:151], v[56:59], v[44:47]\n\t" \
  "s_add_u32 m0, s53, 5120\n\t" \
  "s_nop 0\n\t" \
  "buffer_load_dwordx4 v30, s[40:43], s50 offen offset:3072 sc1 lds\n\t" \
  "s_waitcnt vmcnt(19)\n\t" \
  "ds_read_b128 v[52:55], v25 offset:11264\n\t" \
  "s_waitcnt lgkmcnt(2)\n\t" \
  "v_mfma_f32_16x16x32_bf16 v[40:43], a[152:155], v[60:63], v[40:43]\n\t" \
  "v_mfma_f32_16x16x32_bf16 v[44:47], a[156:159], v[60:63], v[44:47]\n\t" \
  "s_add_u32 m0, s53, 9216\n\t" \
  "s_nop 0\n\t" \
  "buffer_load_dwordx4 v29, s[40:43], s50 offen offset:0 sc1 lds\n\t" \
  "s_waitcnt vmcnt(19)\n\t" \
  "ds_read_b128 v[56:59], v25 offset:12288\n\t" \
  "s_waitcnt lgkmcnt(2)\n\t" \
  "v_mfma_f32_16x16x32_bf16 v[40:43], a[160:163], v[48:51], v[40:43]\n\t" \
  "v_mfma_f32_16x16x32_bf16 v[44:47], a[164:167], v[48:51], v[44:47]\n\t" \
  "s_add_u32 m0, s53, 9216\n\t" \
  "s_nop 0\n\t" \
  "buffer_load_dwordx4 v29, s[40:43], s50 offen offset:1024 sc1 lds\n\t" \
  "s_waitcnt vmcnt(19)\n\t" \
  "ds_read_b128 v[60:63], v25 offset:13312\n\t" \
  "s_waitcnt lgkmcnt(2)\n\t" \
  "v_mfma_f32_16x16x32_bf16 v[40:43], a[168:171], v[52:55], v[40:43]\n\t" \
  "v_mfma_f32_16x16x32_bf16 v[44:47], a[172:175], v[52:55], v[44:47]\n\t" \
  "s_add_u32 m0, s53, 9216\n\t" \
  "s_nop 0\n\t" \
  "buffer_load_dwordx4 v28, s[40:43], s50 offen offset:2048 sc1 lds\n\t" \
  "s_waitcnt vmcnt(19)\n\t" \
  "ds_read_b128 v[48:51], v25 offset:14336\n\t" \
  "s_waitcnt lgkmcnt(2)\n\t" \
  "v_mfma_f32_16x16x32_bf16 v[40:43], a[176:179], v[56:59], v[40:43]\n\t" \
  "v_mfma_f32_16x16x32_bf16 v[44:47], a[180:183], v[56:59], v[44:47]\n\t" \
  "s_add_u32 s52, s56, 0\n\t" \
  "s_and_b32 s52, s52, 0x7fff\n\t" \
  "s_add_u32 s52, s52, s49\n\t" \
  "s_add_u32 m0, s53, 12288\n\t" \
  "s_nop 0\n\t" \
  "buffer_load_dwordx4 v27, s[40:43], s52 offen offset:0 sc1 lds\n\t" \
  "s_waitcnt vmcnt(19)\n\t" \
  "ds_read_b128 v[52:55], v25 offset:15360\n\t" \
  "s_waitcnt lgkmcnt(2)\n\t" \
  "v_mfma_f32_16x16x32_bf16 v[40:43], a[184:187], v[60:63], v[40:43]\n\t" \
  "v_mfma_f32_16x16x32_bf16 v[44:47], a[188:191], v[60:63], v[44:47]\n\t" \
  "s_add_u32 m0, s53, 12288\n\t" \
  "s_nop 0\n\t" \
  "buffer_load_dwordx4 v27, s[40:43], s52 offen offset:1024 sc1 lds\n\t" \
  "s_waitcnt vmcnt(19)\n\t" \
  "ds_read_b128 v[56:59], v25 offset:16384\n\t" \
  "s_waitcnt lgkmcnt(2)\n\t" \
  "v_mfma_f32_16x16x32_bf16 v[40:43], a[192:195], v[48:51], v[40:43]\n\t" \
  "v_mfma_f32_16x16x32_bf16 v[44:47], a[196:199], v[48:51], v[44:47]\n\t" \
  "s_add_u32 m0, s53, 12288\n\t" \
  "s_nop 0\n\t" \
  "buffer_load_dwordx4 v27, s[40:43], s52 offen offset:2048 sc1 lds\n\t" \
  "s_waitcnt vmcnt(19)\n\t" \
  "ds_read_b128 v[60:63], v25 offset:17408\n\t" \
  "s_waitcnt lgkmcnt(2)\n\t" \
  "v_mfma_f32_16x16x32_bf16 v[40:43], a[200:203], v[52:55], v[40:43]\n\t" \
  "v_mfma_f32_16x16x32_bf16 v[44:47], a[204:207], v[52:55], v[44:47]\n\t" \
  "s_add_u32 m0, s53, 12288\n\t" \
  "s_nop 0\n\t" \
  "buffer_load_dwordx4 v27, s[40:43], s52 offen offset:3072 sc1 lds\n\t" \
  "s_waitcnt vmcnt(19)\n\t" \
  "ds_read_b128 v[48:51], v25 offset:18432\n\t" \
  "s_waitcnt lgkmcnt(2)\n\t" \
  "v_mfma_f32_16x16x32_bf16 v[40:43], a[208:211], v[56:59], v[40:43]\n\t" \
  "v_mfma_f32_16x16x32_bf16 v[44:47], a[212:215], v[56:59], v[44:47]\n\t" \
  "s_add_u32 s52, s56, 4096\n\t" \
  "s_and_b32 s52, s52, 0x7fff\n\t" \
  "s_add_u32 s52, s52, s49\n\t" \
  "s_add_u32 m0, s53, 16384\n\t" \
  "s_nop 0\n\t" \
  "buffer_load_dwordx4 v27, s[40:43], s52 offen offset:0 sc1 lds\n\t" \
  "s_waitcnt vmcnt(19)\n\t" \
  "ds_read_b128 v[52:55], v25 offset:19456\n\t" \
  "s_waitcnt lgkmcnt(2)\n\t" \
  "v_mfma_f32_16x16x32_bf16 v[40:43], a[216:219], v[60:63], v[40:43]\n\t" \
  "v_mfma_f32_16x16x32_bf16 v[44:47], a[220:223], v[60:63], v[44:47]\n\t" \
  "s_add_u32 m0, s53, 16384\n\t" \
  "s_nop 0\n\t" \
  "buffer_load_dwordx4 v27, s[40:43], s52 offen offset:1024 sc1 lds\n\t" \
  "s_waitcnt vmcnt(19)\n\t" \
  "ds_read_b128 v[56:59], v25 offset:20480\n\t" \
  "s_waitcnt lgkmcnt(2)\n\t" \
  "v_mfma_f32_16x16x32_bf16 v[40:43], a[224:227], v[48:51], v[40:43]\n\t" \
  "v_mfma_f32_16x16x32_bf16 v[44:47], a[228:231], v[48:51], v[44:47]\n\t" \
  "s_add_u32 m0, s53, 16384\n\t" \
  "s_nop 0\n\t" \
  "buffer_load_dwordx4 v27, s[40:43], s52 offen offset:2048 sc1 lds\n\t" \
  "s_waitcnt vmcnt(19)\n\t" \
  "ds_read_b128 v[60:63], v25 offset:21504\n\t" \
  "s_waitcnt lgkmcnt(2)\n\t" \
  "v_mfma_f32_16x16x32_bf16 v[40:43], a[232:235], v[52:55], v[40:43]\n\t" \
  "v_mfma_f32_16x16x32_bf16 v[44:47], a[236:239], v[52:55], v[44:47]\n\t" \
  "s_add_u32 m0, s53, 16384\n\t" \
  "s_nop 0\n\t" \
  "buffer_load_dwordx4 v27, s[40:43], s52 offen offset:3072 sc1 lds\n\t" \
  "s_waitcnt lgkmcnt(1)\n\t" \
  "v_mfma_f32_16x16x32_bf16 v[40:43], a[240:243], v[56:59], v[40:43]\n\t" \
  "v_mfma_f32_16x16x32_bf16 v[44:47], a[244:247], v[56:59], v[44:47]\n\t" \
  "s_add_u32 s52, s56, 8192\n\t" \
  "s_and_b32 s52, s52, 0x7fff\n\t" \
  "s_add_u32 s52, s52, s49\n\t" \
  "s_add_u32 m0, s53, 20480\n\t" \
  "s_nop 0\n\t" \
  "buffer_load_dwordx4 v27, s[40:43], s52 offen offset:0 sc1 lds\n\t" \
  "s_waitcnt lgkmcnt(0)\n\t" \
  "v_mfma_f32_16x16x32_bf16 v[40:43], a[248:251], v[60:63], v[40:43]\n\t" \
  "v_mfma_f32_16x16x32_bf16 v[44:47], a[252:255], v[60:63], v[44:47]\n\t" \
  "s_add_u32 m0, s53, 20480\n\t" \
  "s_nop 0\n\t" \
  "buffer_load_dwordx4 v27, s[40:43], s52 offen offset:1024 sc1 lds\n\t" \
  "s_branch L_done_%=\n\t" \
  "L_last_%=:\n\t" \
  "s_waitcnt vmcnt(19)\n\t" \
  "ds_read_b128 v[48:51], v25 offset:2048\n\t" \
  "s_waitcnt lgkmcnt(2)\n\t" \
  "v_mfma_f32_16x16x32_bf16 v[40:43], a[80:83], v[56:59], v[40:43]\n\t" \
  "v_mfma_f32_16x16x32_bf16 v[44:47], a[84:87], v[56:59], v[44:47]\n\t" \
  "s_waitcnt vmcnt(18)\n\t" \
  "ds_read_b128 v[52:55], v25 offset:3072\n\t" \
  "s_waitcnt lgkmcnt(2)\n\t" \
  "v_mfma_f32_16x16x32_bf16 v[40:43], a[88:91], v[60:63], v[40:43]\n\t" \
  "v_mfma_f32_16x16x32_bf16 v[44:47], a[92:95], v[60:63], v[44:47]\n\t" \
  "s_waitcnt vmcnt(17)\n\t" \
  "ds_read_b128 v[56:59], v25 offset:4096\n\t" \
  "s_waitcnt lgkmcnt(2)\n\t" \
  "v_mfma_f32_16x16x32_bf16 v[40:43], a[96:99], v[48:51], v[40:43]\n\t" \
  "v_mfma_f32_16x16x32_bf16 v[44:47], a[100:103], v[48:51], v[44:47]\n\t" \
  "s_waitcnt vmcnt(16)\n\t" \
  "ds_read_b128 v[60:63], v25 offset:5120\n\t" \
  "s_waitcnt lgkmcnt(2)\n\t" \
  "v_mfma_f32_16x16x32_bf16 v[40:43], a[104:107], v[52:55], v[40:43]\n\t" \
  "v_mfma_f32_16x16x32_bf16 v[44:47], a[108:111], v[52:55], v[44:47]\n\t" \
  "s_waitcnt vmcnt(15)\n\t" \
  "ds_read_b128 v[48:51], v25 offset:6144\n\t" \
  "s_waitcnt lgkmcnt(2)\n\t" \
  "v_mfma_f32_16x16x32_bf16 v[40:43], a[112:115], v[56:59], v[40:43]\n\t" \
  "v_mfma_f32_16x16x32_bf16 v[44:47], a[116:119], v[56:59], v[44:47]\n\t" \
  "s_waitcnt vmcnt(14)\n\t" \
  "ds_read_b128 v[52:55], v25 offset:7168\n\t" \
  "s_waitcnt lgkmcnt(2)\n\t" \
  "v_mfma_f32_16x16x32_bf16 v[40:43], a[120:123], v[60:63], v[40:43]\n\t" \
  "v_mfma_f32_16x16x32_bf16 v[44:47], a[124:127], v[60:63], v[44:47]\n\t" \
  "s_waitcnt vmcnt(13)\n\t" \
  "ds_read_b128 v[56:59], v25 offset:8192\n\t" \
  "s_waitcnt lgkmcnt(2)\n\t" \
  "v_mfma_f32_16x16x32_bf16 v[40:43], a[128:131], v[48:51], v[40:43]\n\t" \
  "v_mfma_f32_16x16x32_bf16 v[44:47], a[132:135], v[48:51], v[44:47]\n\t" \
  "s_waitcnt vmcnt(12)\n\t" \
  "ds_read_b128 v[60:63], v25 offset:9216\n\t" \
  "s_waitcnt lgkmcnt(2)\n\t" \
  "v_mfma_f32_16x16x32_bf16 v[40:43], a[136:139], v[52:55], v[40:43]\n\t" \
  "v_mfma_f32_16x16x32_bf16 v[44:47], a[140:143], v[52:55], v[44:47]\n\t" \
  "s_waitcnt vmcnt(11)\n\t" \
  "ds_read_b128 v[48:51], v25 offset:10240\n\t" \
  "s_waitcnt lgkmcnt(2)\n\t" \
  "v_mfma_f32_16x16x32_bf16 v[40:43], a[144:147], v[56:59], v[40:43]\n\t" \
  "v_mfma_f32_16x16x32_bf16 v[44:47], a[148:151], v[56:59], v[44:47]\n\t" \
  "s_waitcnt vmcnt(10)\n\t" \
  "ds_read_b128 v[52:55], v25 offset:11264\n\t" \
  "s_waitcnt lgkmcnt(2)\n\t" \
  "v_mfma_f32_16x16x32_bf16 v[40:43], a[152:155], v[60:63], v[40:43]\n\t" \
  "v_mfma_f32_16x16x32_bf16 v[44:47], a[156:159], v[60:63], v[44:47]\n\t" \
  "s_waitcnt vmcnt(9)\n\t" \
  "ds_read_b128 v[56:59], v25 offset:12288\n\t" \
  "s_waitcnt lgkmcnt(2)\n\t" \
  "v_mfma_f32_16x16x32_bf16 v[40:43], a[160:163], v[48:51], v[40:43]\n\t" \
  "v_mfma_f32_16x16x32_bf16 v[44:47], a[164:167], v[48:51], v[44:47]\n\t" \
  "s_waitcnt vmcnt(8)\n\t" \
  "ds_read_b128 v[60:63], v25 offset:13312\n\t" \
  "s_waitcnt lgkmcnt(2)\n\t" \
  "v_mfma_f32_16x16x32_bf16 v[40:43], a[168:171], v[52:55], v[40:43]\n\t" \
  "v_mfma_f32_16x16x32_bf16 v[44:47], a[172:175], v[52:55], v[44:47]\n\t" \
  "s_waitcnt vmcnt(7)\n\t" \
  "ds_read_b128 v[48:51], v25 offset:14336\n\t" \
  "s_waitcnt lgkmcnt(2)\n\t" \
  "v_mfma_f32_16x16x32_bf16 v[40:43], a[176:179], v[56:59], v[40:43]\n\t" \
  "v_mfma_f32_16x16x32_bf16 v[44:47], a[180:183], v[56:59], v[44:47]\n\t" \
  "s_waitcnt vmcnt(6)\n\t" \
  "ds_read_b128 v[52:55], v25 offset:15360\n\t" \
  "s_waitcnt lgkmcnt(2)\n\t" \
  "v_mfma_f32_16x16x32_bf16 v[40:43], a[184:187], v[60:63], v[40:43]\n\t" \
  "v_mfma_f32_16x16x32_bf16 v[44:47], a[188:191], v[60:63], v[44:47]\n\t" \
  "s_waitcnt vmcnt(5)\n\t" \
  "ds_read_b128 v[56:59], v25 offset:16384\n\t" \
  "s_waitcnt lgkmcnt(2)\n\t" \
  "v_mfma_f32_16x16x32_bf16 v[40:43], a[192:195], v[48:51], v[40:43]\n\t" \
  "v_mfma_f32_16x16x32_bf16 v[44:47], a[196:199], v[48:51], v[44:47]\n\t" \
  "s_waitcnt vmcnt(4)\n\t" \
  "ds_read_b128 v[60:63], v25 offset:17408\n\t" \
  "s_waitcnt lgkmcnt(2)\n\t" \
  "v_mfma_f32_16x16x32_bf16 v[40:43], a[200:203], v[52:55], v[40:43]\n\t" \
  "v_mfma_f32_16x16x32_bf16 v[44:47], a[204:207], v[52:55], v[44:47]\n\t" \
  "s_waitcnt vmcnt(3)\n\t" \
  "ds_read_b128 v[48:51], v25 offset:18432\n\t" \
  "s_waitcnt lgkmcnt(2)\n\t" \
  "v_mfma_f32_16x16x32_bf16 v[40:43], a[208:211], v[56:59], v[40:43]\n\t" \
  "v_mfma_f32_16x16x32_bf16 v[44:47], a[212:215], v[56:59], v[44:47]\n\t" \
  "s_waitcnt vmcnt(2)\n\t" \
  "ds_read_b128 v[52:55], v25 offset:19456\n\t" \
  "s_waitcnt lgkmcnt(2)\n\t" \
  "v_mfma_f32_16x16x32_bf16 v[40:43], a[216:219], v[60:63], v[40:43]\n\t" \
  "v_mfma_f32_16x16x32_bf16 v[44:47], a[220:223], v[60:63], v[44:47]\n\t" \
  "s_waitcnt vmcnt(1)\n\t" \
  "ds_read_b128 v[56:59], v25 offset:20480\n\t" \
  "s_waitcnt lgkmcnt(2)\n\t" \
  "v_mfma_f32_16x16x32_bf16 v[40:43], a[224:227], v[48:51], v[40:43]\n\t" \
  "v_mfma_f32_16x16x32_bf16 v[44:47], a[228:231], v[48:51], v[44:47]\n\t" \
  "s_waitcnt vmcnt(0)\n\t" \
  "ds_read_b128 v[60:63], v25 offset:21504\n\t" \
  "s_waitcnt lgkmcnt(2)\n\t" \
  "v_mfma_f32_16x16x32_bf16 v[40:43], a[232:235], v[52:55], v[40:43]\n\t" \
  "v_mfma_f32_16x16x32_bf16 v[44:47], a[236:239], v[52:55], v[44:47]\n\t" \
  "s_waitcnt lgkmcnt(1)\n\t" \
  "v_mfma_f32_16x16x32_bf16 v[40:43], a[240:243], v[56:59], v[40:43]\n\t" \
  "v_mfma_f32_16x16x32_bf16 v[44:47], a[244:247], v[56:59], v[44:47]\n\t" \
  "s_waitcnt lgkmcnt(0)\n\t" \
  "v_mfma_f32_16x16x32_bf16 v[40:43], a[248:251], v[60:63], v[40:43]\n\t" \
  "v_mfma_f32_16x16x32_bf16 v[44:47], a[252:255], v[60:63], v[44:47]\n\t" \
  "L_done_%=:\n\t" \
  "s_mov_b32 m0, s54\n\t" \
  "s_nop 6\n\t"
#define CHAIN_ASM \
  "s_mov_b32 s68, 0x00ff00ff\n\t" \
  "s_mov_b32 s69, 0x00ff00ff\n\t" \
  "s_mov_b32 s72, 0x3f803f80\n\t" \
  "s_mov_b32 s73, 0x3f803f80\n\t" \
  "s_lshl_b32 s7, s28, 4\n\t" \
  "s_or_b32 s7, s7, s49\n\t" \
  "s_cmp_eq_u32 s7, 2\n\t" \
  "s_cselect_b32 s7, 8, 0\n\t" \
  "s_add_u32 s0, s29, 8\n\t" \
  "s_mov_b32 s3, 0\n\t" \
  "s_mov_b32 s4, 0\n\t" \
  "s_mov_b32 s25, 0\n\t" \
  "v_mov_b64 v[14:15], 0\n\t" \
  "s_lshl_b32 s22, s50, 4\n\t" \
  "s_or_b32 s22, s22, s49\n\t" \
  "s_mov_b32 s74, 0x3f80\n\t" \
  "s_mov_b32 s75, 0x3f80\n\t" \
  "s_and_b32 s96, s57, 0xff\n\t" \
  "s_bfe_u32 s97, s57, 0x80008\n\t" \
  "s_mul_i32 s98, s67, 6\n\t" \
  "s_and_b32 s94, s56, 0xffff\n\t" \
  "s_lshr_b32 s95, s57, 16\n\t" \
  "s_bfe_u32 s38, s56, 0x80010\n\t" \
  "s_lshr_b32 s39, s56, 24\n\t" \
  "s_sub_u32 s38, s38, 32\n\t" \
  "s_sub_u32 s39, s39, 32\n\t" \
  "s_sub_u32 s34, s94, s38\n\t" \
  "s_sub_u32 s35, s95, s39\n\t" \
  "s_lshl_b32 s66, s49, 14\n\t" \
  "s_add_u32 s66, s66, 32768\n\t" \
  "v_add_u32 v124, s66, v1\n\t" \
  "ds_read_b128 v[32:35], v124 offset:0\n\t" \
  "ds_read_b128 v[36:39], v124 offset:1024\n\t" \
  "ds_read_b128 v[40:43], v124 offset:2048\n\t" \
  "ds_read_b128 v[44:47], v124 offset:3072\n\t" \
  "ds_read_b128 v[48:51], v124 offset:4096\n\t" \
  "ds_read_b128 v[52:55], v124 offset:5120\n\t" \
  "ds_read_b128 v[80:83], v124 offset:6144\n\t" \
  "ds_read_b128 v[84:87], v124 offset:7168\n\t" \
  "ds_read_b128 v[88:91], v124 offset:8192\n\t" \
  "ds_read_b128 v[92:95], v124 offset:9216\n\t" \
  "ds_read_b128 v[96:99], v124 offset:10240\n\t" \
  "ds_read_b128 v[100:103], v124 offset:11264\n\t" \
  "ds_read_b128 v[104:107], v124 offset:12288\n\t" \
  "ds_read_b128 v[108:111], v124 offset:13312\n\t" \
  "ds_read_b128 v[112:115], v124 offset:14336\n\t" \
  "ds_read_b128 v[116:119], v124 offset:15360\n\t" \
  "s_branch L_first_%=\n\t" \
  "L_red_%=:\n\t" \
  "s_waitcnt vmcnt(3)\n\t" \
  "v_readfirstlane_b32 s26, v14\n\t" \
  "v_readfirstlane_b32 s27, v15\n\t" \
  "s_cmp_eq_u32 s44, 0\n\t" \
  "s_cbranch_scc1 L_sync_%=\n\t" \
  "L_red2_%=:\n\t" \
  "s_memrealtime s[8:9]\n\t" \
  "s_mov_b32 s36, s98\n\t" \
  "s_add_u32 s98, s98, s94\n\t" \
  "s_and_b32 s66, s27, 0xff\n\t" \
  "s_memrealtime s[10:11]\n\t" \
  "s_bfe_u32 s67, s27, 0xc0008\n\t" \
  "s_lshr_b32 s27, s27, 20\n\t" \
  "s_cmp_eq_u32 s66, s44\n\t" \
  "s_memrealtime s[12:13]\n\t" \
  "s_cselect_b32 s98, s26, s98\n\t" \
  "s_cselect_b32 s95, s67, s95\n\t" \
  "s_cselect_b32 s94, s27, s94\n\t" \
  "s_memrealtime s[14:15]\n\t" \
  "s_mov_b32 s20, 1\n\t" \
  "s_xor_b32 s70, s70, 114688\n\t" \
  "s_add_u32 s51, s51, 524288\n\t" \
  "s_memrealtime s[16:17]\n\t" \
  "s_add_u32 s52, s52, 131072\n\t" \
  "s_mov_b32 s63, s53\n\t" \
  "s_mov_b32 s64, s54\n\t" \
  "s_memrealtime s[18:19]\n\t" \
  "L_w1_%=:\n\t" \
  "s_waitcnt lgkmcnt(0)\n\t" \
  "s_add_u32 s8, s8, s10\n\t" \
  "s_add_u32 s12, s12, s14\n\t" \
  "s_add_u32 s16, s16, s18\n\t" \
  "s_sub_u32 s66, s98, s8\n\t" \
  "s_sub_u32 s66, s66, s12\n\t" \
  "s_sub_u32 s66, s66, s16\n\t" \
  "s_sub_u32 s5, s98, s66\n\t" \
  "s_add_u32 s67, s66, s96\n\t" \
  "s_cmp_gt_u32 s67, 254\n\t" \
  "s_cbranch_scc1 L_late1_%=\n\t" \
  "L_ontime1_%=:\n\t" \
  "s_max_i32 s67, s66, 0\n\t" \
  "s_lshl_b32 s67, s67, 1\n\t" \
  "s_and_b32 s67, s67, -4\n\t" \
  "s_sub_u32 s66, 520, s67\n\t" \
  "s_getpc_b64 s[78:79]\n\t" \
  "s_add_u32 s78, s78, s66\n\t" \
  "s_addc_u32 s79, s79, 0\n\t" \
  "s_setpc_b64 s[78:79]\n\t" \
  "s_nop 1\n\t" \
  "s_nop 1\n\t" \
  "s_nop 1\n\t" \
  "s_nop 1\n\t" \
  "s_nop 1\n\t" \
  "s_nop 1\n\t" \
  "s_nop 1\n\t" \
  "s_nop 1\n\t" \
  "s_nop 1\n\t" \
  "s_nop 1\n\t" \
  "s_nop 1\n\t" \
  "s_nop 1\n\t" \
  "s_nop 1\n\t" \
  "s_nop 1\n\t" \
  "s_nop 1\n\t" \
  "s_nop 1\n\t" \
  "s_nop 1\n\t" \
  "s_nop 1\n\t" \
  "s_nop 1\n\t" \
  "s_nop 1\n\t" \
  "s_nop 1\n\t" \
  "s_nop 1\n\t" \
  "s_nop 1\n\t" \
  "s_nop 1\n\t" \
  "s_nop 1\n\t" \
  "s_nop 1\n\t" \
  "s_nop 1\n\t" \
  "s_nop 1\n\t" \
  "s_nop 1\n\t" \
  "s_nop 1\n\t" \
  "s_nop 1\n\t" \
  "s_nop 1\n\t" \
  "s_nop 1\n\t" \
  "s_nop 1\n\t" \
  "s_nop 1\n\t" \
  "s_nop 1\n\t" \
  "s_nop 1\n\t" \
  "s_nop 1\n\t" \
  "s_nop 1\n\t" \
  "s_nop 1\n\t" \
  "s_nop 1\n\t" \
  "s_nop 1\n\t" \
  "s_nop 1\n\t" \
  "s_nop 1\n\t" \
  "s_nop 1\n\t" \
  "s_nop 1\n\t" \
  "s_nop 1\n\t" \
  "s_nop 1\n\t" \
  "s_nop 1\n\t" \
  "s_nop 1\n\t" \
  "s_nop 1\n\t" \
  "s_nop 1\n\t" \
  "s_nop 1\n\t" \
  "s_nop 1\n\t" \
  "s_nop 1\n\t" \
  "s_nop 1\n\t" \
  "s_nop 1\n\t" \
  "s_nop 1\n\t" \
  "s_nop 1\n\t" \
  "s_nop 1\n\t" \
  "s_nop 1\n\t" \
  "s_nop 1\n\t" \
  "s_nop 1\n\t" \
  "s_nop 1\n\t" \
  "s_nop 1\n\t" \
  "s_nop 1\n\t" \
  "s_nop 1\n\t" \
  "s_nop 1\n\t" \
  "s_nop 1\n\t" \
  "s_nop 1\n\t" \
  "s_nop 1\n\t" \
  "s_nop 1\n\t" \
  "s_nop 1\n\t" \
  "s_nop 1\n\t" \
  "s_nop 1\n\t" \
  "s_nop 1\n\t" \
  "s_nop 1\n\t" \
  "s_nop 1\n\t" \
  "s_nop 1\n\t" \
  "s_nop 1\n\t" \
  "s_nop 1\n\t" \
  "s_nop 1\n\t" \
  "s_nop 1\n\t" \
  "s_nop 1\n\t" \
  "s_nop 1\n\t" \
  "s_nop 1\n\t" \
  "s_nop 1\n\t" \
  "s_nop 1\n\t" \
  "s_nop 1\n\t" \
  "s_nop 1\n\t" \
  "s_nop 1\n\t" \
  "s_nop 1\n\t" \
  "s_nop 1\n\t" \
  "s_nop 1\n\t" \
  "s_nop 1\n\t" \
  "s_nop 1\n\t" \
  "s_nop 1\n\t" \
  "s_nop 1\n\t" \
  "s_nop 1\n\t" \
  "s_nop 1\n\t" \
  "s_nop 1\n\t" \
  "s_nop 1\n\t" \
  "s_nop 1\n\t" \
  "s_nop 1\n\t" \
  "s_nop 1\n\t" \
  "s_nop 1\n\t" \
  "s_nop 1\n\t" \
  "s_nop 1\n\t" \
  "s_nop 1\n\t" \
  "s_nop 1\n\t" \
  "s_nop 1\n\t" \
  "s_nop 1\n\t" \
  "s_nop 1\n\t" \
  "s_nop 1\n\t" \
  "s_nop 1\n\t" \
  "s_nop 1\n\t" \
  "s_nop 1\n\t" \
  "s_nop 1\n\t" \
  "s_nop 1\n\t" \
  "s_nop 1\n\t" \
  "s_nop 1\n\t" \
  "s_nop 1\n\t" \
  "s_nop 1\n\t" \
  "s_nop 1\n\t" \
  "s_nop 1\n\t" \
  "s_nop 1\n\t" \
  "s_nop 1\n\t" \
  "L_poll1_%=:\n\t" \
  "buffer_load_dwordx4 v[56:59], v4, s[40:43], s45 offen offset:0 sc1\n\t" \
  "buffer_load_dwordx4 v[60:63], v4, s[40:43], s45 offen offset:1024 sc1\n\t" \
  "buffer_load_dwordx4 v[64:67], v4, s[40:43], s45 offen offset:2048 sc1\n\t" \
  "buffer_load_dwordx4 v[68:71], v4, s[40:43], s45 offen offset:3072 sc1\n\t" \
  "s_cmp_eq_u32 s20, 0\n\t" \
  "s_cbranch_scc1 L_nb_%=\n\t" \
  "s_barrier\n\t" \
  "s_mov_b32 s20, 0\n\t" \
  "s_add_u32 s55, s55, 1\n\t" \
  "s_add_u32 s53, s53, 128\n\t" \
  "s_cmp_eq_u32 s55, 3\n\t" \
  "s_cselect_b32 s55, 0, s55\n\t" \
  "s_cselect_b32 s53, s65, s53\n\t" \
  "s_addc_u32 s54, s54, 0\n\t" \
  "s_cmp_eq_u32 s44, 62\n\t" \
  "s_cselect_b32 s64, 0, s64\n\t" \
  "s_sub_u32 s66, s5, s36\n\t" \
  "s_add_u32 s67, s34, 24\n\t" \
  "s_sub_u32 s34, s34, 2\n\t" \
  "s_cmp_lt_u32 s66, s67\n\t" \
  "s_cselect_b32 s66, s66, 0\n\t" \
  "s_max_u32 s34, s34, s66\n\t" \
  "v_add_f32 v72, v72, v73\n\t" \
  "v_add_f32 v74, v74, v75\n\t" \
  "v_add_f32 v76, v76, v77\n\t" \
  "v_add_f32 v78, v78, v79\n\t" \
  "v_cvt_pk_bf16_f32 v126, v72, v74\n\t" \
  "v_cvt_pk_bf16_f32 v127, v76, v78\n\t" \
  "ds_write_b64 v12, v[126:127] offset:14336\n\t" \
  "L_nb_%=:\n\t" \
  "v_add_u32 v124, 114688, v1\n\t" \
  "ds_read_b128 v[24:27], v124\n\t" \
  "v_mov_b32 v73, 115712\n\t" \
  "ds_read_b32 v73, v73\n\t" \
  "s_lshl_b32 s66, s49, 14\n\t" \
  "s_add_u32 s66, s66, 32768\n\t" \
  "v_add_u32 v124, s66, v1\n\t" \
  "ds_read_b128 v[32:35], v124 offset:0\n\t" \
  "ds_read_b128 v[36:39], v124 offset:1024\n\t" \
  "ds_read_b128 v[40:43], v124 offset:2048\n\t" \
  "ds_read_b128 v[44:47], v124 offset:3072\n\t" \
  "ds_read_b128 v[48:51], v124 offset:4096\n\t" \
  "ds_read_b128 v[52:55], v124 offset:5120\n\t" \
  "ds_read_b128 v[80:83], v124 offset:6144\n\t" \
  "ds_read_b128 v[84:87], v124 offset:7168\n\t" \
  "ds_read_b128 v[88:91], v124 offset:8192\n\t" \
  "ds_read_b128 v[92:95], v124 offset:9216\n\t" \
  "ds_read_b128 v[96:99], v124 offset:10240\n\t" \
  "ds_read_b128 v[100:103], v124 offset:11264\n\t" \
  "ds_read_b128 v[104:107], v124 offset:12288\n\t" \
  "ds_read_b128 v[108:111], v124 offset:13312\n\t" \
  "ds_read_b128 v[112:115], v124 offset:14336\n\t" \
  "ds_read_b128 v[116:119], v124 offset:15360\n\t" \
  "s_mul_i32 s66, s25, 9\n\t" \
  "s_mul_i32 s67, s25, 6\n\t" \
  "s_cmp_lt_u32 s44, 4\n\t" \
  "s_cselect_b32 s66, 0, s66\n\t" \
  "s_cselect_b32 s67, 0, s67\n\t" \
  "s_add_u32 s38, s38, s66\n\t" \
  "s_add_u32 s39, s39, s67\n\t" \
  "s_mov_b32 s25, 0\n\t" \
  "s_lshl_b32 s66, s44, 10\n\t" \
  "s_and_b32 s67, s66, -8192\n\t" \
  "s_add_u32 s66, s66, s67\n\t" \
  "s_add_u32 s4, s66, s30\n\t" \
  "v_lshlrev_b32 v72, 3, v1\n\t" \
  "s_waitcnt lgkmcnt(15)\n\t" \
  "v_readfirstlane_b32 s66, v73\n\t" \
  "s_lshr_b32 s67, s66, 16\n\t" \
  "s_max_u32 s67, s67, s34\n\t" \
  "s_add_u32 s26, s67, s38\n\t" \
  "s_and_b32 s66, s66, 0xffff\n\t" \
  "s_max_u32 s66, s66, s35\n\t" \
  "s_add_u32 s66, s66, s39\n\t" \
  "s_lshl_b32 s66, s66, 8\n\t" \
  "s_add_u32 s66, s66, s44\n\t" \
  "s_add_u32 s66, s66, 1\n\t" \
  "s_lshl_b32 s67, s26, 20\n\t" \
  "s_or_b32 s66, s66, s67\n\t" \
  "v_mov_b32 v75, s66\n\t" \
  "s_waitcnt vmcnt(3)\n\t" \
  "v_mfma_f32_16x16x32_bf16 v[28:31], v[24:27], v[56:59], 0\n\t" \
  "s_waitcnt vmcnt(2)\n\t" \
  "v_mfma_f32_16x16x32_bf16 v[28:31], v[24:27], v[60:63], v[28:31]\n\t" \
  "s_waitcnt vmcnt(1)\n\t" \
  "v_mfma_f32_16x16x32_bf16 v[28:31], v[24:27], v[64:67], v[28:31]\n\t" \
  "s_waitcnt vmcnt(0)\n\t" \
  "v_mfma_f32_16x16x32_bf16 v[28:31], v[24:27], v[68:71], v[28:31]\n\t" \
  "s_nop 7\n\t" \
  "v_cmp_u_f32 vcc, v28, v28\n\t" \
  "v_add_f32_dpp v120, v28, v28 row_ror:8 row_mask:0xf bank_mask:0xf\n\t" \
  "v_add_f32_dpp v121, v29, v29 row_ror:8 row_mask:0xf bank_mask:0xf\n\t" \
  "v_add_f32_dpp v122, v30, v30 row_ror:8 row_mask:0xf bank_mask:0xf\n\t" \
  "v_add_f32_dpp v123, v31, v31 row_ror:8 row_mask:0xf bank_mask:0xf\n\t" \
  "s_cbranch_vccnz L_slow1_%=\n\t" \
  "L_ok1_%=:\n\t" \
  "v_cvt_pk_bf16_f32 v120, v120, v121\n\t" \
  "v_cvt_pk_bf16_f32 v121, v122, v123\n\t" \
  "buffer_store_dwordx2 v[120:121], v5, s[40:43], s47 offen sc1\n\t" \
  "s_memrealtime s[8:9]\n\t" \
  "s_mov_b32 s99, s98\n\t" \
  "s_add_u32 s98, s98, s95\n\t" \
  "s_lshl_b32 s66, s44, 10\n\t" \
  "s_memrealtime s[10:11]\n\t" \
  "s_and_b32 s24, s66, -8192\n\t" \
  "s_add_u32 s24, s24, s66\n\t" \
  "s_add_u32 s24, s24, s23\n\t" \
  "s_memrealtime s[12:13]\n\t" \
  "s_add_u32 s44, s44, 1\n\t" \
  "s_mov_b32 s48, s45\n\t" \
  "s_add_u32 s45, s45, 524288\n\t" \
  "s_memrealtime s[14:15]\n\t" \
  "s_cmp_eq_u32 s45, 1572864\n\t" \
  "s_cselect_b32 s45, 0, s45\n\t" \
  "s_mov_b32 s46, s47\n\t" \
  "s_memrealtime s[16:17]\n\t" \
  "s_add_u32 s21, s47, 4096\n\t" \
  "s_add_u32 s47, s47, 16384\n\t" \
  "s_and_b32 s47, s47, 0xffff\n\t" \
  "s_memrealtime s[18:19]\n\t" \
  "s_add_u32 s98, s98, s3\n\t" \
  "s_mov_b32 s3, 0\n\t" \
  "s_add_u32 s67, s98, s26\n\t" \
  "v_mov_b32 v74, s67\n\t" \
  "s_mov_b64 s[78:79], exec\n\t" \
  "s_and_b64 exec, exec, 15\n\t" \
  "buffer_store_dwordx2 v[74:75], v72, s[40:43], s4 offen sc1\n\t" \
  "s_mov_b64 exec, s[78:79]\n\t" \
  "v_mov_b32 v72, 115728\n\t" \
  "v_mov_b32 v73, s98\n\t" \
  "ds_write_b32 v72, v73\n\t" \
  "L_w2_%=:\n\t" \
  "s_waitcnt lgkmcnt(0)\n\t" \
  "s_add_u32 s8, s8, s10\n\t" \
  "s_add_u32 s12, s12, s14\n\t" \
  "s_add_u32 s16, s16, s18\n\t" \
  "s_sub_u32 s66, s98, s8\n\t" \
  "s_sub_u32 s66, s66, s12\n\t" \
  "s_sub_u32 s66, s66, s16\n\t" \
  "s_sub_u32 s6, s98, s66\n\t" \
  "s_add_u32 s67, s66, s97\n\t" \
  "s_cmp_gt_u32 s67, 254\n\t" \
  "s_cbranch_scc1 L_late2_%=\n\t" \
  "L_ontime2_%=:\n\t" \
  "s_max_i32 s67, s66, 0\n\t" \
  "s_lshl_b32 s67, s67, 1\n\t" \
  "s_and_b32 s67, s67, -4\n\t" \
  "s_sub_u32 s66, 520, s67\n\t" \
  "s_getpc_b64 s[78:79]\n\t" \
  "s_add_u32 s78, s78, s66\n\t" \
  "s_addc_u32 s79, s79, 0\n\t" \
  "s_setpc_b64 s[78:79]\n\t" \
  "s_nop 1\n\t" \
  "s_nop 1\n\t" \
  "s_nop 1\n\t" \
  "s_nop 1\n\t" \
  "s_nop 1\n\t" \
  "s_nop 1\n\t" \
  "s_nop 1\n\t" \
  "s_nop 1\n\t" \
  "s_nop 1\n\t" \
  "s_nop 1\n\t" \
  "s_nop 1\n\t" \
  "s_nop 1\n\t" \
  "s_nop 1\n\t" \
  "s_nop 1\n\t" \
  "s_nop 1\n\t" \
  "s_nop 1\n\t" \
  "s_nop 1\n\t" \
  "s_nop 1\n\t" \
  "s_nop 1\n\t" \
  "s_nop 1\n\t" \
  "s_nop 1\n\t" \
  "s_nop 1\n\t" \
  "s_nop 1\n\t" \
  "s_nop 1\n\t" \
  "s_nop 1\n\t" \
  "s_nop 1\n\t" \
  "s_nop 1\n\t" \
  "s_nop 1\n\t" \
  "s_nop 1\n\t" \
  "s_nop 1\n\t" \
  "s_nop 1\n\t" \
  "s_nop 1\n\t" \
  "s_nop 1\n\t" \
  "s_nop 1\n\t" \
  "s_nop 1\n\t" \
  "s_nop 1\n\t" \
  "s_nop 1\n\t" \
  "s_nop 1\n\t" \
  "s_nop 1\n\t" \
  "s_nop 1\n\t" \
  "s_nop 1\n\t" \
  "s_nop 1\n\t" \
  "s_nop 1\n\t" \
  "s_nop 1\n\t" \
  "s_nop 1\n\t" \
  "s_nop 1\n\t" \
  "s_nop 1\n\t" \
  "s_nop 1\n\t" \
  "s_nop 1\n\t" \
  "s_nop 1\n\t" \
  "s_nop 1\n\t" \
  "s_nop 1\n\t" \
  "s_nop 1\n\t" \
  "s_nop 1\n\t" \
  "s_nop 1\n\t" \
  "s_nop 1\n\t" \
  "s_nop 1\n\t" \
  "s_nop 1\n\t" \
  "s_nop 1\n\t" \
  "s_nop 1\n\t" \
  "s_nop 1\n\t" \
  "s_nop 1\n\t" \
  "s_nop 1\n\t" \
  "s_nop 1\n\t" \
  "s_nop 1\n\t" \
  "s_nop 1\n\t" \
  "s_nop 1\n\t" \
  "s_nop 1\n\t" \
  "s_nop 1\n\t" \
  "s_nop 1\n\t" \
  "s_nop 1\n\t" \
  "s_nop 1\n\t" \
  "s_nop 1\n\t" \
  "s_nop 1\n\t" \
  "s_nop 1\n\t" \
  "s_nop 1\n\t" \
  "s_nop 1\n\t" \
  "s_nop 1\n\t" \
  "s_nop 1\n\t" \
  "s_nop 1\n\t" \
  "s_nop 1\n\t" \
  "s_nop 1\n\t" \
  "s_nop 1\n\t" \
  "s_nop 1\n\t" \
  "s_nop 1\n\t" \
  "s_nop 1\n\t" \
  "s_nop 1\n\t" \
  "s_nop 1\n\t" \
  "s_nop 1\n\t" \
  "s_nop 1\n\t" \
  "s_nop 1\n\t" \
  "s_nop 1\n\t" \
  "s_nop 1\n\t" \
  "s_nop 1\n\t" \
  "s_nop 1\n\t" \
  "s_nop 1\n\t" \
  "s_nop 1\n\t" \
  "s_nop 1\n\t" \
  "s_nop 1\n\t" \
  "s_nop 1\n\t" \
  "s_nop 1\n\t" \
  "s_nop 1\n\t" \
  "s_nop 1\n\t" \
  "s_nop 1\n\t" \
  "s_nop 1\n\t" \
  "s_nop 1\n\t" \
  "s_nop 1\n\t" \
  "s_nop 1\n\t" \
  "s_nop 1\n\t" \
  "s_nop 1\n\t" \
  "s_nop 1\n\t" \
  "s_nop 1\n\t" \
  "s_nop 1\n\t" \
  "s_nop 1\n\t" \
  "s_nop 1\n\t" \
  "s_nop 1\n\t" \
  "s_nop 1\n\t" \
  "s_nop 1\n\t" \
  "s_nop 1\n\t" \
  "s_nop 1\n\t" \
  "s_nop 1\n\t" \
  "s_nop 1\n\t" \
  "s_nop 1\n\t" \
  "s_nop 1\n\t" \
  "s_nop 1\n\t" \
  "s_nop 1\n\t" \
  "s_nop 1\n\t" \
  "L_b5_%=:\n\t" \
  "s_barrier\n\t" \
  "L_poll2_%=:\n\t" \
  "buffer_load_dwordx4 v[24:27], v2, s[40:43], s46 offen sc1\n\t" \
  "buffer_load_dwordx4 v[28:31], v2, s[40:43], s21 offen sc1\n\t" \
  "v_xor_b32 v13, 114688, v13\n\t" \
  "v_xor_b32 v7, 114688, v7\n\t" \
  "L_chk0_%=:\n\t" \
  "v_readfirstlane_b32 s66, v16\n\t" \
  "s_cmp_ge_u32 s66, s64\n\t" \
  "s_cbranch_scc1 L_chkd0_%=\n\t" \
  "s_sub_u32 s58, s58, 1\n\t" \
  "s_cbranch_scc1 L_exit_%=\n\t" \
  "v_mov_b32 v125, s63\n\t" \
  "buffer_load_dword v16, v125, s[40:43], 0 offen sc1\n\t" \
  "s_waitcnt vmcnt(0)\n\t" \
  "s_branch L_chk0_%=\n\t" \
  "L_chkd0_%=:\n\t" \
  "v_mov_b32 v16, s53\n\t" \
  "v_mul_u32_u24 v127, s7, v1\n\t" \
  "s_cmp_eq_u32 s7, 0\n\t" \
  "s_cselect_b32 s24, s24, s0\n\t" \
  "buffer_load_dwordx2 v[14:15], v127, s[40:43], s24 offen sc1\n\t" \
  "s_sub_u32 s66, s6, s99\n\t" \
  "s_add_u32 s67, s35, 24\n\t" \
  "s_sub_u32 s35, s35, 2\n\t" \
  "s_cmp_lt_u32 s66, s67\n\t" \
  "s_cselect_b32 s66, s66, 0\n\t" \
  "s_max_u32 s35, s35, s66\n\t" \
  "s_lshl_b32 s66, s34, 16\n\t" \
  "s_or_b32 s66, s66, s35\n\t" \
  "v_mov_b32 v126, s66\n\t" \
  "buffer_store_dword v126, off, s[40:43], s37 sc1\n\t" \
  "buffer_load_dword v16, v16, s[40:43], 0 offen sc1\n\t" \
  "s_cmp_eq_u32 s22, 1\n\t" \
  "s_cbranch_scc1 L_pub_%=\n\t" \
  "v_mov_b64 v[56:57], 0\n\t" \
  "v_mov_b64 v[58:59], 0\n\t" \
  "v_mov_b64 v[60:61], 0\n\t" \
  "v_mov_b64 v[62:63], 0\n\t" \
  "v_mov_b64 v[64:65], 0\n\t" \
  "v_mov_b64 v[66:67], 0\n\t" \
  "v_mov_b64 v[68:69], 0\n\t" \
  "v_mov_b64 v[70:71], 0\n\t" \
  "v_mov_b64 v[72:73], 0\n\t" \
  "v_mov_b64 v[74:75], 0\n\t" \
  "v_mov_b64 v[76:77], 0\n\t" \
  "v_mov_b64 v[78:79], 0\n\t" \
  "v_add_u32 v19, s60, v1\n\t" \
  "v_add_u32 v18, s61, v1\n\t" \
  "v_add_u32 v22, s62, v1\n\t" \
  "s_cbranch_execz L_dryret_%=\n\t" \
  "s_waitcnt vmcnt(4) lgkmcnt(0)\n\t" \
  "L_go_%=:\n\t" \
  "ds_write_b128 v17, v[24:27] offset:0\n\t" \
  "v_smfmac_f32_16x16x64_bf16 v[56:59], v[24:27], v[32:39], v0\n\t" \
  "v_max3_u32 v124, v24, v25, v26\n\t" \
  "s_waitcnt vmcnt(3)\n\t" \
  "L_go2_%=:\n\t" \
  "rmt_chain_dry:\n\t" \
  "ds_write_b128 v17, v[28:31] offset:4096\n\t" \
  "v_smfmac_f32_16x16x64_bf16 v[60:63], v[24:27], v[40:47], v0\n\t" \
  "v_max3_u32 v124, v124, v27, v28\n\t" \
  "v_smfmac_f32_16x16x64_bf16 v[64:67], v[24:27], v[48:55], v0\n\t" \
  "v_max3_u32 v124, v124, v29, v30\n\t" \
  "v_smfmac_f32_16x16x64_bf16 v[68:71], v[24:27], v[80:87], v0\n\t" \
  "v_max_u32 v124, v124, v31\n\t" \
  "v_smfmac_f32_16x16x64_bf16 v[56:59], v[28:31], v[88:95], v0\n\t" \
  "v_cmp_eq_u32 vcc, -1, v124\n\t" \
  "v_smfmac_f32_16x16x64_bf16 v[60:63], v[28:31], v[96:103], v0\n\t" \
  "v_smfmac_f32_16x16x64_bf16 v[64:67], v[28:31], v[104:111], v0\n\t" \
  "s_cbranch_vccnz L_retry2_%=\n\t" \
  "s_waitcnt lgkmcnt(0)\n\t" \
  "s_barrier\n\t" \
  "ds_read_b128 v[32:35], v19 offset:0\n\t" \
  "ds_read_b128 v[36:39], v19 offset:4096\n\t" \
  "v_smfmac_f32_16x16x64_bf16 v[68:71], v[28:31], v[112:119], v0\n\t" \
  "ds_read_b128 v[40:43], v18 offset:0\n\t" \
  "v_smfmac_f32_16x16x64_bf16 v[72:75], v[24:27], v[128:135], v0\n\t" \
  "ds_read_b128 v[44:47], v18 offset:4096\n\t" \
  "v_smfmac_f32_16x16x64_bf16 v[76:79], v[24:27], v[192:199], v0\n\t" \
  "ds_read_b128 v[48:51], v22 offset:0\n\t" \
  "v_smfmac_f32_16x16x64_bf16 v[72:75], v[28:31], v[136:143], v0\n\t" \
  "ds_read_b128 v[52:55], v22 offset:4096\n\t" \
  "v_smfmac_f32_16x16x64_bf16 v[76:79], v[28:31], v[200:207], v0\n\t" \
  "s_waitcnt lgkmcnt(5)\n\t" \
  "v_smfmac_f32_16x16x64_bf16 v[56:59], v[32:35], a[0:7], v0\n\t" \
  "ds_read_b128 v[96:99], v13 offset:0\n\t" \
  "v_smfmac_f32_16x16x64_bf16 v[60:63], v[32:35], a[48:55], v0\n\t" \
  "s_waitcnt lgkmcnt(5)\n\t" \
  "v_smfmac_f32_16x16x64_bf16 v[56:59], v[36:39], a[8:15], v0\n\t" \
  "ds_read_b128 v[100:103], v13 offset:1024\n\t" \
  "v_smfmac_f32_16x16x64_bf16 v[60:63], v[36:39], a[56:63], v0\n\t" \
  "s_waitcnt lgkmcnt(5)\n\t" \
  "v_smfmac_f32_16x16x64_bf16 v[56:59], v[40:43], a[16:23], v0\n\t" \
  "ds_read_b128 v[104:107], v13 offset:2048\n\t" \
  "v_smfmac_f32_16x16x64_bf16 v[60:63], v[40:43], a[64:71], v0\n\t" \
  "s_waitcnt lgkmcnt(5)\n\t" \
  "v_smfmac_f32_16x16x64_bf16 v[56:59], v[44:47], a[24:31], v0\n\t" \
  "ds_read_b128 v[108:111], v13 offset:3072\n\t" \
  "v_smfmac_f32_16x16x64_bf16 v[60:63], v[44:47], a[72:79], v0\n\t" \
  "s_waitcnt lgkmcnt(5)\n\t" \
  "v_smfmac_f32_16x16x64_bf16 v[56:59], v[48:51], a[32:39], v0\n\t" \
  "ds_read_b128 v[120:123], v13 offset:4096\n\t" \
  "v_smfmac_f32_16x16x64_bf16 v[60:63], v[48:51], a[80:87], v0\n\t" \
  "s_waitcnt lgkmcnt(5)\n\t" \
  "v_smfmac_f32_16x16x64_bf16 v[56:59], v[52:55], a[40:47], v0\n\t" \
  "ds_read_b128 v[18:21], v13 offset:5120\n\t" \
  "v_smfmac_f32_16x16x64_bf16 v[60:63], v[52:55], a[88:95], v0\n\t" \
  "v_smfmac_f32_16x16x64_bf16 v[64:67], v[32:35], a[96:103], v0\n\t" \
  "v_add_f32 v56, v56, v57\n\t" \
  "v_smfmac_f32_16x16x64_bf16 v[68:71], v[32:35], a[144:151], v0\n\t" \
  "v_add_f32 v58, v58, v59\n\t" \
  "v_smfmac_f32_16x16x64_bf16 v[64:67], v[36:39], a[104:111], v0\n\t" \
  "v_add_f32 v60, v60, v61\n\t" \
  "v_smfmac_f32_16x16x64_bf16 v[68:71], v[36:39], a[152:159], v0\n\t" \
  "v_add_f32 v62, v62, v63\n\t" \
  "v_smfmac_f32_16x16x64_bf16 v[64:67], v[40:43], a[112:119], v0\n\t" \
  "v_cvt_pk_bf16_f32 v126, v56, v60\n\t" \
  "ds_write_b32 v6, v126 offset:8192\n\t" \
  "v_smfmac_f32_16x16x64_bf16 v[68:71], v[40:43], a[160:167], v0\n\t" \
  "v_cvt_pk_bf16_f32 v127, v58, v62\n\t" \
  "ds_write_b32 v6, v127 offset:8208\n\t" \
  "v_smfmac_f32_16x16x64_bf16 v[64:67], v[44:47], a[120:127], v0\n\t" \
  "ds_read_b128 v[56:59], v13 offset:6144\n\t" \
  "v_smfmac_f32_16x16x64_bf16 v[68:71], v[44:47], a[168:175], v0\n\t" \
  "ds_read_b128 v[60:63], v13 offset:7168\n\t" \
  "v_smfmac_f32_16x16x64_bf16 v[64:67], v[48:51], a[128:135], v0\n\t" \
  "v_smfmac_f32_16x16x64_bf16 v[68:71], v[48:51], a[176:183], v0\n\t" \
  "v_smfmac_f32_16x16x64_bf16 v[64:67], v[52:55], a[136:143], v0\n\t" \
  "s_waitcnt lgkmcnt(2)\n\t" \
  "s_barrier\n\t" \
  "ds_read_b128 v[80:83], v8 offset:8192\n\t" \
  "v_smfmac_f32_16x16x64_bf16 v[68:71], v[52:55], a[184:191], v0\n\t" \
  "ds_read_b128 v[84:87], v8 offset:8704\n\t" \
  "v_smfmac_f32_16x16x64_bf16 v[72:75], v[32:35], v[144:151], v0\n\t" \
  "ds_read_b128 v[88:91], v8 offset:9216\n\t" \
  "v_add_f32 v64, v64, v65\n\t" \
  "v_smfmac_f32_16x16x64_bf16 v[76:79], v[32:35], v[208:215], v0\n\t" \
  "ds_read_b128 v[92:95], v8 offset:9728\n\t" \
  "v_add_f32 v66, v66, v67\n\t" \
  "v_add_f32 v68, v68, v69\n\t" \
  "v_add_f32 v70, v70, v71\n\t" \
  "v_cvt_pk_bf16_f32 v126, v64, v68\n\t" \
  "ds_write_b32 v6, v126 offset:10240\n\t" \
  "v_cvt_pk_bf16_f32 v127, v66, v70\n\t" \
  "ds_write_b32 v6, v127 offset:10256\n\t" \
  "v_smfmac_f32_16x16x64_bf16 v[72:75], v[36:39], v[152:159], v0\n\t" \
  "s_waitcnt lgkmcnt(5)\n\t" \
  "v_mfma_f32_16x16x32_bf16 v[112:115], v[96:99], v[80:83], 0\n\t" \
  "v_mfma_f32_16x16x32_bf16 v[116:119], v[120:123], v[80:83], 0\n\t" \
  "s_waitcnt lgkmcnt(4)\n\t" \
  "v_mfma_f32_16x16x32_bf16 v[112:115], v[100:103], v[84:87], v[112:115]\n\t" \
  "v_mfma_f32_16x16x32_bf16 v[116:119], v[18:21], v[84:87], v[116:119]\n\t" \
  "s_waitcnt lgkmcnt(0)\n\t" \
  "s_barrier\n\t" \
  "ds_read_b128 v[64:67], v9 offset:10240\n\t" \
  "v_mfma_f32_16x16x32_bf16 v[112:115], v[104:107], v[88:91], v[112:115]\n\t" \
  "ds_read_b128 v[68:71], v9 offset:10752\n\t" \
  "v_mfma_f32_16x16x32_bf16 v[116:119], v[56:59], v[88:91], v[116:119]\n\t" \
  "ds_read_b128 v[96:99], v9 offset:11264\n\t" \
  "v_mfma_f32_16x16x32_bf16 v[112:115], v[108:111], v[92:95], v[112:115]\n\t" \
  "ds_read_b128 v[100:103], v9 offset:11776\n\t" \
  "v_mfma_f32_16x16x32_bf16 v[116:119], v[60:63], v[92:95], v[116:119]\n\t" \
  "ds_read_b128 v[104:107], v7 offset:0\n\t" \
  "v_smfmac_f32_16x16x64_bf16 v[76:79], v[36:39], v[216:223], v0\n\t" \
  "ds_read_b128 v[108:111], v7 offset:1024\n\t" \
  "s_waitcnt lgkmcnt(5)\n\t" \
  "v_mfma_f32_16x16x32_bf16 v[120:123], v[64:67], v[80:83], 0\n\t" \
  "v_max3_f32 v124, v112, v113, v114\n\t" \
  "s_waitcnt lgkmcnt(4)\n\t" \
  "v_mfma_f32_16x16x32_bf16 v[120:123], v[68:71], v[84:87], v[120:123]\n\t" \
  "v_max3_f32 v124, v124, v115, v116\n\t" \
  "v_max3_f32 v124, v124, v117, v118\n\t" \
  "s_waitcnt lgkmcnt(3)\n\t" \
  "v_mfma_f32_16x16x32_bf16 v[120:123], v[96:99], v[88:91], v[120:123]\n\t" \
  "v_max_f32 v124, v124, v119\n\t" \
  "v_mov_b64 v[80:81], s[72:73]\n\t" \
  "s_waitcnt lgkmcnt(2)\n\t" \
  "v_mfma_f32_16x16x32_bf16 v[120:123], v[100:103], v[92:95], v[120:123]\n\t" \
  "v_mov_b64 v[82:83], s[72:73]\n\t" \
  "v_smfmac_f32_16x16x64_bf16 v[72:75], v[40:43], v[160:167], v0\n\t" \
  "v_mov_b64 v[68:69], s[74:75]\n\t" \
  "v_smfmac_f32_16x16x64_bf16 v[76:79], v[40:43], v[224:231], v0\n\t" \
  "v_max3_f32 v125, v124, v120, v121\n\t" \
  "v_max3_f32 v124, v124, v120, v121\n\t" \
  "v_smfmac_f32_16x16x64_bf16 v[72:75], v[44:47], v[168:175], v0\n\t" \
  "s_nop 0\n\t" \
  "v_permlane16_swap_b32 v124, v125\n\t" \
  "v_max_f32 v126, v124, v125\n\t" \
  "v_max_f32 v124, v124, v125\n\t" \
  "v_smfmac_f32_16x16x64_bf16 v[76:79], v[44:47], v[232:239], v0\n\t" \
  "s_nop 0\n\t" \
  "v_permlane32_swap_b32 v124, v126\n\t" \
  "v_max_f32 v124, v124, v126\n\t" \
  "v_mul_f32 v124, s59, v124\n\t" \
  "v_fma_f32 v112, v112, s59, -v124\n\t" \
  "v_smfmac_f32_16x16x64_bf16 v[72:75], v[48:51], v[176:183], v0\n\t" \
  "v_fma_f32 v113, v113, s59, -v124\n\t" \
  "v_fma_f32 v114, v114, s59, -v124\n\t" \
  "v_smfmac_f32_16x16x64_bf16 v[76:79], v[48:51], v[240:247], v0\n\t" \
  "v_fma_f32 v115, v115, s59, -v124\n\t" \
  "v_fma_f32 v116, v116, s59, -v124\n\t" \
  "v_smfmac_f32_16x16x64_bf16 v[72:75], v[52:55], v[184:191], v0\n\t" \
  "v_fma_f32 v117, v117, s59, -v124\n\t" \
  "v_fma_f32 v118, v118, s59, -v124\n\t" \
  "v_smfmac_f32_16x16x64_bf16 v[76:79], v[52:55], v[248:255], v0\n\t" \
  "v_fma_f32 v119, v119, s59, -v124\n\t" \
  "v_fma_f32 v120, v120, s59, -v124\n\t" \
  "v_fma_f32 v121, v121, s59, -v124\n\t" \
  "v_exp_f32 v112, v112\n\t" \
  "v_exp_f32 v113, v113\n\t" \
  "v_exp_f32 v114, v114\n\t" \
  "v_exp_f32 v115, v115\n\t" \
  "v_exp_f32 v116, v116\n\t" \
  "v_exp_f32 v117, v117\n\t" \
  "v_exp_f32 v118, v118\n\t" \
  "v_exp_f32 v119, v119\n\t" \
  "v_exp_f32 v120, v120\n\t" \
  "v_exp_f32 v121, v121\n\t" \
  "v_cvt_pk_bf16_f32 v18, v112, v113\n\t" \
  "v_cvt_pk_bf16_f32 v19, v114, v115\n\t" \
  "v_cvt_pk_bf16_f32 v20, v116, v117\n\t" \
  "v_cvt_pk_bf16_f32 v21, v118, v119\n\t" \
  "v_cvt_pk_bf16_f32 v22, v120, v120\n\t" \
  "v_cvt_pk_bf16_f32 v23, v121, v121\n\t" \
  "s_waitcnt lgkmcnt(0)\n\t" \
  "v_mfma_f32_16x16x32_bf16 v[112:115], v[80:83], v[18:21], 0\n\t" \
  "v_cvt_pk_bf16_f32 v64, v72, v73\n\t" \
  "v_cvt_pk_bf16_f32 v65, v74, v75\n\t" \
  "v_mfma_f32_16x16x32_bf16 v[96:99], v[104:107], v[18:21], 0\n\t" \
  "v_cvt_pk_bf16_f32 v66, v76, v77\n\t" \
  "v_cvt_pk_bf16_f32 v67, v78, v79\n\t" \
  "v_mfma_f32_16x16x32_bf16 v[100:103], v[108:111], v[18:21], 0\n\t" \
  "s_cmp_eq_u32 s44, 63\n\t" \
  "s_cbranch_scc1 L_final_%=\n\t" \
  "v_mfma_f32_16x16x16_bf16 v[112:115], v[68:69], v[22:23], v[112:115]\n\t" \
  "v_mov_b64 v[116:117], 0\n\t" \
  "v_mov_b64 v[118:119], 0\n\t" \
  "v_mfma_f32_16x16x16_bf16 v[96:99], v[64:65], v[22:23], v[96:99]\n\t" \
  "v_mov_b64 v[120:121], 0\n\t" \
  "v_mov_b64 v[122:123], 0\n\t" \
  "v_mfma_f32_16x16x16_bf16 v[100:103], v[66:67], v[22:23], v[100:103]\n\t" \
  "v_rcp_f32 v126, v112\n\t" \
  "s_nop 1\n\t" \
  "v_cndmask_b32 v96, v98, v96, s[68:69]\n\t" \
  "v_cndmask_b32 v97, v99, v97, s[68:69]\n\t" \
  "v_mul_f32 v96, v96, v126\n\t" \
  "v_mul_f32 v97, v97, v126\n\t" \
  "s_nop 0\n\t" \
  "v_cndmask_b32 v100, v102, v100, s[68:69]\n\t" \
  "v_cndmask_b32 v101, v103, v101, s[68:69]\n\t" \
  "v_pk_mul_f32 v[100:101], v[100:101], v[126:127] op_sel_hi:[1,0]\n\t" \
  "v_cvt_pk_bf16_f32 v96, v96, v100\n\t" \
  "v_cvt_pk_bf16_f32 v97, v97, v101\n\t" \
  "ds_write_b64 v11, v[96:97] offset:12288\n\t" \
  "s_mov_b32 m0, s70\n\t" \
  "s_nop 0\n\t" \
  "buffer_load_dwordx4 v1, s[40:43], s51 offen offset:0 sc1 lds\n\t" \
  "s_waitcnt lgkmcnt(0)\n\t" \
  "s_barrier\n\t" \
  "ds_read_b128 v[56:59], v10 offset:12288\n\t" \
  "ds_read_b128 v[60:63], v10 offset:12800\n\t" \
  "ds_read_b128 v[64:67], v10 offset:13312\n\t" \
  "ds_read_b128 v[68:71], v10 offset:13824\n\t" \
  "buffer_load_dwordx4 v1, s[40:43], s51 offen offset:1024 sc1 lds\n\t" \
  "s_waitcnt lgkmcnt(3)\n\t" \
  "v_smfmac_f32_16x16x64_bf16 v[116:119], v[56:59], a[192:199], v0\n\t" \
  "v_smfmac_f32_16x16x64_bf16 v[120:123], v[56:59], a[200:207], v0\n\t" \
  "s_waitcnt lgkmcnt(2)\n\t" \
  "v_smfmac_f32_16x16x64_bf16 v[116:119], v[60:63], a[208:215], v0\n\t" \
  "v_smfmac_f32_16x16x64_bf16 v[120:123], v[60:63], a[216:223], v0\n\t" \
  "s_waitcnt lgkmcnt(1)\n\t" \
  "v_smfmac_f32_16x16x64_bf16 v[116:119], v[64:67], a[224:231], v0\n\t" \
  "v_smfmac_f32_16x16x64_bf16 v[120:123], v[64:67], a[232:239], v0\n\t" \
  "s_waitcnt lgkmcnt(0)\n\t" \
  "v_smfmac_f32_16x16x64_bf16 v[116:119], v[68:71], a[240:247], v0\n\t" \
  "v_smfmac_f32_16x16x64_bf16 v[120:123], v[68:71], a[248:255], v0\n\t" \
  "s_nop 2\n\t" \
  "v_cvt_pk_bf16_f32 v96, v116, v117\n\t" \
  "v_cvt_pk_bf16_f32 v98, v118, v119\n\t" \
  "s_nop 2\n\t" \
  "v_cvt_pk_bf16_f32 v97, v120, v121\n\t" \
  "v_cvt_pk_bf16_f32 v99, v122, v123\n\t" \
  "buffer_store_dwordx4 v[96:99], v3, s[40:43], s45 offen sc1\n\t" \
  "rmt_chain_dry2:\n\t" \
  "s_cmp_eq_u32 s49, 0\n\t" \
  "s_cbranch_scc1 L_red_%=\n\t" \
  "s_cmp_eq_u32 s49, 7\n\t" \
  "s_cbranch_scc0 L_idle_%=\n\t" \
  "s_setpc_b64 s[76:77]\n\t" \
  "L_idle_%=:\n\t" \
  "s_waitcnt vmcnt(3)\n\t" \
  "v_add_f32 v72, v72, v73\n\t" \
  "v_add_f32 v74, v74, v75\n\t" \
  "v_add_f32 v76, v76, v77\n\t" \
  "v_add_f32 v78, v78, v79\n\t" \
  "v_cvt_pk_bf16_f32 v126, v72, v74\n\t" \
  "v_cvt_pk_bf16_f32 v127, v76, v78\n\t" \
  "ds_write_b64 v12, v[126:127] offset:14336\n\t" \
  "s_lshl_b32 s66, s49, 14\n\t" \
  "s_add_u32 s66, s66, 32768\n\t" \
  "v_add_u32 v124, s66, v1\n\t" \
  "ds_read_b128 v[32:35], v124 offset:0\n\t" \
  "ds_read_b128 v[36:39], v124 offset:1024\n\t" \
  "ds_read_b128 v[40:43], v124 offset:2048\n\t" \
  "ds_read_b128 v[44:47], v124 offset:3072\n\t" \
  "ds_read_b128 v[48:51], v124 offset:4096\n\t" \
  "ds_read_b128 v[52:55], v124 offset:5120\n\t" \
  "ds_read_b128 v[80:83], v124 offset:6144\n\t" \
  "ds_read_b128 v[84:87], v124 offset:7168\n\t" \
  "ds_read_b128 v[88:91], v124 offset:8192\n\t" \
  "ds_read_b128 v[92:95], v124 offset:9216\n\t" \
  "ds_read_b128 v[96:99], v124 offset:10240\n\t" \
  "ds_read_b128 v[100:103], v124 offset:11264\n\t" \
  "ds_read_b128 v[104:107], v124 offset:12288\n\t" \
  "ds_read_b128 v[108:111], v124 offset:13312\n\t" \
  "ds_read_b128 v[112:115], v124 offset:14336\n\t" \
  "ds_read_b128 v[116:119], v124 offset:15360\n\t" \
  "s_barrier\n\t" \
  "s_lshl_b32 s66, s49, 10\n\t" \
  "s_add_u32 s66, s66, 5120\n\t" \
  "s_add_u32 s67, s70, s66\n\t" \
  "s_mov_b32 m0, s67\n\t" \
  "s_add_u32 s67, s51, s66\n\t" \
  "buffer_load_dwordx4 v1, s[40:43], s67 offen offset:0 sc1 lds\n\t" \
  "buffer_load_dwordx4 v1, s[40:43], s67 offen offset:1024 sc1 lds\n\t" \
  "v_mov_b64 v[64:65], -1\n\t" \
  "v_mov_b64 v[66:67], -1\n\t" \
  "s_cmp_eq_u32 s49, 3\n\t" \
  "s_cbranch_scc1 L_i3_%=\n\t" \
  "buffer_load_dwordx4 v1, s[40:43], s67 offen offset:2048 sc1 lds\n\t" \
  "s_cmp_eq_u32 s49, 2\n\t" \
  "s_cbranch_scc1 L_i2_%=\n\t" \
  "s_cmp_lg_u32 s50, 0\n\t" \
  "s_cbranch_scc1 L_idone_%=\n\t" \
  "v_mov_b32 v68, s44\n\t" \
  "v_mov_b32 v69, s1\n\t" \
  "buffer_store_dwordx2 v[68:69], off, s[40:43], s31 sc1\n\t" \
  "s_branch L_idone_%=\n\t" \
  "L_i2_%=:\n\t" \
  "s_xor_b32 s67, s47, 0x8000\n\t" \
  "buffer_store_dwordx2 v[64:65], v5, s[40:43], s67 offen sc1\n\t" \
  "s_cmp_eq_u32 s28, 0\n\t" \
  "s_cbranch_scc0 L_idone_%=\n\t" \
  "v_mov_b32 v125, v14\n\t" \
  "v_and_b32 v124, 0xffff, v125\n\t" \
  "s_nop 0\n\t" \
  "v_max_u32_dpp v125, v125, v125 row_ror:1 row_mask:0xf bank_mask:0xf\n\t" \
  "v_max_u32_dpp v124, v124, v124 row_ror:1 row_mask:0xf bank_mask:0xf\n\t" \
  "s_nop 0\n\t" \
  "v_max_u32_dpp v125, v125, v125 row_ror:2 row_mask:0xf bank_mask:0xf\n\t" \
  "v_max_u32_dpp v124, v124, v124 row_ror:2 row_mask:0xf bank_mask:0xf\n\t" \
  "s_nop 0\n\t" \
  "v_max_u32_dpp v125, v125, v125 row_ror:4 row_mask:0xf bank_mask:0xf\n\t" \
  "v_max_u32_dpp v124, v124, v124 row_ror:4 row_mask:0xf bank_mask:0xf\n\t" \
  "s_nop 0\n\t" \
  "v_max_u32_dpp v125, v125, v125 row_ror:8 row_mask:0xf bank_mask:0xf\n\t" \
  "v_max_u32_dpp v124, v124, v124 row_ror:8 row_mask:0xf bank_mask:0xf\n\t" \
  "v_and_b32 v125, 0xffff0000, v125\n\t" \
  "v_or_b32 v125, v125, v124\n\t" \
  "v_mov_b32 v124, v125\n\t" \
  "s_nop 1\n\t" \
  "v_permlane16_swap_b32 v124, v125\n\t" \
  "v_pk_max_u16 v125, v124, v125\n\t" \
  "v_mov_b32 v124, v125\n\t" \
  "s_nop 1\n\t" \
  "v_permlane32_swap_b32 v124, v125\n\t" \
  "v_pk_max_u16 v125, v124, v125\n\t" \
  "v_mov_b32 v124, 115712\n\t" \
  "ds_write_b32 v124, v125\n\t" \
  "s_branch L_idone_%=\n\t" \
  "L_i3_%=:\n\t" \
  "buffer_store_dwordx4 v[64:67], v4, s[40:43], s48 offen offset:0 sc1\n\t" \
  "buffer_store_dwordx4 v[64:67], v4, s[40:43], s48 offen offset:1024 sc1\n\t" \
  "buffer_store_dwordx4 v[64:67], v4, s[40:43], s48 offen offset:2048 sc1\n\t" \
  "buffer_store_dwordx4 v[64:67], v4, s[40:43], s48 offen offset:3072 sc1\n\t" \
  "L_idone_%=:\n\t" \
  "s_xor_b32 s70, s70, 114688\n\t" \
  "s_add_u32 s51, s51, 524288\n\t" \
  "s_add_u32 s52, s52, 131072\n\t" \
  "s_mov_b32 s63, s53\n\t" \
  "s_mov_b32 s64, s54\n\t" \
  "s_add_u32 s55, s55, 1\n\t" \
  "s_add_u32 s53, s53, 128\n\t" \
  "s_cmp_eq_u32 s55, 3\n\t" \
  "s_cselect_b32 s55, 0, s55\n\t" \
  "s_cselect_b32 s53, s65, s53\n\t" \
  "s_addc_u32 s54, s54, 0\n\t" \
  "s_cmp_eq_u32 s44, 62\n\t" \
  "s_cselect_b32 s64, 0, s64\n\t" \
  "s_lshl_b32 s66, s44, 10\n\t" \
  "s_and_b32 s24, s66, -8192\n\t" \
  "s_add_u32 s24, s24, s66\n\t" \
  "s_add_u32 s24, s24, s23\n\t" \
  "s_add_u32 s44, s44, 1\n\t" \
  "s_mov_b32 s48, s45\n\t" \
  "s_add_u32 s45, s45, 524288\n\t" \
  "s_cmp_eq_u32 s45, 1572864\n\t" \
  "s_cselect_b32 s45, 0, s45\n\t" \
  "s_mov_b32 s46, s47\n\t" \
  "s_add_u32 s21, s47, 4096\n\t" \
  "s_add_u32 s47, s47, 16384\n\t" \
  "s_and_b32 s47, s47, 0xffff\n\t" \
  "s_waitcnt lgkmcnt(0)\n\t" \
  "s_branch L_b5_%=\n\t" \
  "L_retry2_%=:\n\t" \
  "s_nop 7\n\t" \
  "s_sub_u32 s58, s58, 1\n\t" \
  "s_cbranch_scc1 L_exit_%=\n\t" \
  "v_mov_b32 v125, 115728\n\t" \
  "ds_read_b32 v125, v125\n\t" \
  "s_waitcnt lgkmcnt(0)\n\t" \
  "v_readfirstlane_b32 s76, v125\n\t" \
  "s_mov_b32 s77, 0\n\t" \
  "s_memrealtime s[8:9]\n\t" \
  "s_nop 2\n\t" \
  "s_memrealtime s[10:11]\n\t" \
  "s_nop 2\n\t" \
  "s_memrealtime s[12:13]\n\t" \
  "s_nop 2\n\t" \
  "s_memrealtime s[14:15]\n\t" \
  "s_nop 2\n\t" \
  "s_memrealtime s[16:17]\n\t" \
  "s_nop 2\n\t" \
  "s_memrealtime s[18:19]\n\t" \
  "L_w3_%=:\n\t" \
  "s_waitcnt lgkmcnt(0)\n\t" \
  "s_add_u32 s8, s8, s10\n\t" \
  "s_add_u32 s12, s12, s14\n\t" \
  "s_add_u32 s16, s16, s18\n\t" \
  "s_sub_u32 s66, s76, s8\n\t" \
  "s_sub_u32 s66, s66, s12\n\t" \
  "s_sub_u32 s66, s66, s16\n\t" \
  "s_add_u32 s67, s66, s77\n\t" \
  "s_cmp_gt_u32 s67, 254\n\t" \
  "s_cbranch_scc1 L_late3_%=\n\t" \
  "L_ontime3_%=:\n\t" \
  "s_max_i32 s67, s66, 0\n\t" \
  "s_lshl_b32 s67, s67, 1\n\t" \
  "s_and_b32 s67, s67, -4\n\t" \
  "s_sub_u32 s66, 520, s67\n\t" \
  "s_getpc_b64 s[78:79]\n\t" \
  "s_add_u32 s78, s78, s66\n\t" \
  "s_addc_u32 s79, s79, 0\n\t" \
  "s_setpc_b64 s[78:79]\n\t" \
  "s_nop 1\n\t" \
  "s_nop 1\n\t" \
  "s_nop 1\n\t" \
  "s_nop 1\n\t" \
  "s_nop 1\n\t" \
  "s_nop 1\n\t" \
  "s_nop 1\n\t" \
  "s_nop 1\n\t" \
  "s_nop 1\n\t" \
  "s_nop 1\n\t" \
  "s_nop 1\n\t" \
  "s_nop 1\n\t" \
  "s_nop 1\n\t" \
  "s_nop 1\n\t" \
  "s_nop 1\n\t" \
  "s_nop 1\n\t" \
  "s_nop 1\n\t" \
  "s_nop 1\n\t" \
  "s_nop 1\n\t" \
  "s_nop 1\n\t" \
  "s_nop 1\n\t" \
  "s_nop 1\n\t" \
  "s_nop 1\n\t" \
  "s_nop 1\n\t" \
  "s_nop 1\n\t" \
  "s_nop 1\n\t" \
  "s_nop 1\n\t" \
  "s_nop 1\n\t" \
  "s_nop 1\n\t" \
  "s_nop 1\n\t" \
  "s_nop 1\n\t" \
  "s_nop 1\n\t" \
  "s_nop 1\n\t" \
  "s_nop 1\n\t" \
  "s_nop 1\n\t" \
  "s_nop 1\n\t" \
  "s_nop 1\n\t" \
  "s_nop 1\n\t" \
  "s_nop 1\n\t" \
  "s_nop 1\n\t" \
  "s_nop 1\n\t" \
  "s_nop 1\n\t" \
  "s_nop 1\n\t" \
  "s_nop 1\n\t" \
  "s_nop 1\n\t" \
  "s_nop 1\n\t" \
  "s_nop 1\n\t" \
  "s_nop 1\n\t" \
  "s_nop 1\n\t" \
  "s_nop 1\n\t" \
  "s_nop 1\n\t" \
  "s_nop 1\n\t" \
  "s_nop 1\n\t" \
  "s_nop 1\n\t" \
  "s_nop 1\n\t" \
  "s_nop 1\n\t" \
  "s_nop 1\n\t" \
  "s_nop 1\n\t" \
  "s_nop 1\n\t" \
  "s_nop 1\n\t" \
  "s_nop 1\n\t" \
  "s_nop 1\n\t" \
  "s_nop 1\n\t" \
  "s_nop 1\n\t" \
  "s_nop 1\n\t" \
  "s_nop 1\n\t" \
  "s_nop 1\n\t" \
  "s_nop 1\n\t" \
  "s_nop 1\n\t" \
  "s_nop 1\n\t" \
  "s_nop 1\n\t" \
  "s_nop 1\n\t" \
  "s_nop 1\n\t" \
  "s_nop 1\n\t" \
  "s_nop 1\n\t" \
  "s_nop 1\n\t" \
  "s_nop 1\n\t" \
  "s_nop 1\n\t" \
  "s_nop 1\n\t" \
  "s_nop 1\n\t" \
  "s_nop 1\n\t" \
  "s_nop 1\n\t" \
  "s_nop 1\n\t" \
  "s_nop 1\n\t" \
  "s_nop 1\n\t" \
  "s_nop 1\n\t" \
  "s_nop 1\n\t" \
  "s_nop 1\n\t" \
  "s_nop 1\n\t" \
  "s_nop 1\n\t" \
  "s_nop 1\n\t" \
  "s_nop 1\n\t" \
  "s_nop 1\n\t" \
  "s_nop 1\n\t" \
  "s_nop 1\n\t" \
  "s_nop 1\n\t" \
  "s_nop 1\n\t" \
  "s_nop 1\n\t" \
  "s_nop 1\n\t" \
  "s_nop 1\n\t" \
  "s_nop 1\n\t" \
  "s_nop 1\n\t" \
  "s_nop 1\n\t" \
  "s_nop 1\n\t" \
  "s_nop 1\n\t" \
  "s_nop 1\n\t" \
  "s_nop 1\n\t" \
  "s_nop 1\n\t" \
  "s_nop 1\n\t" \
  "s_nop 1\n\t" \
  "s_nop 1\n\t" \
  "s_nop 1\n\t" \
  "s_nop 1\n\t" \
  "s_nop 1\n\t" \
  "s_nop 1\n\t" \
  "s_nop 1\n\t" \
  "s_nop 1\n\t" \
  "s_nop 1\n\t" \
  "s_nop 1\n\t" \
  "s_nop 1\n\t" \
  "s_nop 1\n\t" \
  "s_nop 1\n\t" \
  "s_nop 1\n\t" \
  "s_nop 1\n\t" \
  "s_nop 1\n\t" \
  "s_nop 1\n\t" \
  "s_nop 1\n\t" \
  "buffer_load_dwordx4 v[24:27], v2, s[40:43], s46 offen sc1\n\t" \
  "buffer_load_dwordx4 v[28:31], v2, s[40:43], s21 offen sc1\n\t" \
  "v_mul_u32_u24 v127, s7, v1\n\t" \
  "buffer_load_dwordx2 v[14:15], v127, s[40:43], s24 offen sc1\n\t" \
  "s_branch L_again_%=\n\t" \
  "L_first_%=:\n\t" \
  "v_lshlrev_b32 v124, 3, v1\n\t" \
  "s_cmp_eq_u32 s49, 0\n\t" \
  "s_cselect_b32 s66, s29, 0x80000000\n\t" \
  "buffer_load_dwordx2 v[14:15], v124, s[40:43], s66 offen sc1\n\t" \
  "v_mov_b32 v16, s53\n\t" \
  "buffer_load_dword v16, v16, s[40:43], 0 offen sc1\n\t" \
  "v_mov_b64 v[56:57], 0\n\t" \
  "v_mov_b64 v[58:59], 0\n\t" \
  "v_mov_b64 v[60:61], 0\n\t" \
  "v_mov_b64 v[62:63], 0\n\t" \
  "v_mov_b64 v[64:65], 0\n\t" \
  "v_mov_b64 v[66:67], 0\n\t" \
  "v_mov_b64 v[68:69], 0\n\t" \
  "v_mov_b64 v[70:71], 0\n\t" \
  "v_mov_b64 v[72:73], 0\n\t" \
  "v_mov_b64 v[74:75], 0\n\t" \
  "v_mov_b64 v[76:77], 0\n\t" \
  "v_mov_b64 v[78:79], 0\n\t" \
  "v_add_u32 v19, s60, v1\n\t" \
  "v_add_u32 v18, s61, v1\n\t" \
  "v_add_u32 v22, s62, v1\n\t" \
  "s_waitcnt lgkmcnt(0)\n\t" \
  "s_branch L_go_%=\n\t" \
  "L_again_%=:\n\t" \
  "v_mov_b64 v[56:57], 0\n\t" \
  "v_mov_b64 v[58:59], 0\n\t" \
  "v_mov_b64 v[60:61], 0\n\t" \
  "v_mov_b64 v[62:63], 0\n\t" \
  "v_mov_b64 v[64:65], 0\n\t" \
  "v_mov_b64 v[66:67], 0\n\t" \
  "v_mov_b64 v[68:69], 0\n\t" \
  "v_mov_b64 v[70:71], 0\n\t" \
  "v_mov_b64 v[72:73], 0\n\t" \
  "v_mov_b64 v[74:75], 0\n\t" \
  "v_mov_b64 v[76:77], 0\n\t" \
  "v_mov_b64 v[78:79], 0\n\t" \
  "v_add_u32 v19, s60, v1\n\t" \
  "v_add_u32 v18, s61, v1\n\t" \
  "v_add_u32 v22, s62, v1\n\t" \
  "s_waitcnt vmcnt(0) lgkmcnt(0)\n\t" \
  "s_branch L_go_%=\n\t" \
  "L_dryret_%=:\n\t" \
  "s_setpc_b64 s[74:75]\n\t" \
  "L_pub_%=:\n\t" \
  "s_sub_u32 s67, s52, 131072\n\t" \
  "ds_read_b128 v[56:59], v1 offset:10240\n\t" \
  "ds_read_b128 v[60:63], v1 offset:11264\n\t" \
  "ds_read_b128 v[64:67], v1 offset:14336\n\t" \
  "ds_read_b128 v[68:71], v1 offset:15360\n\t" \
  "s_waitcnt lgkmcnt(0)\n\t" \
  "buffer_store_dwordx4 v[56:59], v1, s[40:43], s67 offen offset:0 sc1\n\t" \
  "buffer_store_dwordx4 v[60:63], v1, s[40:43], s67 offen offset:1024 sc1\n\t" \
  "buffer_store_dwordx4 v[64:67], v1, s[40:43], s67 offen offset:2048 sc1\n\t" \
  "buffer_store_dwordx4 v[68:71], v1, s[40:43], s67 offen offset:3072 sc1\n\t" \
  "s_nop 1\n\t" \
  "v_mov_b64 v[56:57], 0\n\t" \
  "v_mov_b64 v[58:59], 0\n\t" \
  "v_mov_b64 v[60:61], 0\n\t" \
  "v_mov_b64 v[62:63], 0\n\t" \
  "v_mov_b64 v[64:65], 0\n\t" \
  "v_mov_b64 v[66:67], 0\n\t" \
  "v_mov_b64 v[68:69], 0\n\t" \
  "v_mov_b64 v[70:71], 0\n\t" \
  "v_mov_b64 v[72:73], 0\n\t" \
  "v_mov_b64 v[74:75], 0\n\t" \
  "v_mov_b64 v[76:77], 0\n\t" \
  "v_mov_b64 v[78:79], 0\n\t" \
  "v_add_u32 v19, s60, v1\n\t" \
  "v_add_u32 v18, s61, v1\n\t" \
  "v_add_u32 v22, s62, v1\n\t" \
  "s_cbranch_execz L_dryret_%=\n\t" \
  "s_waitcnt vmcnt(8)\n\t" \
  "ds_write_b128 v17, v[24:27] offset:0\n\t" \
  "v_smfmac_f32_16x16x64_bf16 v[56:59], v[24:27], v[32:39], v0\n\t" \
  "v_max3_u32 v124, v24, v25, v26\n\t" \
  "s_waitcnt vmcnt(7)\n\t" \
  "s_branch L_go2_%=\n\t" \
  "L_sync_%=:\n\t" \
  "v_lshlrev_b32 v124, 3, v1\n\t" \
  "L_sync2_%=:\n\t" \
  "s_mov_b32 m0, s28\n\t" \
  "v_writelane_b32 v14, s2, m0\n\t" \
  "v_writelane_b32 v15, s1, m0\n\t" \
  "s_nop 1\n\t" \
  "v_cmp_ne_u32 vcc, s1, v15\n\t" \
  "v_mov_b32 v125, v14\n\t" \
  "s_nop 1\n\t" \
  "s_cbranch_vccz L_sync3_%=\n\t" \
  "s_sub_u32 s58, s58, 1\n\t" \
  "s_cbranch_scc1 L_exit_%=\n\t" \
  "s_sleep 2\n\t" \
  "buffer_load_dwordx2 v[14:15], v124, s[40:43], s29 offen sc1\n\t" \
  "s_waitcnt vmcnt(0)\n\t" \
  "s_branch L_sync2_%=\n\t" \
  "L_sync3_%=:\n\t" \
  "v_max_u32_dpp v125, v125, v125 row_ror:1 row_mask:0xf bank_mask:0xf\n\t" \
  "s_nop 1\n\t" \
  "v_max_u32_dpp v125, v125, v125 row_ror:2 row_mask:0xf bank_mask:0xf\n\t" \
  "s_nop 1\n\t" \
  "v_max_u32_dpp v125, v125, v125 row_ror:4 row_mask:0xf bank_mask:0xf\n\t" \
  "s_nop 1\n\t" \
  "v_max_u32_dpp v125, v125, v125 row_ror:8 row_mask:0xf bank_mask:0xf\n\t" \
  "s_nop 0\n\t" \
  "v_readlane_b32 s8, v125, 0\n\t" \
  "v_readlane_b32 s9, v125, 16\n\t" \
  "v_readlane_b32 s10, v125, 32\n\t" \
  "v_readlane_b32 s11, v125, 48\n\t" \
  "s_max_u32 s8, s8, s9\n\t" \
  "s_max_u32 s10, s10, s11\n\t" \
  "s_max_u32 s8, s8, s10\n\t" \
  "s_mul_i32 s8, s8, 6\n\t" \
  "s_add_u32 s98, s98, s8\n\t" \
  "s_add_u32 s98, s98, 600\n\t" \
  "s_sub_u32 s98, s98, s94\n\t" \
  "s_mov_b32 s27, 0xff\n\t" \
  "s_mov_b32 s3, 180\n\t" \
  "s_branch L_red2_%=\n\t" \
  "L_slow1_%=:\n\t" \
  "v_max3_u32 v125, v56, v57, v58\n\t" \
  "v_max3_u32 v125, v125, v59, v60\n\t" \
  "v_max3_u32 v125, v125, v61, v62\n\t" \
  "v_max3_u32 v125, v125, v63, v64\n\t" \
  "v_max3_u32 v125, v125, v65, v66\n\t" \
  "v_max3_u32 v125, v125, v67, v68\n\t" \
  "v_max3_u32 v125, v125, v69, v70\n\t" \
  "v_max_u32 v125, v125, v71\n\t" \
  "v_cmp_eq_u32 vcc, -1, v125\n\t" \
  "s_nop 1\n\t" \
  "s_cbranch_vccz L_ok1_%=\n\t" \
  "s_sub_u32 s58, s58, 1\n\t" \
  "s_cbranch_scc1 L_exit_%=\n\t" \
  "s_add_u32 s98, s98, 372\n\t" \
  "s_mov_b32 s25, 1\n\t" \
  "s_memrealtime s[8:9]\n\t" \
  "s_nop 2\n\t" \
  "s_memrealtime s[10:11]\n\t" \
  "s_nop 2\n\t" \
  "s_memrealtime s[12:13]\n\t" \
  "s_nop 2\n\t" \
  "s_memrealtime s[14:15]\n\t" \
  "s_nop 2\n\t" \
  "s_memrealtime s[16:17]\n\t" \
  "s_nop 2\n\t" \
  "s_memrealtime s[18:19]\n\t" \
  "s_branch L_w1_%=\n\t" \
  "L_late1_%=:\n\t" \
  "s_cmp_lt_i32 s67, 0\n\t" \
  "s_cbranch_scc0 L_far1_%=\n\t" \
  "s_mov_b32 s25, 1\n\t" \
  "s_cmp_lt_i32 s66, -23808\n\t" \
  "s_cbranch_scc1 L_lost1_%=\n\t" \
  "s_add_u32 s98, s98, 372\n\t" \
  "s_add_u32 s67, s67, 372\n\t" \
  "s_add_u32 s66, s66, 372\n\t" \
  "s_branch L_late1_%=\n\t" \
  "L_lost1_%=:\n\t" \
  "s_sub_u32 s98, s98, s66\n\t" \
  "s_mov_b32 s66, 0\n\t" \
  "s_branch L_ontime1_%=\n\t" \
  "L_far1_%=:\n\t" \
  "s_cmp_gt_u32 s67, 254\n\t" \
  "s_cbranch_scc0 L_ontime1_%=\n\t" \
  "s_cmp_gt_u32 s67, 2976\n\t" \
  "s_cselect_b32 s67, s66, 0\n\t" \
  "s_sub_u32 s98, s98, s67\n\t" \
  "s_sub_u32 s58, s58, 1\n\t" \
  "s_cbranch_scc1 L_exit_%=\n\t" \
  "s_sleep 3\n\t" \
  "s_memrealtime s[8:9]\n\t" \
  "s_nop 2\n\t" \
  "s_memrealtime s[10:11]\n\t" \
  "s_nop 2\n\t" \
  "s_memrealtime s[12:13]\n\t" \
  "s_nop 2\n\t" \
  "s_memrealtime s[14:15]\n\t" \
  "s_nop 2\n\t" \
  "s_memrealtime s[16:17]\n\t" \
  "s_nop 2\n\t" \
  "s_memrealtime s[18:19]\n\t" \
  "s_branch L_w1_%=\n\t" \
  "L_late2_%=:\n\t" \
  "s_cmp_lt_i32 s67, 0\n\t" \
  "s_cbranch_scc0 L_far2_%=\n\t" \
  "s_mov_b32 s25, 1\n\t" \
  "s_cmp_lt_i32 s66, -23808\n\t" \
  "s_cbranch_scc1 L_lost2_%=\n\t" \
  "s_add_u32 s98, s98, 372\n\t" \
  "s_add_u32 s67, s67, 372\n\t" \
  "s_add_u32 s66, s66, 372\n\t" \
  "s_branch L_late2_%=\n\t" \
  "L_lost2_%=:\n\t" \
  "s_sub_u32 s98, s98, s66\n\t" \
  "s_mov_b32 s66, 0\n\t" \
  "s_branch L_ontime2_%=\n\t" \
  "L_far2_%=:\n\t" \
  "s_cmp_gt_u32 s67, 254\n\t" \
  "s_cbranch_scc0 L_ontime2_%=\n\t" \
  "s_cmp_gt_u32 s67, 2976\n\t" \
  "s_cselect_b32 s67, s66, 0\n\t" \
  "s_sub_u32 s98, s98, s67\n\t" \
  "s_sub_u32 s58, s58, 1\n\t" \
  "s_cbranch_scc1 L_exit_%=\n\t" \
  "s_sleep 3\n\t" \
  "s_memrealtime s[8:9]\n\t" \
  "s_nop 2\n\t" \
  "s_memrealtime s[10:11]\n\t" \
  "s_nop 2\n\t" \
  "s_memrealtime s[12:13]\n\t" \
  "s_nop 2\n\t" \
  "s_memrealtime s[14:15]\n\t" \
  "s_nop 2\n\t" \
  "s_memrealtime s[16:17]\n\t" \
  "s_nop 2\n\t" \
  "s_memrealtime s[18:19]\n\t" \
  "s_branch L_w2_%=\n\t" \
  "L_late3_%=:\n\t" \
  "s_cmp_lt_i32 s67, 0\n\t" \
  "s_cbranch_scc0 L_far3_%=\n\t" \
  "s_mov_b32 s25, 1\n\t" \
  "s_cmp_lt_i32 s66, -23808\n\t" \
  "s_cbranch_scc1 L_lost3_%=\n\t" \
  "s_add_u32 s76, s76, 372\n\t" \
  "s_add_u32 s67, s67, 372\n\t" \
  "s_add_u32 s66, s66, 372\n\t" \
  "s_branch L_late3_%=\n\t" \
  "L_lost3_%=:\n\t" \
  "s_sub_u32 s76, s76, s66\n\t" \
  "s_mov_b32 s66, 0\n\t" \
  "s_branch L_ontime3_%=\n\t" \
  "L_far3_%=:\n\t" \
  "s_cmp_gt_u32 s67, 254\n\t" \
  "s_cbranch_scc0 L_ontime3_%=\n\t" \
  "s_cmp_gt_u32 s67, 2976\n\t" \
  "s_cselect_b32 s67, s66, 0\n\t" \
  "s_sub_u32 s76, s76, s67\n\t" \
  "s_sub_u32 s58, s58, 1\n\t" \
  "s_cbranch_scc1 L_exit_%=\n\t" \
  "s_sleep 3\n\t" \
  "s_memrealtime s[8:9]\n\t" \
  "s_nop 2\n\t" \
  "s_memrealtime s[10:11]\n\t" \
  "s_nop 2\n\t" \
  "s_memrealtime s[12:13]\n\t" \
  "s_nop 2\n\t" \
  "s_memrealtime s[14:15]\n\t" \
  "s_nop 2\n\t" \
  "s_memrealtime s[16:17]\n\t" \
  "s_nop 2\n\t" \
  "s_memrealtime s[18:19]\n\t" \
  "s_branch L_w3_%=\n\t" \
  "L_final_%=:\n\t" \
  "v_add_f32 v72, v72, v73\n\t" \
  "v_add_f32 v74, v74, v75\n\t" \
  "v_add_f32 v76, v76, v77\n\t" \
  "v_add_f32 v78, v78, v79\n\t" \
  "v_cvt_pk_bf16_f32 v126, v72, v74\n\t" \
  "v_cvt_pk_bf16_f32 v127, v76, v78\n\t" \
  "ds_write_b64 v12, v[126:127] offset:14336\n\t" \
  "s_waitcnt lgkmcnt(0)\n\t" \
  "s_barrier\n\t" \
  "s_cmp_lg_u32 s49, 1\n\t" \
  "s_cbranch_scc1 L_exit_%=\n\t" \
  "s_cmp_lg_u32 s50, 0\n\t" \
  "s_cbranch_scc1 L_exit_%=\n\t" \
  "ds_read_b128 v[56:59], v1 offset:10240\n\t" \
  "ds_read_b128 v[60:63], v1 offset:11264\n\t" \
  "ds_read_b128 v[64:67], v1 offset:14336\n\t" \
  "ds_read_b128 v[68:71], v1 offset:15360\n\t" \
  "s_waitcnt lgkmcnt(0)\n\t" \
  "buffer_store_dwordx4 v[56:59], v1, s[40:43], s52 offen offset:0 sc1\n\t" \
  "buffer_store_dwordx4 v[60:63], v1, s[40:43], s52 offen offset:1024 sc1\n\t" \
  "buffer_store_dwordx4 v[64:67], v1, s[40:43], s52 offen offset:2048 sc1\n\t" \
  "buffer_store_dwordx4 v[68:71], v1, s[40:43], s52 offen offset:3072 sc1\n\t" \
  "v_mov_b32 v126, 64\n\t" \
  "v_mov_b32 v127, s1\n\t" \
  "s_waitcnt vmcnt(0)\n\t" \
  "buffer_store_dwordx2 v[126:127], off, s[40:43], s31 sc1\n\t" \
  "L_exit_%=:\n\t" \
  "s_waitcnt vmcnt(0) lgkmcnt(0)\n\t"
#define CLOB_X "memory", "v40", "v41", "v42", "v43", "v44", "v45", "v46", "v47", "v48", "v49", "v50", "v51", "v52", "v53", "v54", "v55", "v56", "v57", "v58", "v59", "v60", "v61", "v62", "v63"
#define CLOB_RING "memory", "v48", "v49", "v50", "v51", "v52", "v53", "v54", "v55", "v56", "v57", "v58", "v59", "v60", "v61", "v62", "v63", "v64", "v65", "v66", "v67", "v68", "v69", "v70", "v71", "v72", "v73", "v74", "v75", "v76", "v77", "v78", "v79", "v80", "v81", "v82", "v83", "v84", "v85", "v86", "v87", "v88", "v89", "v90", "v91", "v92", "v93", "v94", "v95", "v96", "v97", "v98", "v99", "v100", "v101", "v102", "v103", "v104", "v105", "v106", "v107"
#define CLOB_CHAIN "memory", "vcc", "scc", "v18", "v19", "v20", "v21", "v22", "v23", "v32", "v33", "v34", "v35", "v36", "v37", "v38", "v39", "v40", "v41", "v42", "v43", "v44", "v45", "v46", "v47", "v48", "v49", "v50", "v51", "v52", "v53", "v54", "v55", "v56", "v57", "v58", "v59", "v60", "v61", "v62", "v63", "v64", "v65", "v66", "v67", "v68", "v69", "v70", "v71", "v72", "v73", "v74", "v75", "v76", "v77", "v78", "v79", "v80", "v81", "v82", "v83", "v84", "v85", "v86", "v87", "v88", "v89", "v90", "v91", "v92", "v93", "v94", "v95", "v96", "v97", "v98", "v99", "v100", "v101", "v102", "v103", "v104", "v105", "v106", "v107", "v108", "v109", "v110", "v111", "v112", "v113", "v114", "v115", "v116", "v117", "v118", "v119", "v120", "v121", "v122", "v123", "v124", "v125", "v126", "v127", "s68", "s69", "s72", "s73", "s74", "s75", "s76", "s77", "s78", "s79", "s80", "s81", "s82", "s83", "s84", "s85", "s86", "s87", "s88", "s89", "s90", "s91", "s92", "s93", "s94", "s95", "s96", "s97", "s98", "s99", "s8", "s9", "s10", "s11", "s12", "s13", "s14", "s15", "s16", "s17", "s18", "s19", "s20", "s21", "s22", "s0", "s3", "s4", "s5", "s6", "s7", "s24", "s25", "s26", "s27", "s34", "s35", "s36", "s38", "s39"

#define PIN_K() asm volatile("" : "+{a[128:159]}"(W[4]), "+{a[160:191]}"(W[5]), "+{a[192:223]}"(W[6]), "+{a[224:255]}"(W[7]))
#define PIN_V() asm volatile("" : "+{v[128:159]}"(W[8]), "+{v[160:191]}"(W[9]), "+{v[192:223]}"(W[10]), "+{v[224:255]}"(W[11]))
template <int GI> DEV void read_dense(u32x32* W, u32 lds, int, int w) {
  const int lane = lane_id();
  const u32 g = lane >> 4, q = (lane >> 2) & 3, p = lane & 3;
#pragma unroll
  for (int t = 0; t < 2; ++t)
#pragma unroll
    for (int hf = 0; hf < 2; ++hf) {
      const u32 base = lds + traddr(8 * g + 4 * hf + q, 4 * w + 2 * t + (p >> 1), p);
#pragma unroll
      for (int k = 0; k < 8; ++k) { u32x2 v = trd(base + k * 8192); const int r = (t * 16 + GI * 8 + k) * 4 + 2 * hf; W[r >> 5][r & 31] = v.x; W[r >> 5][(r & 31) + 1] = v.y; }
    }
}
struct Ptrs { const u32* x; const u32* mem0; const u32* wq; const u32* wk; const u32* wv; const u32* wo; const u32* wg; const u32* wu; const u32* wd; u32* out; char* ws; int d1; int d2; u32 tag; };

DEV u32 xbase(const char* ws) { return (u32)((((unsigned long long)ws + 65536 + 2097151) & ~2097151ull) - (unsigned long long)ws); }
#define XM(off, sel) (xb + ((off) >> 13) * 16384u + (sel) * 8192u + ((off) & 8191u))
DEV void chain_role(const Ptrs& P, int h, int side, int lane0, int w) {
  extern __shared__ __attribute__((aligned(16))) char smem[];
  const int rank = 2 * h + side; const u32 xb = xbase(P.ws);
  auto rs = mkrs(P.ws, WS_USED); const u32x4 srd = mksrd(P.ws, WS_USED);
  u32x32 W[12];
  { const int lane = lane0, c = lane & 15, g = lane >> 4;
    u32 vo[4]; stage_offsets(vo, 8192, h * 256, lane);
    u32x16 ta; const u32 q = (lane >> 2) & 3, p = lane & 3;
#pragma unroll
    for (int i = 0; i < 16; ++i) ta[i] = 32768 + traddr(16 * (g >> 1) + 4 * (g & 1) + 2 * ((i >> 2) & 1) + (q & 1) + 8 * (q >> 1), 4 * w + 2 * (i >> 3) + (p >> 1), p) + ((w + (i & 3)) & 3) * 16384;
    asm volatile(CHAIN_LOAD
                 : "={a[0:31]}"(W[0]), "={a[32:63]}"(W[1]), "={a[64:95]}"(W[2]), "={a[96:127]}"(W[3]), "={a[128:159]}"(W[4]), "={a[160:191]}"(W[5]), "={a[192:223]}"(W[6]), "={a[224:255]}"(W[7]),
                   "={v[128:159]}"(W[8]), "={v[160:191]}"(W[9]), "={v[192:223]}"(W[10]), "={v[224:255]}"(W[11]), "+{v[8:23]}"(ta)
                 : "{v[0:3]}"(u32x4{vo[0], vo[1], vo[2], vo[3]}), "{v4}"((h * 128 + (lane >> 3)) * 1024 + side * 512 + w * 128 + (lane & 7) * 16), "{v5}"(32768 + w * 16384 + lane * 16), "{v6}"(32768 + w * 16384 + g * 256 + c * 8),
                   "{s[4:7]}"(mksrd(P.wq, 4u << 20)), "{s[8:11]}"(mksrd(P.wk, 4u << 20)), "{s[12:15]}"(mksrd(P.wv, 4u << 20)), "{s[16:19]}"(mksrd(P.wo, 4u << 20)), "{s20}"(w), "{s21}"(side)
                 : CLOB_LOAD);
  }
  const int lane = lane_id(), c = lane & 15, g = lane >> 4;
  if (w == 0) { const u32 one = (c & 1) ? 0x3f800000u : 0x3f80u; const int d = (c >> 1) & 3;
    *(u32x4*)(smem + 114688 + 1024) = u32x4{0u, 0u, 0u, 0u};
    *(u32x4*)(smem + 114688 + lane * 16) = u32x4{d == 0 ? one : 0u, d == 1 ? one : 0u, d == 2 ? one : 0u, d == 3 ? one : 0u}; }
  TS(1);
  u32 budget = SPIN;
  const int mi = 2 * (c >> 2) + ((c >> 1) & 1), mo = 2 * g + (c & 1);
  u32x8 m0;
#pragma unroll
  for (int b = 0; b < 2; ++b) { const u32x4 v = *(const u32x4*)(P.mem0 + mi * 256 + (8 * (w + 4 * b) + mo) * 4); m0[4 * b] = v.x; m0[4 * b + 1] = v.y; m0[4 * b + 2] = v.z; m0[4 * b + 3] = v.w; }
  asm volatile(DRY2 :: "{s[40:43]}"(srd), "{s49}"(w), "{s50}"(side), "{s22}"(side * 16 + w) : CLOB_DRY2);
  TS(2);
  u32x4 va, vb;
  for (;;) { cbar(); va = ld16(rs, C_VOTE + lane * 128, 0); vb = ld16(rs, C_VOTE + lane * 128 + 16, 0);
    if (__all(va.y == P.tag && va.w == P.tag && vb.y == P.tag && vb.w == P.tag)) break;
    if (budget == 0) END();
    --budget; __builtin_amdgcn_s_sleep(2); }
  cbar();
  const u32 sel = __popcll(__ballot(va.x & 1u)) + __popcll(__ballot(va.z & 1u)) + __popcll(__ballot(vb.x & 1u)) + __popcll(__ballot(vb.z & 1u)) > 128 ? 1u : 0u;
  const u32 now = (u32)__builtin_amdgcn_s_memrealtime(), t0 = now - ((now - (rfl(va.x) >> 1)) & 0x7fffffffu);
  for (;;) { cbar(); const u32x2 x = ld8(rs, C_APROG + (h * 3 + lane % 3) * 128, 0);
    if (__all(x.y == P.tag && (x.x >= 1 || lane % 3 == 2))) break;
    if (budget == 0) END();
    --budget; __builtin_amdgcn_s_sleep(1); }
  cbar();
  TS(3);
  dma_run<4>(srd, lane * 16, OFF_KV + h * 16384 + w * 4096, 16384 + w * 4096);
  const int p = lane & 7, fw = 2 * (c >> 2), fr = 2 * g;
  u32x16 cv;
  cv[0] = (lane & 1) ? 0xEEEEu : 0x4444u;
  cv[1] = lane * 16;
  const u32 cpy = NOREP ? 0 : __builtin_amdgcn_s_getreg(20 | (1 << 11)) & 3;
  cv[2] = XM(X_H2 + cpy * 32768 + (64 * w + lane) * 16, sel);
  cv[3] = XM(X_IN + (32 * side + 8 * w + (c >> 2) * 2 + (g >> 1)) * 4096 + h * 128 + ((c & 3) * 2 + (g & 1)) * 16, sel);
  cv[4] = XM(X_IN + rank * 4096 + lane * 16, sel);
  cv[5] = (!NOREP || (c < 8 && g < 2)) ? XM(X_H2 + ((c >> 3) + 2 * (g >> 1)) * 32768 + rank * 128 + ((2 * (p & 1) + (g & 1)) * 2 + (p >> 2)) * 16 + ((p >> 1) & 1) * 8, sel) : OOB;
  cv[6] = w * 512 + (c >> 2) * 128 + (((2 * g) ^ fw) << 4) + (c & 3) * 4;
  cv[7] = 16384 + 8192 + w * 2048 + lane * 16;
  cv[8] = g * 128 + (((c & 7) ^ fr) << 4);
  cv[9] = g * 128 + (((2 * (c >> 2) + (c & 1)) ^ fr) << 4);
  cv[10] = g * 128 + mi * 16;
  cv[11] = w * 512 + g * 128 + (c & 7) * 16 + (c >> 3) * 8;
  cv[12] = w * 512 + lane * 8;
  cv[13] = 16384 + lane * 16;
  cv[14] = 0; cv[15] = 0;
  u32x16 cs; u32x8 ct;
  cs[0] = 0; cs[1] = 0; cs[2] = 0; cs[3] = 0; cs[4] = 4 * 64 * 4096; cs[5] = w; cs[6] = side;
  cs[7] = OFF_KV + (32 + h) * 16384 + w * 2048; cs[8] = OFF_KM + h * 4096; cs[9] = C_APROG + (h * 3 + 2) * 128; cs[10] = 1; cs[11] = 2;
  cs[12] = P.d1; cs[13] = P.d2; cs[14] = rfl(budget); cs[15] = __builtin_bit_cast(u32, SC2);
  ct[0] = ((w + 1) & 3) * 1024; ct[1] = ((w + 2) & 3) * 1024; ct[2] = ((w + 3) & 3) * 1024; ct[3] = C_APROG + (h * 3 + 1) * 128; ct[4] = 1; ct[5] = C_APROG + h * 3 * 128; ct[6] = 0; ct[7] = rfl(t0);
  bar_vm<0>();
  const u32 rdy = rfl((u32)__builtin_amdgcn_s_memrealtime() - t0 + 1);
  st16(rs, u32x4{rdy, P.tag, 0u, 0u}, (w == 0 && lane == 0) ? C_READY + rank * 128 : OOB, 0);
  TS(4);
  asm volatile(CHAIN_ASM
               : "+{v[0:15]}"(cv), "+{v[24:31]}"(m0), "+{s[44:59]}"(cs), "+{s[60:67]}"(ct)
               : "{v16}"(0u), "{v17}"((64 * w + lane) * 16), "{s[40:43]}"(srd), "{s71}"(OFF_DBG + (rank * 4 + w) * DBGS), "{s70}"(98304 + w * 2048), "{s23}"(rfl(w == 0 ? XM(X_PR + 512 + cpy * 128, sel) : OOB)), "{s30}"(rfl(rank == 0 ? XM(X_PR + 512, sel) : OOB)), "{s28}"(rank), "{s2}"(rdy), "{s1}"(P.tag), "{s37}"(rfl(w == 0 ? C_READY + rank * 128 + 8 : OOB)), "{s31}"(C_CPROG + h * 128), "{s29}"(C_READY),
                 "{a[0:31]}"(W[0]), "{a[32:63]}"(W[1]), "{a[64:95]}"(W[2]), "{a[96:127]}"(W[3]),
                 "{a[128:159]}"(W[4]), "{a[160:191]}"(W[5]), "{a[192:223]}"(W[6]), "{a[224:255]}"(W[7]),
                 "{v[128:159]}"(W[8]), "{v[160:191]}"(W[9]), "{v[192:223]}"(W[10]), "{v[224:255]}"(W[11])
               : CLOB_CHAIN);
}

template <int NM> DEV void gemm_role(const Ptrs& P, int idx, int split, int lane, int w, u32& budget) {
  constexpr int NS = NM == 3 ? 3 : 2;
  auto rs = mkrs(P.ws, WS_USED); const u32x4 srd = mksrd(P.ws, WS_USED);
  const u32 pitch = NM == 3 ? 8192 : 2752, bytes = 512 * pitch;
  const u32x4 s0 = mksrd(NM == 3 ? P.wq : P.wg, bytes), s1 = mksrd(NM == 3 ? P.wk : P.wu, bytes), s2 = mksrd(P.wv, bytes);
  u32 vo[4]; stage_offsets(vo, pitch, idx * 256, lane);
  const u32 G0 = 0, G1 = 65536;
  u32x32 W[12];
  const u32 prog = (lane == 0 && w == 1) ? C_APROG + (NM == 3 ? idx * 3 + split : 96 + idx * 2 + split) * 128 : OOB;
  st8(rs, 0u, P.tag, prog);
  auto xdma = [&](int seg, u32 dst) { dma_run<8>(srd, lane * 16, rfl(OFF_XN + seg * 32768 + w * 8192), rfl(dst + w * 8192)); };
  if constexpr (NM == 3) {
    u32x4 fl;
#define PEEK_X() asm volatile("s_nop 4\n\tbuffer_load_dwordx4 %0, off, %1, %2 sc1" : "=v"(fl) : "s"(srd), "s"(C_LNF + split * 128) : "memory")
#define FIRST_X() asm volatile("" : "+v"(fl)); if (!__all(fl.y == P.tag && fl.w == P.tag)) { if (!wait_ln(rs, split, P.tag, budget)) END(); } xdma(split, 131072)
    if (split == 0) {
      bar_vm<16>(); read_dense<0>(W + 4, G0, lane, w); bar_lds(); PEEK_X(); stage(s2, vo, pitch, 0, G0, w);
      bar_vm<16>(); read_dense<1>(W + 4, G1, lane, w); bar_lds(); PIN_K(); FIRST_X(); stage(s2, vo, pitch, 256, G1, w); TS(1);
      bar_vm<0>(); read_dense<0>(W + 8, G0, lane, w); read_dense<1>(W + 8, G1, lane, w); PIN_V();
    } else if (split == 1) {
      bar_vm<16>(); read_dense<0>(W + 8, G0, lane, w); bar_lds(); PEEK_X(); stage(s1, vo, pitch, 0, G0, w);
      bar_vm<16>(); read_dense<1>(W + 8, G1, lane, w); bar_lds(); PIN_V(); FIRST_X(); stage(s1, vo, pitch, 256, G1, w); TS(1);
      bar_vm<0>(); read_dense<0>(W + 4, G0, lane, w); read_dense<1>(W + 4, G1, lane, w); PIN_K();
    } else {
      bar_vm<16>(); read_dense<1>(W + 4, G0, lane, w); bar_lds(); PEEK_X(); stage(s1, vo, pitch, 0, G0, w);
      bar_vm<16>(); read_dense<1>(W + 8, G1, lane, w); bar_lds(); FIRST_X(); stage(s2, vo, pitch, 0, G1, w); TS(1);
      bar_vm<0>(); read_dense<0>(W + 4, G0, lane, w); read_dense<0>(W + 8, G1, lane, w); PIN_K(); PIN_V();
    }
    bar_lds(); TS(2);
  } else {
    stage(s0, vo, pitch, 0, G0, w); stage(s0, vo, pitch, 256, G1, w);
    bar_vm<16>(); read_dense<0>(W, G0, lane, w); bar_lds(); stage(s1, vo, pitch, 0, G0, w);
    bar_vm<16>(); read_dense<1>(W, G1, lane, w); bar_lds(); stage(s1, vo, pitch, 256, G1, w);
    if (!wait_ln(rs, lane, P.tag, budget)) END();
    xdma(split, 131072);
    bar_vm<8>(); read_dense<0>(W + 4, G0, lane, w); read_dense<1>(W + 4, G1, lane, w);
    bar_vm<0>();
  }
  u32 n = 0;
  auto st_kv = [&](const f32x16& k, const f32x16& v, int s) { const u32 skv = OFF_KV + (s * 32 + idx) * 16384;
#pragma unroll
    for (int T = 0; T < 2; ++T) {
      st16(rs, u32x4{pk(k[4 * T], k[8 + 4 * T]), pk(k[4 * T + 1], k[9 + 4 * T]), pk(k[4 * T + 2], k[10 + 4 * T]), pk(k[4 * T + 3], k[11 + 4 * T])}, (T * 4 + w) * 1024 + lane * 16, skv);
      st16(rs, u32x4{pk(v[8 * T], v[8 * T + 1]), pk(v[8 * T + 2], v[8 * T + 3]), pk(v[8 * T + 4], v[8 * T + 5]), pk(v[8 * T + 6], v[8 * T + 7])}, 8192 + (w * 2 + T) * 1024 + lane * 16, skv); } };
  auto st_q = [&](const f32x16& q, int s) {
#pragma unroll
    for (int T = 0; T < 2; ++T)
      st16(rs, u32x4{pk(q[4 * T], q[8 + 4 * T]), pk(q[4 * T + 1], q[9 + 4 * T]), pk(q[4 * T + 2], q[10 + 4 * T]), pk(q[4 * T + 3], q[11 + 4 * T])}, (T * 4 + w) * 1024 + lane * 16, OFF_Q + (s * 32 + idx) * 8192); };
  int sfirst = split;
  if constexpr (NM == 3) {
    f32x16 q, k, v;
#define KV_ONLY(b) asm volatile(A_ASMKV : "={v[80:95]}"(k), "={v[96:111]}"(v) \
                 : "{v39}"((b) + lane * 16), "{a[128:159]}"(W[4]), "{a[160:191]}"(W[5]), "{a[192:223]}"(W[6]), "{a[224:255]}"(W[7]), \
                   "{v[128:159]}"(W[8]), "{v[160:191]}"(W[9]), "{v[192:223]}"(W[10]), "{v[224:255]}"(W[11]) : CLOB_X)
#define Q_ONLY(b) asm volatile(A_ASMQ : "={v[64:79]}"(q) : "{v39}"((b) + lane * 16), "{a[0:31]}"(W[0]), "{a[32:63]}"(W[1]), "{a[64:95]}"(W[2]), "{a[96:127]}"(W[3]) : CLOB_X)
    KV_ONLY(131072); st_kv(k, v, split);
    bar_vm<0>(); st8(rs, 1u, P.tag, prog); TS(3);
    if (!wait_ln(rs, split + NS, P.tag, budget)) END();
    xdma(split + NS, 98304);
    stage(s0, vo, pitch, 0, G0, w);
    bar_vm<16>();
    KV_ONLY(98304); st_kv(k, v, split + NS);
    bar_vm<0>(); st8(rs, 2u, P.tag, prog); TS(4);
    read_dense<0>(W, G0, lane, w); bar_lds(); stage(s0, vo, pitch, 256, G0, w);
    if (!wait_ln(rs, lane, P.tag, budget)) END();
    bar_vm<0>(); read_dense<1>(W, G0, lane, w); bar_lds();
    xdma(split + 2 * NS, 0);
    Q_ONLY(131072); st_q(q, split); Q_ONLY(98304); st_q(q, split + NS);
    bar_vm<4>();
    sfirst += 2 * NS;
  }
  if constexpr (NM == 3) {
    xdma(sfirst + NS, 131072);
    f32x16 q, k, v;
    asm volatile(A_ASM3 : "={v[64:79]}"(q), "={v[80:95]}"(k), "={v[96:111]}"(v)
                 : "{v39}"(lane * 16), "{a[0:31]}"(W[0]), "{a[32:63]}"(W[1]), "{a[64:95]}"(W[2]), "{a[96:127]}"(W[3]),
                   "{a[128:159]}"(W[4]), "{a[160:191]}"(W[5]), "{a[192:223]}"(W[6]), "{a[224:255]}"(W[7]),
                   "{v[128:159]}"(W[8]), "{v[160:191]}"(W[9]), "{v[192:223]}"(W[10]), "{v[224:255]}"(W[11]) : CLOB_X);
    st_kv(k, v, sfirst); st_q(q, sfirst);
    bar_vm<0>(); st8(rs, 3u, P.tag, prog);
    sfirst += NS; n = 3;
  }
  { const int ln = lane_id();
    u32x16 sc; u32x4 so;
    sc[0] = OFF_XN + (sfirst + NS) * 32768 + w * 8192; sc[1] = w * 8192; sc[2] = OOB / 2; sc[3] = OOB / 2; sc[4] = OOB / 2; sc[5] = OOB / 2;
    sc[6] = w == 1 ? C_APROG + (NM == 3 ? idx * 3 + split : 96 + idx * 2 + split) * 128 : OOB; sc[7] = n; sc[8] = (63 - sfirst) / NS + 1;
    sc[9] = 0; sc[10] = 0; sc[11] = 0; sc[12] = 0; sc[13] = 0;
    if constexpr (NM == 3) { sc[14] = OFF_KV + (sfirst * 32 + idx) * 16384; sc[15] = sc[14] + 4096; so[0] = OFF_Q + (sfirst * 32 + idx) * 8192; so[1] = so[0] + 4096; }
    else { sc[14] = OFF_H + sfirst * 88064; sc[15] = sc[14] + 44032; so[0] = 0; so[1] = 0; }
    so[2] = 0; so[3] = 0;
    u32 va = 131072 + ln * 16;
    if constexpr (NM == 3)
      asm volatile(A_LOOP3 : "+{s[44:59]}"(sc), "+{s[60:63]}"(so), "+{v39}"(va)
                   : "{v38}"(ln * 16), "{v35}"(P.tag), "{v37}"(w * 1024 + ln * 16), "{v36}"(8192 + w * 2048 + ln * 16), "{s[40:43]}"(srd),
                     "{a[0:31]}"(W[0]), "{a[32:63]}"(W[1]), "{a[64:95]}"(W[2]), "{a[96:127]}"(W[3]), "{a[128:159]}"(W[4]), "{a[160:191]}"(W[5]), "{a[192:223]}"(W[6]), "{a[224:255]}"(W[7]),
                     "{v[128:159]}"(W[8]), "{v[160:191]}"(W[9]), "{v[192:223]}"(W[10]), "{v[224:255]}"(W[11]) : CLOB_AL);
    else
      asm volatile(A_LOOP2 : "+{s[44:59]}"(sc), "+{s[60:63]}"(so), "+{v39}"(va)
                   : "{v38}"(ln * 16), "{v35}"(P.tag), "{v37}"(idx * 4 + w < 43 ? (idx * 4 + w) * 1024 + ln * 16 : OOB), "{s[40:43]}"(srd),
                     "{a[0:31]}"(W[0]), "{a[32:63]}"(W[1]), "{a[64:95]}"(W[2]), "{a[96:127]}"(W[3]), "{a[128:159]}"(W[4]), "{a[160:191]}"(W[5]), "{a[192:223]}"(W[6]), "{a[224:255]}"(W[7]) : CLOB_AL);
  }
  TS(5);
}

DEV void att_role(const Ptrs& P, int lane, u32& budget) {
  auto rs = mkrs(P.ws, WS_USED);
  const int c = lane & 15, g = lane >> 4;
  const float NEG = -3.0e38f;
  auto ticket = [&]() { u32 t = 0; if (lane == 0) t = __hip_atomic_fetch_add((u32*)(P.ws + C_TICKET), 1u, __ATOMIC_RELAXED, __HIP_MEMORY_SCOPE_AGENT); return t; };
  if (!wait_ln(rs, 0, P.tag, budget)) END();
  for (u32 tk = ticket();; tk = ticket()) {
    const int un = rfl(tk), s = un >> 5, h = un & 31;
    if (un >= 2048) break;
    if (!wait_ge(rs, C_APROG + (h * 3 + s % 3) * 128, true, max(s / 3 + 1, 3), P.tag, budget, 2)) END();
    const u32 sq = OFF_Q + (s * 32 + h) * 8192, skv = OFF_KV + (s * 32 + h) * 16384;
    u32x4 Q[8], K[8], Vf[8];
#pragma unroll
    for (int i = 0; i < 8; ++i) { Q[i] = ld16(rs, i * 1024 + lane * 16, sq); K[i] = ld16(rs, i * 1024 + lane * 16, skv); }
#pragma unroll
    for (int i = 0; i < 8; ++i) Vf[i] = ld16(rs, 8192 + i * 1024 + lane * 16, skv);
    f32x4 s00, s01, s11;
    MF0(s00, K[0], Q[0]); MF0(s01, K[0], Q[4]); MF0(s11, K[4], Q[4]);
#pragma unroll
    for (int ks = 1; ks < 4; ++ks) { MFA(s00, K[ks], Q[ks]); MFA(s01, K[ks], Q[4 + ks]); MFA(s11, K[4 + ks], Q[4 + ks]); }
    asm volatile("s_nop 7" : "+v"(s00), "+v"(s01), "+v"(s11));
    float mx[2], lx[2]; u32x4 Px[2];
    { float a[4], b[4], d[4];
#pragma unroll
      for (int r = 0; r < 4; ++r) { const bool ok = 4 * g + r <= c; a[r] = ok ? s00[r] : NEG; b[r] = s01[r]; d[r] = ok ? s11[r] : NEG; }
      mx[0] = x32(x16(fmaxf(fmaxf(a[0], a[1]), fmaxf(a[2], a[3])), true), true);
      mx[1] = x32(x16(fmaxf(fmaxf(fmaxf(b[0], b[1]), fmaxf(b[2], b[3])), fmaxf(fmaxf(d[0], d[1]), fmaxf(d[2], d[3]))), true), true);
#pragma unroll
      for (int r = 0; r < 4; ++r) { const bool ok = 4 * g + r <= c;
        a[r] = ok ? __builtin_amdgcn_exp2f((a[r] - mx[0]) * SC2) : 0.f; b[r] = __builtin_amdgcn_exp2f((b[r] - mx[1]) * SC2); d[r] = ok ? __builtin_amdgcn_exp2f((d[r] - mx[1]) * SC2) : 0.f; }
      lx[0] = (a[0] + a[1]) + (a[2] + a[3]); lx[1] = (b[0] + b[1]) + (b[2] + b[3]) + (d[0] + d[1]) + (d[2] + d[3]);
      Px[0] = u32x4{pk(a[0], a[1]), pk(a[2], a[3]), 0u, 0u}; Px[1] = u32x4{pk(b[0], b[1]), pk(b[2], b[3]), pk(d[0], d[1]), pk(d[2], d[3])}; }
    asm volatile("s_nop 1" : "+v"(Px[0]), "+v"(Px[1]));
    f32x4 o[16];
#pragma unroll
    for (int i = 0; i < 8; ++i) { MF0(o[2 * i], Vf[i], Px[0]); MF0(o[2 * i + 1], Vf[i], Px[1]); }
    if (!wait_ge(rs, C_CPROG + h * 128, true, s + 1, P.tag, budget, 3)) END();
    const u32 sm = OFF_KM + (s * 32 + h) * 4096;
    u32x4 km[4]; u32x2 vm[4];
#pragma unroll
    for (int i = 0; i < 4; ++i) km[i] = ld16(rs, i * 512 + g * 128 + (((2 * (c >> 2) + (c & 1)) ^ (2 * g)) << 4), sm);
#pragma unroll
    for (int i = 0; i < 4; ++i) vm[i] = ld8(rs, 2048 + i * 512 + lane * 8, sm);
    TW(0);
    f32x4 m[2];
    MF0(m[0], km[0], Q[0]); MF0(m[1], km[0], Q[4]);
#pragma unroll
    for (int ks = 1; ks < 4; ++ks) { MFA(m[0], km[ks], Q[ks]); MFA(m[1], km[ks], Q[4 + ks]); }
    asm volatile("s_nop 7" : "+v"(m[0]), "+v"(m[1]));
    float sc[2]; u32x2 Pm[2];
#pragma unroll
    for (int T = 0; T < 2; ++T) {
      const float M = x32(x16(fmaxf(fmaxf(m[T][0], m[T][1]), mx[T]), true), true);
      const float al = __builtin_amdgcn_exp2f((mx[T] - M) * SC2);
      const float e0 = __builtin_amdgcn_exp2f((m[T][0] - M) * SC2), e1 = __builtin_amdgcn_exp2f((m[T][1] - M) * SC2);
      const float rt = __builtin_amdgcn_rcpf(x32(x16(al * lx[T] + (e0 + e1), false), false));
      sc[T] = al * rt; Pm[T] = u32x2{pk(e0 * rt, e1 * rt), 0u};
    }
    u32x2 va[8];
#pragma unroll
    for (int i = 0; i < 8; ++i) { va[i] = u32x2{(i & 1) ? vm[i >> 1].y : vm[i >> 1].x, 0u}; o[2 * i] *= sc[0]; o[2 * i + 1] *= sc[1]; }
#pragma unroll
    for (int i = 0; i < 8; ++i) { MH1(o[2 * i], va[i], Pm[0]); MH1(o[2 * i + 1], va[i], Pm[1]); }
    asm volatile("s_nop 7" : "+v"(o[0]), "+v"(o[1]), "+v"(o[2]), "+v"(o[3]), "+v"(o[4]), "+v"(o[5]), "+v"(o[6]), "+v"(o[7]), "+v"(o[8]), "+v"(o[9]), "+v"(o[10]), "+v"(o[11]), "+v"(o[12]), "+v"(o[13]), "+v"(o[14]), "+v"(o[15]));
#pragma unroll
    for (int i = 0; i < 8; ++i) { const int T = i >> 2, ks = i & 3; const f32x4 o0 = o[4 * ks + T], o1 = o[4 * ks + 2 + T];
      st16(rs, u32x4{pk(o0[0], o1[0]), pk(o0[1], o1[1]), pk(o0[2], o1[2]), pk(o0[3], o1[3])}, ks * 1024 + lane * 16, OFF_O + ((s * 2 + T) * 32 + h) * 4096); }
    TW(1);
    asm volatile("s_waitcnt vmcnt(0)" ::: "memory");
    TW(2);
    TS(8 + (s >> 3));
    st8(rs, 1u, P.tag, lane == 0 ? C_OFLAG + un * 128 : OOB);
  }
}

DEV void ln_role(const Ptrs& P, int i0, int w) {
  extern __shared__ __attribute__((aligned(16))) char smem[];
  auto rs = mkrs(P.ws, WS_USED);
  bar_vm<12>();
  TS(24);
  for (int ph = 0; ph < 4; ++ph) {
    const int lane = lane_id(), c = lane & 15, g = lane >> 4, tile = i0 + 32 * ph;
    const char* row = smem + ph * 16384 + c * 1024;
    u32x4 v[4]; f32x4 sm = {0, 0, 0, 0}; const u32x4 one = {0x3f803f80u, 0x3f803f80u, 0x3f803f80u, 0x3f803f80u};
#pragma unroll
    for (int i = 0; i < 4; ++i) { v[i] = *(const u32x4*)(row + (((w * 4 + i) * 4 + g) ^ ((c & 3) << 2)) * 16); sm = mf32(one, v[i], sm); }
    float* red = (float*)(smem + 65536 + ph * 1024);
    red[c * 4 + w] = sm[0]; bar_lds();
    const f32x4 ps = *(const f32x4*)(red + c * 4);
    const float mean = ((ps[0] + ps[1]) + (ps[2] + ps[3])) * (1.f / 512.f), mb = rbf(mean);
    f32x2 f[16], sq = {0.f, 0.f}, s2 = {0.f, 0.f};
#pragma unroll
    for (int i = 0; i < 16; ++i) { const u32 d = v[i >> 2][i & 3]; f[i] = f32x2{blo(d), bhi(d)}; const f32x2 e = f[i] - mean; if (i & 1) s2 += e * e; else sq += e * e; }
    red[64 + c * 4 + w] = x32(x16((sq.x + sq.y) + (s2.x + s2.y), false), false); bar_lds();
    const f32x4 pq = *(const f32x4*)(red + 64 + c * 4);
    const float var = rbf(((pq[0] + pq[1]) + (pq[2] + pq[3])) * (1.f / 512.f));
    const float rsd = __builtin_amdgcn_rcpf(rbf(__builtin_amdgcn_sqrtf(rbf(var + 1e-5f))));
#pragma unroll
    for (int i = 0; i < 4; ++i) { u32x4 o;
#pragma unroll
      for (int j = 0; j < 4; ++j) { const f32x2 e = f[4 * i + j] - mb; const u32 df = pk(e.x, e.y); const f32x2 q = f32x2{blo(df), bhi(df)} * rsd; o[j] = pk(q.x, q.y); }
      st16(rs, o, (w * 4 + i) * 1024 + lane * 16, OFF_XN + tile * 16384); }
    st4(rs, 0u, (tile == 0 && w == 0 && lane == 0) ? C_TICKET : OOB);
    bar_vm<0>();
    st8(rs, 1u, P.tag, (w == 0 && lane == 0) ? C_LNF + (tile >> 1) * 128 + (tile & 1) * 8 : OOB);
    if (ph == 0) TS(25);
  }
  TS(26);
}
DEV void helper_role(const Ptrs& P, int r, int lane, int w) {
  auto rs = mkrs(P.ws, WS_USED);
  const u32 xb = xbase(P.ws), u = w & 1; const u32x4 srd = mksrd(P.ws, WS_USED);
  u32 vo[4];
#pragma unroll
  for (int i = 0; i < 4; ++i) vo[i] = (2 * i + (lane >> 5)) * 1024 + (r & 1) * 512 + (lane & 31) * 16;
  stage(mksrd(P.wo, 4u << 20), vo, 512, (r >> 1) * 256, 32768, w);
  { const u32x4 S = {~0u, ~0u, ~0u, ~0u}; const u32 sl = lane >> 3;
#pragma unroll
    for (int i = 0; i < 6; ++i) st16(rs, S, XM(X_IN + (i >> 1) * 64 * 4096 + r * 4096 + w * 1024 + lane * 16, i & 1), 0);
    st16(rs, S, XM(X_H2 + (w * 4 + (sl >> 1)) * 8192 + r * 128 + (lane & 7) * 16, sl & 1), 0);
    if (w == 0) st16(rs, S, XM(X_PR + r * 1024 + 512 + (lane & 31) * 16, lane >> 5), 0); }
  const u32 pa = lane == 0 ? XM(X_PR + r * 1024 + w * 128, u) : OOB, pb = pa ^ 8192u;
  unsigned long long t0, t1, t2; u32 z = 0;
#define PRB(a, t) "buffer_atomic_or %3, " a ", %6, 0 offen sc0\n\ts_waitcnt vmcnt(0)\n\ts_memtime " t "\n\ts_waitcnt lgkmcnt(0)\n\tv_mov_b32 %3, 0\n\t"
  asm volatile("s_waitcnt vmcnt(0)\n\ts_memtime %0\n\ts_waitcnt lgkmcnt(0)\n\t" PRB("%4", "%1") PRB("%5", "%2")
               : "=&s"(t0), "=&s"(t1), "=&s"(t2), "+v"(z) : "v"(pa), "v"(pb), "s"(srd) : "memory");
  const u32 da = (u32)(t1 - t0), db = (u32)(t2 - t1);
  st8(rs, ((u32)__builtin_amdgcn_s_memrealtime() << 1) | ((u ? db > da : da > db) ? 1u : 0u), P.tag, lane == 0 ? C_VOTE + r * 128 + w * 8 : OOB);
  TS(1);
  if (r == 0 && w == 0) {
    u32 budget = SPIN;
#pragma unroll
    for (int i = 0; i < 4; ++i) if (!wait_ge(rs, C_ARRIVE + (i * 64 + lane) * 8, true, 1, P.tag, budget, 2)) END();
    st4(rs, P.tag, lane == 0 ? C_EPOCH : OOB);
  }
}

DEV void out_role(const Ptrs& P, int set, int sl, int lane, int w, u32& budget) {
  extern __shared__ __attribute__((aligned(16))) char smem[];
  auto rs = mkrs(P.ws, WS_USED); const u32x4 srd = mksrd(P.ws, WS_USED);
  const int c = lane & 15, g = lane >> 4;
  u32x32 W[8], D[3];
  { const u32 vo = (w * 1024 + 4 * g) * 1024 + sl * 64 + c * 4, vd = (w * 352 + 4 * g) * 1024 + sl * 64 + c * 4;
    asm volatile(OUT_LOAD
                 : "={a[0:31]}"(W[0]), "={a[32:63]}"(W[1]), "={a[64:95]}"(W[2]), "={a[96:127]}"(W[3]), "={a[128:159]}"(W[4]), "={a[160:191]}"(W[5]), "={a[192:223]}"(W[6]), "={a[224:255]}"(W[7]),
                   "={v[160:191]}"(D[0]), "={v[192:223]}"(D[1]), "={v[224:255]}"(D[2])
                 : "{v[0:3]}"(u32x4{vo, vo + 16384, vd, vd + 16384}), "{s[4:7]}"(mksrd(P.wo, 4u << 20)), "{s[8:11]}"(mksrd(P.wd, 1376 * 1024)), "{s12}"((sl & 7) * 131072) : CLOB_OL, "scc"); }
  TS(6);
  const u32 ring = 36864 + w * 24576;
  u32 rb = 0, hok = 0, pre = 0; unsigned long long omask = 0;
  const int T = set & 1, s0 = set >> 1;
  for (int n = 0; s0 + 3 * n < 64; ++n, rb ^= 16384) {
    const int s = s0 + 3 * n, lane = lane_id(), c = lane & 15, g = lane >> 4;
    while (hok <= (u32)s / 2) {
      cbar();
      const u32x2 x = ld8(rs, C_APROG + (96 + (lane & 31)) * 128, 0); u32 v = lane >= 22 ? ~0u : x.y == P.tag ? x.x : 0u;
#pragma unroll
      for (int m = 1; m < 32; m <<= 1) v = min(v, (u32)__shfl_xor((int)v, m));
      hok = rfl(v);
      if (hok <= (u32)s / 2) { if (budget == 0) END(); --budget; __builtin_amdgcn_s_sleep(8); }
    }
    if (!pre && ((omask >> n) & 3) != 3) {
      cbar(); const int sk = s + 3 * (lane >> 3);
      unsigned long long m = __ballot(ld8(rs, sk < 64 ? C_OFLAG + (sk * 32 + w * 8 + (lane & 7)) * 128 : OOB, 0).y == P.tag && sk < 64);
      m &= m >> 4; m &= m >> 2; m &= m >> 1; m &= 0x0101010101010101ull;
      omask |= ((m * 0x0102040810204080ull) >> 56) << n; }
    cbar();
    f32x8 y, a;
    const u32 hv = w * 11264 + lane * 16;
    u32x4 xv;
    TU(0); TU(3); TU(4);
    if ((omask >> n) & 1) {
      const u32 nx = (s + 3 < 64 && hok > (u32)(s + 3) / 2 && ((omask >> (n + 1)) & 1)) ? 2u : 0u;
      asm volatile(OUT_M_ASM : "={v[32:39]}"(y), "={v[40:47]}"(a), "={v[20:23]}"(xv)
                   : "{v31}"(hv), "{v30}"(hv + 4096), "{v29}"(hv + 8192), "{v28}"(w < 3 ? hv + 8192 : OOB), "{v27}"(w * 32768 + lane * 16), "{v26}"(c * 1024 + sl * 64 + g * 16), "{v25}"(ring + lane * 16), "{s53}"(ring), "{s56}"((sl & 7) * 4096), "{s[40:43]}"(srd),
                     "{s[44:47]}"(u32x4{OFF_O + s * 262144 + T * 131072, OFF_H + s * 88064 + T * 44032, 0u, (u32)(s * 32768 + T * 16384)}), "{s48}"(rfl(pre | nx)), "{s[8:11]}"(mksrd(P.x, 2u << 20)),
                     "{v[160:191]}"(D[0]), "{v[192:223]}"(D[1]), "{v[224:255]}"(D[2]),
                     "{a[0:31]}"(W[0]), "{a[32:63]}"(W[1]), "{a[64:95]}"(W[2]), "{a[96:127]}"(W[3]),
                     "{a[128:159]}"(W[4]), "{a[160:191]}"(W[5]), "{a[192:223]}"(W[6]), "{a[224:255]}"(W[7]) : CLOB_RING, "s49", "s50", "s51", "s52", "s54", "scc");
      pre = nx >> 1;
    } else {
      xv = *(const u32x4*)(P.x + (size_t)(s * 32 + T * 16 + c) * 256 + sl * 16 + g * 4);
      asm volatile(OUT_H_ASM : "={v[32:39]}"(y)
                   : "{v31}"(hv), "{v30}"(hv + 4096), "{v29}"(hv + 8192), "{v28}"(w < 3 ? hv + 8192 : OOB), "{s[40:43]}"(srd), "{s44}"(OFF_H + s * 88064 + T * 44032),
                     "{v[160:191]}"(D[0]), "{v[192:223]}"(D[1]), "{v[224:255]}"(D[2]) : CLOB_RING);
      TO(0);
      if (!wait_ge(rs, C_OFLAG + (s * 32 + w * 8 + (lane & 7)) * 128, true, 1, P.tag, budget, 1)) END();
      TO(1);
      asm volatile(OUT_O_ASM : "={v[40:47]}"(a)
                   : "{v27}"(w * 32768 + lane_id() * 16), "{v25}"(ring + lane_id() * 16), "{s53}"(ring), "{s56}"((sl & 7) * 4096), "{s[40:43]}"(srd), "{s44}"(OFF_O + s * 262144 + T * 131072),
                     "{a[0:31]}"(W[0]), "{a[32:63]}"(W[1]), "{a[64:95]}"(W[2]), "{a[96:127]}"(W[3]),
                     "{a[128:159]}"(W[4]), "{a[160:191]}"(W[5]), "{a[192:223]}"(W[6]), "{a[224:255]}"(W[7]) : CLOB_RING, "s52", "s54", "scc");
      asm volatile("" : "+v"(xv));
    }
    TO(2); TU(1);
    f32x4* red = (f32x4*)(smem + rb + w * 4096 + lane * 16);
#pragma unroll
    for (int r = 0; r < 4; ++r) red[r * 64] = f32x4{a[r], a[4 + r], y[r], y[4 + r]};
    bar_lds();
    TU(2);
    {
      const f32x4* rd = (const f32x4*)(smem + rb + w * 1024 + lane * 16);
      const f32x4 v = (rd[0] + rd[256]) + (rd[512] + rd[768]);
      const u32 sb = pk(v[0], v[1]), yb = pk(v[2], v[3]);
      const float y0 = blo(yb), y1 = bhi(yb);
      const u32 gb = pk(y0 * __builtin_amdgcn_rcpf(1.f + __expf(-y0)), y1 * __builtin_amdgcn_rcpf(1.f + __expf(-y1)));
      const u32 pb = pk(blo(sb) * blo(gb), bhi(sb) * bhi(gb)), xd = w == 0 ? xv.x : w == 1 ? xv.y : w == 2 ? xv.z : xv.w;
      P.out[(size_t)(s * 32 + T * 16 + c) * 256 + sl * 16 + g * 4 + w] = pk(blo(xd) + blo(pb), bhi(xd) + bhi(pb));
    }
  }
}

#define X_STAGE "s_mov_b32 %0, m0\n\ts_mov_b32 m0, %8\n\ts_mov_b32 %1, %7\n\ts_nop 1\n\tbuffer_load_dwordx4 %2, %6, %1 offen lds\n\ts_add_u32 m0, m0, 1024\n\ts_nop 0\n\tbuffer_load_dwordx4 %3, %6, %1 offen lds\n\ts_add_u32 m0, m0, 1024\n\ts_nop 0\n\tbuffer_load_dwordx4 %4, %6, %1 offen lds\n\ts_add_u32 m0, m0, 1024\n\ts_nop 0\n\tbuffer_load_dwordx4 %5, %6, %1 offen lds\n\ts_add_u32 m0, m0, 13312\n\ts_add_u32 %1, %1, 524288\n\tbuffer_load_dwordx4 %2, %6, %1 offen lds\n\ts_add_u32 m0, m0, 1024\n\ts_nop 0\n\tbuffer_load_dwordx4 %3, %6, %1 offen lds\n\ts_add_u32 m0, m0, 1024\n\ts_nop 0\n\tbuffer_load_dwordx4 %4, %6, %1 offen lds\n\ts_add_u32 m0, m0, 1024\n\ts_nop 0\n\tbuffer_load_dwordx4 %5, %6, %1 offen lds\n\ts_add_u32 m0, m0, 13312\n\ts_add_u32 %1, %1, 524288\n\tbuffer_load_dwordx4 %2, %6, %1 offen lds\n\ts_add_u32 m0, m0, 1024\n\ts_nop 0\n\tbuffer_load_dwordx4 %3, %6, %1 offen lds\n\ts_add_u32 m0, m0, 1024\n\ts_nop 0\n\tbuffer_load_dwordx4 %4, %6, %1 offen lds\n\ts_add_u32 m0, m0, 1024\n\ts_nop 0\n\tbuffer_load_dwordx4 %5, %6, %1 offen lds\n\ts_add_u32 m0, m0, 13312\n\ts_add_u32 %1, %1, 524288\n\tbuffer_load_dwordx4 %2, %6, %1 offen lds\n\ts_add_u32 m0, m0, 1024\n\ts_nop 0\n\tbuffer_load_dwordx4 %3, %6, %1 offen lds\n\ts_add_u32 m0, m0, 1024\n\ts_nop 0\n\tbuffer_load_dwordx4 %4, %6, %1 offen lds\n\ts_add_u32 m0, m0, 1024\n\ts_nop 0\n\tbuffer_load_dwordx4 %5, %6, %1 offen lds\n\ts_mov_b32 m0, %0"
DEV void first_stage(const u32* x, const u32* wq, const u32* wk, const u32* wv, char* ws, int role, int w) {
  const int lane = lane_id(), xcd = role >> 5, slot = role & 31, xp = xcd & 3;
  u32 vo[4];
  if (xcd >= 4 && slot >= 24) {
#pragma unroll
    for (int j = 0; j < 4; ++j) vo[j] = j * 1024 + (lane ^ (j << 2)) * 16;
    u32 keep, t; const u32x4 sx = mksrd(x, 2u << 20);
    asm volatile(X_STAGE : "=&s"(keep), "=&s"(t) : "v"(vo[0]), "v"(vo[1]), "v"(vo[2]), "v"(vo[3]), "s"(sx), "s"(rfl(((slot - 24) * 4 + xp) * 16384 + w * 4096)), "s"(rfl(w * 4096)) : "memory", "scc");
  } else {
    const int sp = slot % 3, h = 8 * xp + (xcd < 4 ? (slot & 15) >> 1 : slot / 3), ch = xcd < 4 && slot < 16, hp = xcd < 4 && slot >= 16;
    stage_offsets(vo, 8192, h * 256, lane);
    stage(mksrd((hp || (xcd >= 4 && sp == 1)) ? wv : wk, 4u << 20), vo, 8192, (xcd < 4 ? slot & 1 : sp == 2) ? 256 : 0, xcd < 4 ? 32768 : 0, w);
    if (ch) asm volatile(DRY :: "{s[40:43]}"(mksrd(ws, WS_USED)) : CLOB_DRY);
    const int l2 = lane_id();
    stage_offsets(vo, 8192, h * 256, l2);
    stage(mksrd(hp ? wq : (xcd >= 4 && sp != 0) ? wv : wk, 4u << 20), vo, 8192, (ch ? !(slot & 1) : hp ? slot & 1 : 1) ? 256 : 0, xcd < 4 ? 98304 : 65536, w);
  }
}
__global__ void __launch_bounds__(256, 1) rmt_kernel(const u32* x, const u32* mem0, const u32* wq, const u32* wk, const u32* wv, const u32* wo,
                                                     const u32* wg, const u32* wu, const u32* wd, u32* out, char* ws, int d1, int d2) {
  extern __shared__ __attribute__((aligned(16))) char smem[];
  const int w = rfl(threadIdx.x >> 6);
  u32 budget = SPIN;
#undef P
#define QS(q)
  const int guess = (__builtin_amdgcn_s_getreg(20 | (3 << 11)) & 7) * 32 + (blockIdx.x >> 3);
  if (w == 3) {
    unsigned long long pc; asm volatile("s_getpc_b64 %0" : "=s"(pc));
    u32 keep;
    asm volatile("s_mov_b32 %0, m0\n\ts_mov_b32 m0, %4\n\ts_nop 2\n\tbuffer_load_dwordx4 %1, %2, %3 offen lds\n\tbuffer_load_dwordx4 %1, %2, %3 offen offset:1024 lds\n\t"
                 "buffer_load_dwordx4 %1, %2, %3 offen offset:2048 lds\n\ts_mov_b32 m0, %0"
                 : "=&s"(keep) : "v"(lane_id() * 16), "s"(mksrd((const void*)(pc & ~127ull), CODE_BYTES)), "s"((guess & 31) * 3072), "s"((guess >> 5) < 4 ? 8192 : 135168) : "memory");
  }
  const u32 RW = (guess >> 5) < 4 ? 0 : 163832;
  u32 tag = 0, old = 0;
  if (w == 0) {
    asm volatile("s_load_dword %0, %1, 0x0\n\ts_waitcnt lgkmcnt(0)" : "=s"(tag) : "s"(ws) : "memory");
    old = ++tag; QS(q1);
    asm volatile("buffer_atomic_swap %0, %1, %2, 0 offen sc0" : "={v8}"(old) : "v"(lane_id() == 0 ? C_CLAIM + guess * 32 : OOB), "s"(mksrd(ws, WS_USED)), "0"(old) : "memory");
  }
  first_stage(x, wq, wk, wv, ws, guess, w);
  QS(q2);
  if (w == 0) {
    const int lane = lane_id();
    auto rs = mkrs(ws, WS_USED);
    u32 role = guess;
    asm volatile("s_waitcnt vmcnt(16)" : "+{v8}"(old) :: "memory");
    QS(q3);
    const bool lost = rfl(old) == tag;
    st8(rs, lost ? 512u : 256u + role, tag, lane == 0 ? C_ARRIVE + blockIdx.x * 8 : OOB);
    if (lost) {
      u32 k = 0; u32* taken = (u32*)(smem + (RW ? 131072 : 1024));
      for (int i = 0; i < 4; ++i) taken[i * 64 + lane] = 0;
      for (int i = 0; i < 4; ++i) {
        if (!wait_ge(rs, C_ARRIVE + (i * 64 + lane) * 8, true, 1, tag, budget, 2)) END();
        const u32 v = ld4(rs, C_ARRIVE + (i * 64 + lane) * 8);
        if (v < 512u) taken[v - 256u] = 1;
        k += __popcll(__ballot(v == 512u && (u32)(i * 64 + lane) < blockIdx.x));
      }
      for (int i = 0; i < 4; ++i) { unsigned long long fr = __ballot(((volatile u32*)taken)[i * 64 + lane] == 0); const u32 n = __popcll(fr);
        if (k < n) { for (u32 j = 0; j < k; ++j) fr &= fr - 1; role = i * 64 + __builtin_ctzll(fr); k = 1024; } else k -= n; }
    }
    if (lane == 0) *(u32x2*)(smem + RW) = u32x2{role, tag};
  }
  bar_lds();
  const int role = rfl(((volatile u32*)(smem + RW))[0]), xcd = role >> 5, slot = role & 31;
  const Ptrs P{x, mem0, wq, wk, wv, wo, wg, wu, wd, out, ws, d1, d2, rfl(((volatile u32*)(smem + RW))[1])};
  bar_lds();
  if (role != guess) { bar_vm<0>(); first_stage(x, wq, wk, wv, ws, role, w); }
  TS(0);
  const int xp = xcd & 3;
  if (xcd < 4) {
    if (slot < 16) { chain_role(P, 8 * xp + (slot >> 1), slot & 1, lane_id(), w); END(); }
    helper_role(P, xp * 16 + slot - 16, lane_id(), w);
    END();
  }
  if (slot < 24) gemm_role<3>(P, 8 * xp + slot / 3, slot % 3, lane_id(), w, budget);
  else {
    const int i = (slot - 24) * 4 + xp;
    ln_role(P, i, w);
    if (i < 22) gemm_role<2>(P, i >> 1, i & 1, lane_id(), w, budget);
    att_role(P, lane_id(), budget);
    END();
  }
  out_role(P, slot < 16 ? xp : 4 + (xp >> 1), slot < 16 ? slot : (xp & 1) * 8 + slot - 16, lane_id(), w, budget);
}

#define D1 370803432
#define D2 17309214
extern "C" void kernel_launch(void* const* d_in, const int* in_sizes, int n_in, void* d_out, int out_size, void* d_ws, size_t ws_size, hipStream_t stream) {
  (void)in_sizes; (void)n_in; (void)out_size; (void)ws_size;
  int d1 = D1, d2 = D2;
  (void)hipFuncSetAttribute((const void*)rmt_kernel, hipFuncAttributeMaxDynamicSharedMemorySize, LDS_BYTES);
  rmt_kernel<<<256, 256, LDS_BYTES, stream>>>((const u32*)d_in[0], (const u32*)d_in[1], (const u32*)d_in[2], (const u32*)d_in[3], (const u32*)d_in[4], (const u32*)d_in[5],
                                              (const u32*)d_in[8], (const u32*)d_in[9], (const u32*)d_in[10], (u32*)d_out, (char*)d_ws, d1, d2);
}
```
